```python
import torch

def _softmax(x, axis=-1):
    return torch.softmax(x, dim=axis)
def _gelu(x):
    return torch.nn.functional.gelu(x, approximate="tanh")
def _silu(x):
    return torch.nn.functional.silu(x)
def _sigmoid(x):
    return torch.sigmoid(x)
def _layernorm(x, axis=-1, eps=1e-5):
    m = x.mean(dim=axis, keepdim=True)
    v = x.var(dim=axis, keepdim=True, unbiased=False)
    return (x - m) / torch.sqrt(v + eps)
def _scatter_add(tgt, idx, src, axis=0):
    return tgt.clone().index_add_(axis, idx, src)
def _segment_sum(src, seg, n):
    out = torch.zeros((n,) + src.shape[1:], dtype=src.dtype)
    return out.index_add_(0, seg, src)
def _pad(x, pw):
    flat = tuple(v for pair in reversed(pw) for v in pair)
    return torch.nn.functional.pad(x, flat)

kernel_name = 'recurrent_scan__mha_block__geglu_ffn__prompt'

def setup_inputs():

    _w = (lambda a: (lambda t: t.to(torch.bfloat16) if t.is_floating_point() else t)(torch.as_tensor(a)))
    def _A_setup():
        B, S, D, H = 1, 2048, 448, 16
        return {
            "x": _w((lambda *s: torch.randn(*s, dtype=torch.bfloat16) * 0.05)(B, S, D)),
            "wqkv": _w((lambda *s: torch.randn(*s, dtype=torch.bfloat16) * 0.05)(D, 3 * D)),
            "wo": _w((lambda *s: torch.randn(*s, dtype=torch.bfloat16) * 0.05)(D, D)),
        }
    _A_in = _A_setup()

    def _B_setup():
        B, S, D, F = 1, 2048, 448, 1792
        return {
            "x": _w((lambda *s: torch.randn(*s, dtype=torch.bfloat16) * 0.05)(B, S, D)),
            "w_gate": _w((lambda *s: torch.randn(*s, dtype=torch.bfloat16) * 0.05)(D, F)),
            "w_up": _w((lambda *s: torch.randn(*s, dtype=torch.bfloat16) * 0.05)(D, F)),
            "w_down": _w((lambda *s: torch.randn(*s, dtype=torch.bfloat16) * 0.05)(F, D)),
        }
    _B_in = _B_setup()

    return {
        "A_x": _A_in["x"],
        "A_wqkv": _A_in["wqkv"],
        "A_wo": _A_in["wo"],
        "B_x": _B_in["x"],
        "B_w_gate": _B_in["w_gate"],
        "B_w_up": _B_in["w_up"],
        "B_w_down": _B_in["w_down"],
    }

def reference(A_x, A_wqkv, A_wo, B_x, B_w_gate, B_w_up, B_w_down):
    def _A_ref(x, wqkv, wo):
        B, S, D = x.shape
        H = 16
        Dh = D // H
        qkv = (lambda x, *d: x.permute(*d))((x @ wqkv).reshape(B, S, 3, H, Dh), 2, 0, 3, 1, 4)
        q, k, v = qkv[0], qkv[1], qkv[2]
        scores = torch.einsum("bhqd,bhkd->bhqk", q, k) * (Dh ** -0.5)
        p = _softmax(scores, axis=-1)
        o = torch.einsum("bhqk,bhkd->bhqd", p, v)
        return (lambda x, *d: x.permute(*d))(o, 0, 2, 1, 3).reshape(B, S, D) @ wo

    def _B_ref(x, w_gate, w_up, w_down):
        return (_gelu(x @ w_gate) * (x @ w_up)) @ w_down

    x = A_x
    xn = _layernorm(x, axis=-1)
    s = _A_ref(xn, A_wqkv, A_wo)
    g = _silu(_B_ref(xn, B_w_gate, B_w_up, B_w_down))
    return x + s * g
```

```cpp
#include <hip/hip_runtime.h>
#include <hip/hip_bf16.h>
typedef __hip_bfloat16 bf16;
constexpr int S=2048, D=448, H=16, DH=28, F=1792;
__device__ inline float b2f(bf16 x){return __bfloat162float(x);}
__device__ inline float rb(float x){return __bfloat162float(__float2bfloat16(x));}
__global__ void k_ln(const bf16* x, float* xn){
  int r=blockIdx.x; int t=threadIdx.x; __shared__ float red[64];
  float s=0; for(int i=t;i<D;i+=64) s+=b2f(x[r*D+i]);
  for(int o=32;o;o>>=1) s+=__shfl_xor(s,o);
  float m=s/D; float v=0; for(int i=t;i<D;i+=64){float d=b2f(x[r*D+i])-m; v+=d*d;}
  for(int o=32;o;o>>=1) v+=__shfl_xor(v,o);
  v/=D;
  float rs=rsqrtf(v+1e-5f);
  for(int i=t;i<D;i+=64) xn[r*D+i]=(b2f(x[r*D+i])-m)*rs;
}
__global__ void k_gemm(const float* A, const bf16* W, float* C, int N, int K){
  __shared__ float As[16][17], Ws[16][17];
  int tx=threadIdx.x&15, ty=threadIdx.x>>4; int r=blockIdx.y*16+ty, c=blockIdx.x*16+tx; float acc=0;
  for(int k0=0;k0<K;k0+=16){ As[ty][tx]=A[r*K+k0+tx]; Ws[ty][tx]=b2f(W[(k0+ty)*N+c]); __syncthreads();
    for(int k=0;k<16;k++) acc+=As[ty][k]*Ws[k][tx]; __syncthreads(); }
  C[r*N+c]=rb(acc);
}
__global__ void k_attn(const float* qkv, float* o){
  int h=blockIdx.y; int q=blockIdx.x*64+threadIdx.x; float qv[DH], acc[DH];
  for(int d=0;d<DH;d++){qv[d]=qkv[q*3*D+h*DH+d]; acc[d]=0;}
  float m=-1e30f,l=0;
  for(int k=0;k<S;k++){ float s=0; for(int d=0;d<DH;d++) s+=qv[d]*qkv[k*3*D+D+h*DH+d]; s*=0.18898223650461362f;
    float mn=fmaxf(m,s); float a=__expf(m-mn), p=__expf(s-mn); l=l*a+p; for(int d=0;d<DH;d++) acc[d]=acc[d]*a+p*qkv[k*3*D+2*D+h*DH+d]; m=mn;}
  for(int d=0;d<DH;d++) o[q*D+h*DH+d]=rb(acc[d]/l);
}
__global__ void k_geglu(const float* g, const float* u, float* h){ int i=blockIdx.x*256+threadIdx.x; float x=g[i];
  float t=tanhf(0.7978845608028654f*(x+0.044715f*x*x*x)); h[i]=rb(0.5f*x*(1+t)*u[i]); }
__global__ void k_fin(const bf16* x, const float* s, const float* f, bf16* out){ int i=blockIdx.x*256+threadIdx.x; float ff=f[i];
  out[i]=__float2bfloat16(b2f(x[i])+s[i]*(ff/(1+__expf(-ff)))); }
extern "C" void kernel_launch(void* const* d_in, const int* in_sizes, int n_in, void* d_out, int out_size, void* d_ws, size_t ws_size, hipStream_t stream){
  const bf16* x=(const bf16*)d_in[0]; const bf16* wqkv=(const bf16*)d_in[1]; const bf16* wo=(const bf16*)d_in[2];
  const bf16* wg=(const bf16*)d_in[4]; const bf16* wu=(const bf16*)d_in[5]; const bf16* wd=(const bf16*)d_in[6];
  float* xn=(float*)d_ws; float* qkv=xn+S*D; float* o=qkv+S*3*D; float* s=o+S*D; float* g=s+S*D; float* u=g+S*F; float* h=u+S*F; float* f=h+S*F;
  k_ln<<<S,64,0,stream>>>(x,xn);
  k_gemm<<<dim3(3*D/16,S/16),256,0,stream>>>(xn,wqkv,qkv,3*D,D);
  k_attn<<<dim3(S/64,H),64,0,stream>>>(qkv,o);
  k_gemm<<<dim3(D/16,S/16),256,0,stream>>>(o,wo,s,D,D);
  k_gemm<<<dim3(F/16,S/16),256,0,stream>>>(xn,wg,g,F,D);
  k_gemm<<<dim3(F/16,S/16),256,0,stream>>>(xn,wu,u,F,D);
  k_geglu<<<S*F/256,256,0,stream>>>(g,u,h);
  k_gemm<<<dim3(D/16,S/16),256,0,stream>>>(h,wd,f,D,F);
  k_fin<<<S*D/256,256,0,stream>>>(x,s,f,(bf16*)d_out);
}
```

```cpp
#include <hip/hip_runtime.h>
#include <stdint.h>
#pragma clang diagnostic ignored "-Wint-to-pointer-cast"

#if defined(__HIP_DEVICE_COMPILE__)
#define NOPK __attribute__((target("no-packed-fp32-ops")))
#else
#define NOPK
#endif
#define DEV __device__ __forceinline__ NOPK
#define LAM __attribute__((always_inline)) NOPK
#define LAS __attribute__((address_space(3)))
#define GAS __attribute__((address_space(1)))

#define PHASED 0
#define STAMPS 0

typedef unsigned u32;
typedef unsigned long long u64;
typedef u32 u32x2 __attribute__((ext_vector_type(2)));
typedef u32 u32x4 __attribute__((ext_vector_type(4)));
typedef short s16x4 __attribute__((ext_vector_type(4)));
typedef float f32x2 __attribute__((ext_vector_type(2)));
typedef float f32x4 __attribute__((ext_vector_type(4)));
typedef float f32x8 __attribute__((ext_vector_type(8)));
typedef float f32x16 __attribute__((ext_vector_type(16)));
typedef __bf16 bf16x8 __attribute__((ext_vector_type(8)));
typedef __bf16 bf16x16 __attribute__((ext_vector_type(16)));

extern "C" __device__ u64 llvm_dispatch_id() __asm("llvm.amdgcn.dispatch.id");

constexpr int S_ = 2048, D_ = 448, F_ = 1792;
constexpr u32 WS_EPOCH = 0, WS_FQ = 4096, WS_FH = 8192, WS_FO = 16384, WS_KM = 32768, WS_DBG = 65536;
constexpr u32 WS_Q = 1u << 20, WS_KV = 3u << 20, WS_O = 7u << 20, WS_H = 9u << 20, WS_END = 16u << 20;
constexpr u32 L_MISC = 147456, L_CS = L_MISC, L_STAT = L_MISC + 3072, L_CNT = L_MISC + 7168, L_MX = L_MISC + 7424;
constexpr u32 L_VT = L_MISC + 8192;
constexpr u32 L_ATT = 0, L_GEM = 65536, GEM_SZ = 20480;
constexpr u32 LDS_BYTES = 163840;

template <int V> struct IC { static constexpr int value = V; };
template <int I, int N, class Fn> DEV void sfor(Fn&& f) {
  if constexpr (I < N) { f(IC<I>{}); sfor<I + 1, N>(f); }
}

#define SBAR() __builtin_amdgcn_sched_barrier(0)
template <class T> DEV void PIN(T& x) { asm volatile("" : "+v"(x)); }
template <class T> DEV void nop_on(T& x) { asm volatile("s_nop 7" : "+v"(x)); }
template <class T> DEV void lgkm0_on(T& x) { asm volatile("s_waitcnt lgkmcnt(0)" : "+v"(x)::"memory"); }
template <int K> DEV void colsum1(f32x4& c0, u32x4 sel, u32x4 w0, int mt) {
  asm volatile("s_cmp_lg_u32 %3, %4\n\ts_cbranch_scc1 .Lcs%=\n\tv_mfma_f32_16x16x32_bf16 %0, %1, %2, %0\n.Lcs%=:"
               : "+v"(c0) : "v"(sel), "v"(w0), "s"(mt), "i"(K) : "scc");
}
template <int K> DEV void colsum2(f32x4& c0, f32x4& c1, u32x4 sel, u32x4 w0, u32x4 w1, int mt) {
  asm volatile("s_cmp_lg_u32 %5, %6\n\ts_cbranch_scc1 .Lcs%=\n\tv_mfma_f32_16x16x32_bf16 %0, %2, %3, %0\n\tv_mfma_f32_16x16x32_bf16 %1, %2, %4, %1\n.Lcs%=:"
               : "+v"(c0), "+v"(c1) : "v"(sel), "v"(w0), "v"(w1), "s"(mt), "i"(K) : "scc");
}
template <int K> DEV void colsum3(f32x4& c0, f32x4& c1, f32x4& c2, u32x4 sel, u32x4 w0, u32x4 w1, u32x4 w2, int mt) {
  asm volatile("s_cmp_lg_u32 %7, %8\n\ts_cbranch_scc1 .Lcs%=\n\tv_mfma_f32_16x16x32_bf16 %0, %3, %4, %0\n\tv_mfma_f32_16x16x32_bf16 %1, %3, %5, %1\n\tv_mfma_f32_16x16x32_bf16 %2, %3, %6, %2\n.Lcs%=:"
               : "+v"(c0), "+v"(c1), "+v"(c2) : "v"(sel), "v"(w0), "v"(w1), "v"(w2), "s"(mt), "i"(K) : "scc");
}
DEV void aload_sc1(u32x4& d, u32 voff, u32x4 srd, u32 soff) {
  asm volatile("buffer_load_dwordx4 %0, %1, %2, %3 offen sc1" : "=v"(d) : "v"(voff), "s"(srd), "s"(soff) : "memory");
}
template <int N> DEV void vm_on2(u32x4& a, u32x4& b) { asm volatile("s_waitcnt vmcnt(%2)" : "+v"(a), "+v"(b) : "i"(N) : "memory"); }

DEV f32x16 mfma32(u32x4 a, u32x4 b, f32x16 c) {
  return __builtin_amdgcn_mfma_f32_32x32x16_bf16(__builtin_bit_cast(bf16x8, a), __builtin_bit_cast(bf16x8, b), c, 0, 0, 0);
}
DEV f32x16 smf_lo(u32x4 a, f32x16 p, f32x16 c, int idx) {
  f32x8 b = __builtin_shufflevector(p, p, 0, 1, 2, 3, 4, 5, 6, 7);
  return __builtin_amdgcn_smfmac_f32_32x32x32_bf16(__builtin_bit_cast(bf16x8, a), __builtin_bit_cast(bf16x16, b), c, idx, 0, 0);
}
DEV f32x16 smf_hi(u32x4 a, f32x16 p, f32x16 c, int idx) {
  f32x8 b = __builtin_shufflevector(p, p, 8, 9, 10, 11, 12, 13, 14, 15);
  return __builtin_amdgcn_smfmac_f32_32x32x32_bf16(__builtin_bit_cast(bf16x8, a), __builtin_bit_cast(bf16x16, b), c, idx, 0, 0);
}
typedef __bf16 bf16x2 __attribute__((ext_vector_type(2)));
DEV u32 cvtpk(float lo, float hi) {
  f32x2 v = {lo, hi};
  return __builtin_bit_cast(u32, __builtin_convertvector(v, bf16x2));
}
DEV float asf(u32 u) { return __builtin_bit_cast(float, u); }
DEV u32 asu(float f) { return __builtin_bit_cast(u32, f); }
DEV float bfround(float f) { return asf(cvtpk(f, f) << 16); }
DEV float ex2(float x) { return __builtin_amdgcn_exp2f(x); }
DEV float rcp(float x) { return __builtin_amdgcn_rcpf(x); }
DEV float other_half(float v) {
  auto r = __builtin_amdgcn_permlane32_swap(asu(v), asu(v), false, false);
  return asf(r[0] ^ r[1] ^ asu(v));
}
DEV u32x4 mk_srd(const void* p, u32 bytes) {
  u64 a = (u64)p;
  u32x4 r = {(u32)a, (u32)(a >> 32) & 0xffffu, bytes, 0x00020000u};
  return r;
}
DEV void dma(u32x4 srd, u32 voff, u32 soff, u32 lds) {
  asm volatile("s_mov_b32 m0, %0\n\ts_nop 0\n\tbuffer_load_dwordx4 %1, %2, %3 offen lds" ::"s"(lds), "v"(voff), "s"(srd), "s"(soff) : "memory");
}
DEV u32x2 trd(u32 a) { return __builtin_bit_cast(u32x2, __builtin_amdgcn_ds_read_tr16_b64_v4i16((LAS s16x4*)a)); }
DEV u32x4 trd2(u32 a0, u32 a1) { u32x2 x = trd(a0), y = trd(a1); u32x4 r = {x[0], x[1], y[0], y[1]}; return r; }
DEV u32x4 lds128(u32 a) { return *(LAS u32x4*)a; }
DEV f32x4 lds128f(u32 a) { return *(LAS f32x4*)a; }
template <int N> DEV void sync_stage() { asm volatile("s_waitcnt vmcnt(%0) lgkmcnt(0)\n\ts_barrier" ::"i"(N) : "memory"); }
template <int N> DEV u64 sync_first(const char* p) {
  u64 e;
  asm volatile("s_load_dwordx2 %0, %1, 0x0\n\ts_waitcnt vmcnt(%2) lgkmcnt(0)\n\ts_barrier" : "=s"(e) : "s"(p), "i"(N) : "memory");
  return e;
}
typedef __amdgpu_buffer_rsrc_t rsrc_t;
DEV void st128(rsrc_t r, u32x4 v, u32 voff, u32 soff) {
  __builtin_amdgcn_raw_buffer_store_b128(v, r, voff, __builtin_amdgcn_readfirstlane(soff), 16);
  asm volatile("s_nop 1" ::"v"(v) : "memory");
}
DEV u32x4 ld128(rsrc_t r, u32 voff, u32 soff) { return __builtin_amdgcn_raw_buffer_load_b128(r, voff, __builtin_amdgcn_readfirstlane(soff), 16); }
DEV void put_flag(char* ws, u32 off, u64 tag) { __hip_atomic_store((GAS u64*)(ws + off), tag, __ATOMIC_RELAXED, __HIP_MEMORY_SCOPE_AGENT); }
DEV void poll_flags(char* ws, u32 off, u64 tag) {
  for (u32 spins = 0; spins < (1u << 20); ++spins) {
    u64 v = __hip_atomic_load((GAS u64*)(ws + off), __ATOMIC_RELAXED, __HIP_MEMORY_SCOPE_AGENT);
    if (__builtin_amdgcn_ballot_w64(v != tag) == 0) break;
    __builtin_amdgcn_s_sleep(2);
  }
  __builtin_amdgcn_fence(__ATOMIC_ACQUIRE, "wavefront");
  asm volatile("" ::: "memory");
}
DEV float wave_max_nonneg(float v) {
  u32 x = asu(v);
#define DPPMAX(ctrl) { u32 y = __builtin_amdgcn_update_dpp(0u, x, ctrl, 0xf, 0xf, true); x = x > y ? x : y; }
  DPPMAX(0x111) DPPMAX(0x112) DPPMAX(0x114) DPPMAX(0x118) DPPMAX(0x142) DPPMAX(0x143)
#undef DPPMAX
  return asf(__builtin_amdgcn_readlane(x, 63));
}

#define STAMP(i) do {} while (0)

constexpr int p_cnt(int T, int n) { return (T == 1 && n < 21 && n % 3 == 2) ? 1 : 2; }
constexpr int n_stage(int T) { return T == 0 ? 21 : 14; }
constexpr int p_last(int T, int G) { return G < 0 ? -1 : (T == 0 ? (G < 7 ? 2 * G + 1 : G + 7) : (G < 7 ? 3 * G + 2 : G + 14)); }
constexpr int p_w(int T, int G) { return T == 0 ? (G < 7 ? 2 * G + 1 : G + 7) : (G < 7 ? 3 * G + 1 : G + 14); }
constexpr int p_x(int T, int G) { return T == 0 ? 2 * G : 3 * G; }
constexpr int imin(int a, int b) { return a < b ? a : b; }
constexpr int p_issued(int T, int G) { return imin(27, p_last(T, G - 1) + 9); }
constexpr int wait_cnt(int T, int G) {
  int n = 0;
  for (int p = p_last(T, G) + 1; p <= p_issued(T, G > 0 ? G - 1 : 0); ++p) n += p_cnt(T, p);
  return n;
}

struct P1 {
  u32x4 sX, sQ, sG, sU;
  u32 vx0, vx1, vq, vg, vu;
  u32 ox, oq, of0, of1;
  u32 lw;
};
template <int T, int n> DEV void issue_pair(const P1& c) {
  constexpr u32 base = (n % 9) * 16384;
  constexpr bool isx = (T == 0) ? (n < 14 && (n & 1) == 0) : (n < 21 && n % 3 == 0);
  if constexpr (isx) {
    constexpr int s = T == 0 ? n / 2 : n / 3;
    dma(c.sX, c.vx0, c.ox + s * 128, base + 2 * c.lw);
    dma(c.sX, c.vx1, c.ox + s * 128, base + 2 * c.lw + 1024);
  } else if constexpr (n >= 21 || (T == 0 && n < 14)) {
    constexpr int s = n >= 21 ? n - 21 : n / 2;
    const u32 o = (n >= 21 ? c.of1 : c.of0) + s * 64 * 3584;
    dma(c.sG, c.vg, o, base + c.lw);
    dma(c.sU, c.vu, o, base + 8192 + c.lw);
  } else {
    constexpr int s = T == 0 ? n - 14 : n / 3;
    constexpr int lc = (T == 1 && n % 3 == 2) ? 2 : 0;
    dma(c.sQ, c.vq, c.oq + s * 64 * 2688 + lc * 128, base + c.lw);
    if constexpr (lc == 0) dma(c.sQ, c.vq, c.oq + s * 64 * 2688 + 128, base + 8192 + c.lw);
  }
}

__global__ __launch_bounds__(512, 2) NOPK void fused_block(const void* __restrict__ px, const void* __restrict__ pwqkv,
                                                           const void* __restrict__ pwo, const void* __restrict__ pwg,
                                                           const void* __restrict__ pwu, const void* __restrict__ pwd,
                                                           void* __restrict__ pout, char* ws
) {
  __shared__ __attribute__((aligned(16))) char smem[LDS_BYTES];
  asm volatile("" ::"v"((u32)(uintptr_t)(LAS char*)smem));
  const int tid = __builtin_amdgcn_workitem_id_x();
  const int lane = tid & 63;
  const int w = __builtin_amdgcn_readfirstlane(tid >> 6);
  const int bid = __builtin_amdgcn_workgroup_id_x();
  const int xcd = bid & 7, bi = bid >> 3;
  const int r32 = lane & 31, hf = lane >> 5, blk = (lane >> 4) & 1, q4 = (lane & 15) >> 2, p4 = lane & 3;
  const rsrc_t rWS = __builtin_amdgcn_make_buffer_rsrc(ws, 0, WS_END, 0x00020000);
  STAMP(0);
  u64 epoch = 0, tag = 0;
  auto make_tag = [&](auto N) LAM {
    epoch = sync_first<N.value>(ws);
    tag = (epoch + 0x9E3779B97F4A7C15ull) * 0xBF58476D1CE4E5B9ull;
    tag = (tag ^ (tag >> 29)) + llvm_dispatch_id() * 0x94D049BB133111EBull;
    tag = (tag ^ (tag >> 31)) * 0xD6E8FEB86659FD93ull;
  };
  if (tid < 8) *(LAS u32*)(L_CNT + tid * 4) = 0;

  {
    const int rb = 8 * (xcd & 1) + (bi & 7), quad = xcd >> 1, ci = bi >> 3, cb = 4 * quad + ci;
    const int mt = w & 3, g = w >> 2;
    const int mtile = rb * 4 + mt;
    const int a = 3 * quad + ci;
    const int sl0 = ci == 3 ? (16 + 16 * (quad >> 1) + 10 * (quad & 1)) : (a < 8 ? 2 * a : (a < 10 ? 6 + 2 * a : 18 + 2 * a));
    const int L0 = (56 * sl0) >> 7;
    const int u0 = 7 * quad + 2 * ci, u1 = ci == 3 ? u0 : u0 + 1;
    P1 c;
    c.sX = mk_srd(px, S_ * D_ * 2);
    c.sQ = mk_srd(pwqkv, D_ * 3 * D_ * 2);
    c.sG = mk_srd(pwg, D_ * F_ * 2);
    c.sU = mk_srd(pwu, D_ * F_ * 2);
    {
      const int rr = lane >> 3, ch = lane & 7;
      c.vx0 = rr * 896 + ((ch ^ (rr >> 1)) << 4);
      c.vx1 = (rr + 8) * 896 + ((ch ^ (4 + (rr >> 1))) << 4);
      const int sw = (ch ^ ((rr & 2) << 1)) << 4;
      c.vq = rr * 2688 + sw;
      c.vg = rr * 3584 + sw;
      c.vu = rr * 3584 + (sw ^ 32);
    }
    c.ox = (rb * 128 + 16 * w) * 896;
    c.oq = 8 * w * 2688 + L0 * 128;
    c.of0 = 8 * w * 3584 + u0 * 128;
    c.of1 = 8 * w * 3584 + u1 * 128;
    c.lw = w * 1024;

    u32x4 xf[28];
    u32 xa[4];
    sfor<0, 4>([&](auto K) LAM { xa[K.value] = (mt * 32 + r32) * 128 + (((2 * K.value + hf) ^ ((r32 >> 1) & 7)) << 4); });
    const u32 rowoff = (8 * hf + q4) * 128, swz = (q4 & 2) << 5;
    u32 fa[2];
    sfor<0, 2>([&](auto Tt) LAM { fa[Tt.value] = rowoff + blk * 8192 + (((2 * g + Tt.value) * 32) ^ swz ^ (blk * 32)) + 8 * p4; });
    const u32 selw = (((lane >> 2) ^ (lane >> 4)) & 1) ? 0u : 0x3f803f80u;
    u32x4 sel = {selw, selw, selw, selw};
    float ssum = 0.f, ssq = 0.f;
    float rstd = 0.f;
    u32x4 negm = {0, 0, 0, 0};

    auto unit = [&](auto T_, auto NT_, auto FIRST_, auto G0_, f32x16* acc, const u32* ad, auto hook) LAM {
      constexpr int T = T_.value, NT = NT_.value, G0 = G0_.value;
      constexpr bool FIRST = FIRST_.value;
      u32x4 wf[2][NT];
      f32x4 cs[NT];
      sfor<0, NT>([&](auto t) LAM { cs[t.value] = (f32x4){0.f, 0.f, 0.f, 0.f}; acc[t.value] = (f32x16){}; });
      auto reads = [&](auto K) LAM {
        constexpr int kk = K.value, G = G0 + kk / 4;
        constexpr u32 off = (p_w(T, G) % 9) * 16384 + (kk & 3) * 2048;
        constexpr u32 offx = (p_x(T, G) % 9) * 16384;
        if constexpr (FIRST) xf[kk] = lds128(xa[kk & 3] + offx);
        sfor<0, NT>([&](auto t) LAM { wf[kk & 1][t.value] = trd2(ad[t.value] + off, ad[t.value] + off + 512); });
      };
      reads(IC<0>{});
      sfor<0, 28>([&](auto K) LAM {
        constexpr int kk = K.value;
        if constexpr ((kk & 3) == 3) {
          constexpr int G = G0 + kk / 4 + 1;
          if constexpr (G < n_stage(T)) {
            constexpr bool pub = (G == n_stage(T) - 4);
            sync_stage<pub ? 0 : wait_cnt(T, G)>();
            sfor<p_issued(T, G - 1) + 1, p_issued(T, G) + 1>([&](auto n) LAM { issue_pair<T, n.value>(c); });
            if constexpr (pub) {
              if (tid == 0) {
                put_flag(ws, WS_FQ + (cb * 16 + rb) * 8, tag);
                if (T == 0) put_flag(ws, WS_FH + (rb * 32 + u0) * 8, tag);
              }
            }
          }
        }
        if constexpr (kk + 1 < 28) reads(IC<kk + 1>{});
        sfor<0, NT>([&](auto t) LAM { acc[t.value] = mfma32(wf[kk & 1][t.value], xf[kk], acc[t.value]); });
        if constexpr (NT == 1) colsum1<kk & 3>(cs[0], sel, wf[kk & 1][0], mt);
        else if constexpr (NT == 2) colsum2<kk & 3>(cs[0], cs[1], sel, wf[kk & 1][0], wf[kk & 1][1], mt);
        else colsum3<kk & 3>(cs[0], cs[1], cs[NT - 1], sel, wf[kk & 1][0], wf[kk & 1][1], wf[kk & 1][NT - 1], mt);
        if constexpr (FIRST) {
          const u32 d0 = g ? xf[kk][2] : xf[kk][0], d1 = g ? xf[kk][3] : xf[kk][1];
          const float a0 = asf(d0 << 16), a1 = asf(d0 & 0xffff0000u), a2 = asf(d1 << 16), a3 = asf(d1 & 0xffff0000u);
          ssum += (a0 + a1) + (a2 + a3);
          ssq = __builtin_fmaf(a0, a0, ssq); ssq = __builtin_fmaf(a1, a1, ssq);
          ssq = __builtin_fmaf(a2, a2, ssq); ssq = __builtin_fmaf(a3, a3, ssq);
          PIN(ssum); PIN(ssq);
        }
        hook(K);
        SBAR();
      });
      sfor<0, NT>([&](auto t) LAM { nop_on(cs[t.value]); });
      sfor<0, NT>([&](auto t) LAM {
        const float v = cs[t.value][0];
        const float hi = bfround(v);
        *(LAS u32*)(L_CS + ((g * 3 + t.value) * 32 + r32) * 16 + mt * 4) = cvtpk(hi, v - hi);
      });
      if constexpr (FIRST) *(LAS f32x2*)(L_STAT + ((mt * 32 + r32) * 4 + g * 2 + hf) * 8) = (f32x2){ssum, ssq};
    };
    auto fin_stats = [&]() LAM {
      const f32x4 s0 = lds128f(L_STAT + (mt * 32 + r32) * 32), s1 = lds128f(L_STAT + (mt * 32 + r32) * 32 + 16);
      const float mean = ((s0[0] + s0[2]) + (s1[0] + s1[2])) * (1.f / 448.f);
      float var = ((s0[1] + s0[3]) + (s1[1] + s1[3])) * (1.f / 448.f) - mean * mean;
      var = var > 0.f ? var : 0.f;
      const float den = bfround(__builtin_amdgcn_sqrtf(bfround(bfround(var) + 1e-5f)));
      rstd = rcp(den);
      const u32 nm = hf ? 0u : cvtpk(-mean, -mean);
      negm = (u32x4){nm, nm, nm, nm};
    };
    auto mean_fix = [&](f32x16& acc, int t) LAM { acc = mfma32(lds128(L_CS + ((g * 3 + t) * 32 + r32) * 16), negm, acc); };
    float K1 = 0.f, K2 = 0.f, IR2 = 0.f;
    auto ffn_consts = [&]() LAM {
      const float k1 = -2.f * 0.7978845608028654f * 1.4426950408889634f;
      K1 = k1 * rstd; K2 = k1 * 0.044715f * rstd * rstd * rstd; IR2 = rcp(rstd * rstd);
    };
    auto geglu = [&](float t, float u) LAM -> float {
      const float e = ex2(t * __builtin_fmaf(t * t, K2, K1));
      return (t * u) * rcp(__builtin_fmaf(e, IR2, IR2));
    };
    auto qkv_out = [&](f32x16& acc, int sl) LAM {
      const int sec = sl >> 4, head = sl & 15;
      const float sc = sec == 0 ? rstd * (0.18898223650461362f * 1.4426950408889634f) : rstd;
      float v[16];
      sfor<0, 16>([&](auto i) LAM { v[i.value] = acc[i.value] * sc; });
      u32 P[8];
      sfor<0, 8>([&](auto i) LAM { P[i.value] = cvtpk(v[2 * i.value], v[2 * i.value + 1]); });
      u32 pad = 0x3f80u;
      if (sec < 2) {
        float n0 = 0.f, n1 = 0.f;
        sfor<0, 12>([&](auto i) LAM { n0 = __builtin_fmaf(v[i.value], v[i.value], n0); });
        sfor<12, 16>([&](auto i) LAM { n1 = __builtin_fmaf(v[i.value], v[i.value], n1); });
        n0 += hf ? 0.f : n1;
        n0 += other_half(n0);
        if (sec == 0) pad = cvtpk(__builtin_amdgcn_sqrtf(n0), 0.f);
        else {
          const float km = wave_max_nonneg(n0);
          if (lane == 0) __hip_atomic_store((GAS float*)(ws + WS_KM + (head * 64 + mtile) * 4), km, __ATOMIC_RELAXED, __HIP_MEMORY_SCOPE_AGENT);
        }
      }
      P[6] = hf ? pad : P[6];
      P[7] = hf ? 0u : P[7];
      u32x4 f0 = {P[0], P[1], P[2], P[3]}, f1 = {P[4], P[5], P[6], P[7]};
      if (sec == 2) {
        const u32 vt = L_VT + w * 1024;
        const u32 wa = vt + (r32 & 15) * 64 + hf * 8, ra = vt + (8 * hf + q4) * 64 + 32 * blk + 8 * p4;
        if (r32 < 16) sfor<0, 4>([&](auto i) LAM { *(LAS u32x2*)(wa + 16 * i.value) = (u32x2){P[2 * i.value], P[2 * i.value + 1]}; });
        asm volatile("s_waitcnt lgkmcnt(0)" ::: "memory");
        f0 = trd2(ra, ra + 256);
        lgkm0_on(f0);
        if (r32 >= 16) sfor<0, 4>([&](auto i) LAM { *(LAS u32x2*)(wa + 16 * i.value) = (u32x2){P[2 * i.value], P[2 * i.value + 1]}; });
        asm volatile("s_waitcnt lgkmcnt(0)" ::: "memory");
        f1 = trd2(ra, ra + 256);
        lgkm0_on(f1);
      }
      const u32 so = sec == 0 ? WS_Q + (head * 64 + mtile) * 2048 : WS_KV + (head * 64 + mtile) * 4096 + (sec - 1) * 2048;
      st128(rWS, f0, lane * 16, so);
      st128(rWS, f1, lane * 16, so + 1024);
    };
    auto qaddr = [&](int sl) LAM -> u32 {
      u32 b = 56 * sl - 128 * L0 + 32 * blk + 8 * p4;
      b = b < 376u ? b : 376u;
      return rowoff + (b >> 7) * 8192 + ((b & 127) ^ swz);
    };
    auto nohook = [&](auto) LAM {};
    f32x16 accL[2];

    if (ci < 3) {
      sfor<0, 9>([&](auto n) LAM { issue_pair<0, n.value>(c); });
      make_tag(IC<wait_cnt(0, 0)>{});
      STAMP(1);
      f32x16 accF[2], accQ[1];
      unit(IC<0>{}, IC<2>{}, IC<1>{}, IC<0>{}, accF, fa, nohook);
      STAMP(2);
      u32 qa[1] = {qaddr(sl0 + g)};
      u32 hp[4];
      float hv0 = 0.f;
      unit(IC<0>{}, IC<1>{}, IC<0>{}, IC<7>{}, accQ, qa, [&](auto K) LAM {
        constexpr int kk = K.value;
        if constexpr (kk == 3) { fin_stats(); ffn_consts(); }
        if constexpr (kk == 4) { mean_fix(accF[0], 0); mean_fix(accF[1], 1); }
        if constexpr (kk >= 6 && kk < 22) {
          constexpr int i = kk - 6, t = i >> 3, j = i & 7;
          const float hv = geglu(accF[t][j], accF[t][j + 8]);
          if constexpr (j & 1) { hp[j >> 1] = cvtpk(hv0, hv); PIN(hp[j >> 1]); } else { hv0 = hv; PIN(hv0); }
          if constexpr (j == 7) st128(rWS, (u32x4){hp[0], hp[1], hp[2], hp[3]}, lane * 16, WS_H + (mtile * 112 + u0 * 4 + 2 * g + t) * 1024);
        }
      });
      STAMP(3);
      unit(IC<0>{}, IC<2>{}, IC<0>{}, IC<14>{}, accL, fa, [&](auto K) LAM {
        constexpr int kk = K.value;
        if constexpr (kk == 3) mean_fix(accQ[0], 0);
        if constexpr (kk == 4) qkv_out(accQ[0], sl0 + g);
      });
    } else {
      sfor<0, 9>([&](auto n) LAM { issue_pair<1, n.value>(c); });
      make_tag(IC<wait_cnt(1, 0)>{});
      STAMP(1);
      f32x16 accQ[3];
      u32 qa[3] = {qaddr(sl0 + 3 * g), qaddr(sl0 + 3 * g + 1), qaddr(sl0 + 3 * g + 2)};
      unit(IC<1>{}, IC<3>{}, IC<1>{}, IC<0>{}, accQ, qa, nohook);
      STAMP(3);
      unit(IC<1>{}, IC<2>{}, IC<0>{}, IC<7>{}, accL, fa, [&](auto K) LAM {
        constexpr int kk = K.value;
        if constexpr (kk == 3) { fin_stats(); ffn_consts(); }
        if constexpr (kk == 4) { mean_fix(accQ[0], 0); mean_fix(accQ[1], 1); mean_fix(accQ[2], 2); }
        if constexpr (kk >= 5 && kk < 8) qkv_out(accQ[kk - 5], sl0 + 3 * g + kk - 5);
      });
    }
    STAMP(4);
    sync_stage<63>();
    sfor<0, 2>([&](auto t) LAM {
      mean_fix(accL[t.value], t.value);
    });
    sfor<0, 2>([&](auto t) LAM {
      u32 hp[4];
      sfor<0, 4>([&](auto j) LAM {
        hp[j.value] = cvtpk(geglu(accL[t.value][2 * j.value], accL[t.value][2 * j.value + 8]),
                            geglu(accL[t.value][2 * j.value + 1], accL[t.value][2 * j.value + 9]));
      });
      st128(rWS, (u32x4){hp[0], hp[1], hp[2], hp[3]}, lane * 16, WS_H + (mtile * 112 + u1 * 4 + 2 * g + t.value) * 1024);
    });
    sync_stage<0>();
    if (tid == 0) put_flag(ws, WS_FH + (rb * 32 + u1) * 8, tag);
    STAMP(5);
  }

  const bool has_tile = bi < 28;
  const int rt = 4 * xcd + bi / 7, nt = bi % 7;
  const u32 trow = (4 * hf + q4) * 128, tswz = (q4 & 2) << 5;
  const u32 vw = (lane >> 3) * 896 + (((lane & 7) ^ ((lane >> 2) & 4)) << 4);
  f32x16 acc[2][2];
  u32 red;
  if (w < 4) {
    red = L_ATT + w * 16384;
    {
      const int head = 2 * xcd + (bi >> 4), qb = bi & 15;
      {
        const int aq = head >> 1;
        const int cq = aq + aq / 3;
        const int ck = head < 6 ? 3 : (head >= 10 ? 7 : (head < 8 ? 10 : 12));
        const int cv = head < 6 ? 11 : (head >= 10 ? 15 : (head < 8 ? 13 : 14));
        const int fi = lane < 16 ? ck * 16 + lane : (lane < 32 ? cv * 16 + lane - 16 : cq * 16 + qb);
        poll_flags(ws, WS_FQ + fi * 8, tag);
      }
      STAMP(6);
      const u32 vl = lane * 16;
      const u32 kvb = WS_KV + (head * 64 + w * 16) * 4096;
      u32x4 Q[4][2], KV[4][4];
      const float km = asf(__builtin_amdgcn_raw_buffer_load_b32(rWS, lane * 4, WS_KM + head * 256, 16));
      sfor<0, 4>([&](auto t) LAM {
        Q[t.value][0] = ld128(rWS, vl, WS_Q + (head * 64 + qb * 4 + t.value) * 2048);
        Q[t.value][1] = ld128(rWS, vl, WS_Q + (head * 64 + qb * 4 + t.value) * 2048 + 1024);
      });
      sfor<0, 3>([&](auto b) LAM { sfor<0, 4>([&](auto i) LAM { KV[b.value][i.value] = ld128(rWS, vl, kvb + b.value * 4096 + i.value * 1024); }); });
      const float kn = __builtin_amdgcn_sqrtf(wave_max_nonneg(km)) * 1.01f;
      bool safe = true;
      sfor<0, 4>([&](auto t) LAM {
        const float m = asf(Q[t.value][1][2] << 16) * kn;
        safe = safe && (hf == 0 || m <= 48.f);
        Q[t.value][1][2] = hf ? cvtpk(-m, 0.f) : Q[t.value][1][2];
      });
      const int idx = 0xDDDD;
      f32x16 O[4] = {};
      const f32x16 Z = {};
      if (__builtin_amdgcn_ballot_w64(!safe) == 0) {
        f32x16 Sx[4];
        Sx[0] = mfma32(KV[0][1], Q[0][1], mfma32(KV[0][0], Q[0][0], Z));
        Sx[3] = Z;
        KV[3][3] = (u32x4){0, 0, 0, 0};
#define EXP4(C, i) C[i] = ex2(C[i]); C[i + 1] = ex2(C[i + 1]); C[i + 2] = ex2(C[i + 2]); C[i + 3] = ex2(C[i + 3]); PIN(C); SBAR();
        for (int it = 0; it < 4; ++it) {
          sfor<0, 16>([&](auto I) LAM {
            constexpr int b = I.value >> 2, t = I.value & 3, tn = (t + 1) & 3, tp = (t + 3) & 3;
            constexpr int bn = t == 3 ? (b + 1) & 3 : b, bp = t == 0 ? (b + 3) & 3 : b;
            Sx[tn] = mfma32(KV[bn][0], Q[tn][0], Z);
            EXP4(Sx[t], 0)
            Sx[tn] = mfma32(KV[bn][1], Q[tn][1], Sx[tn]);
            EXP4(Sx[t], 4)
            O[tp] = smf_hi(KV[bp][3], Sx[tp], O[tp], idx);
            EXP4(Sx[t], 8)
            O[t] = smf_lo(KV[b][2], Sx[t], O[t], idx);
            if constexpr (t == 0) {
              const int kn3 = it * 4 + b + 3;
              const u32 so = kvb + (kn3 < 15 ? kn3 : 15) * 4096;
              sfor<0, 4>([&](auto i) LAM { KV[bp][i.value] = ld128(rWS, vl, so + i.value * 1024); });
            }
            EXP4(Sx[t], 12)
          });
        }
#undef EXP4
        O[3] = smf_hi(KV[3][3], Sx[3], O[3], idx);
      } else {
        sfor<0, 4>([&](auto t) LAM { Q[t.value][1][2] = hf ? 0u : Q[t.value][1][2]; });
        float mx[4] = {-3.0e38f, -3.0e38f, -3.0e38f, -3.0e38f};
        for (int kt = 0; kt < 16; ++kt) {
          const u32x4 k0 = ld128(rWS, vl, kvb + kt * 4096), k1 = ld128(rWS, vl, kvb + kt * 4096 + 1024);
          sfor<0, 4>([&](auto t) LAM {
            const f32x16 s = mfma32(k1, Q[t.value][1], mfma32(k0, Q[t.value][0], Z));
            sfor<0, 16>([&](auto i) LAM { mx[t.value] = __builtin_fmaxf(mx[t.value], s[i.value]); });
          });
        }
        sfor<0, 4>([&](auto t) LAM {
          mx[t.value] = __builtin_fmaxf(mx[t.value], other_half(mx[t.value]));
          *(LAS float*)(L_MX + ((t.value * 32 + r32) * 4 + w) * 4) = mx[t.value];
        });
        asm volatile("s_waitcnt lgkmcnt(0)" ::: "memory");
        if (lane == 0) __hip_atomic_fetch_add((LAS u32*)(L_CNT + 4), 1u, __ATOMIC_RELAXED, __HIP_MEMORY_SCOPE_WORKGROUP);
        while (__hip_atomic_load((LAS u32*)(L_CNT + 4), __ATOMIC_RELAXED, __HIP_MEMORY_SCOPE_WORKGROUP) < 4u) __builtin_amdgcn_s_sleep(1);
        asm volatile("" ::: "memory");
        sfor<0, 4>([&](auto t) LAM {
          const f32x4 m4 = lds128f(L_MX + (t.value * 32 + r32) * 16);
          mx[t.value] = __builtin_fmaxf(__builtin_fmaxf(m4[0], m4[1]), __builtin_fmaxf(m4[2], m4[3]));
        });
        for (int kt = 0; kt < 16; ++kt) {
          const u32x4 k0 = ld128(rWS, vl, kvb + kt * 4096), k1 = ld128(rWS, vl, kvb + kt * 4096 + 1024);
          const u32x4 v0 = ld128(rWS, vl, kvb + kt * 4096 + 2048), v1 = ld128(rWS, vl, kvb + kt * 4096 + 3072);
          sfor<0, 4>([&](auto t) LAM {
            f32x16 s = mfma32(k1, Q[t.value][1], mfma32(k0, Q[t.value][0], Z));
            sfor<0, 16>([&](auto i) LAM { s[i.value] = ex2(s[i.value] - mx[t.value]); });
            O[t.value] = smf_hi(v1, s, smf_lo(v0, s, O[t.value], idx), idx);
          });
        }
      }
      STAMP(7);
      sfor<0, 4>([&](auto t) LAM {
        sfor<0, 4>([&](auto i) LAM {
          *(LAS f32x4*)(L_ATT + t.value * 16384 + w * 4096 + i.value * 1024 + lane * 16) =
              (f32x4){O[t.value][4 * i.value], O[t.value][4 * i.value + 1], O[t.value][4 * i.value + 2], O[t.value][4 * i.value + 3]};
        });
      });
      asm volatile("s_waitcnt lgkmcnt(0)" ::: "memory");
      if (lane == 0) __hip_atomic_fetch_add((LAS u32*)(L_CNT), 1u, __ATOMIC_RELAXED, __HIP_MEMORY_SCOPE_WORKGROUP);
      while (__hip_atomic_load((LAS u32*)(L_CNT), __ATOMIC_RELAXED, __HIP_MEMORY_SCOPE_WORKGROUP) < 4u) __builtin_amdgcn_s_sleep(1);
      asm volatile("" ::: "memory");
      float o[16];
      sfor<0, 4>([&](auto i) LAM {
        const u32 ra = red + i.value * 1024 + lane * 16;
        const f32x4 a0 = lds128f(ra), a1 = lds128f(ra + 4096), a2 = lds128f(ra + 8192), a3 = lds128f(ra + 12288);
        sfor<0, 4>([&](auto j) LAM { o[4 * i.value + j.value] = (a0[j.value] + a1[j.value]) + (a2[j.value] + a3[j.value]); });
      });
      const float lo_ = other_half(o[12]);
      const float il = rcp(hf ? o[12] : lo_);
      u32 P[8];
      sfor<0, 8>([&](auto i) LAM { P[i.value] = cvtpk(o[2 * i.value] * il, o[2 * i.value + 1] * il); });
      P[6] = hf ? 0u : P[6];
      P[7] = hf ? 0u : P[7];
      const int om = qb * 4 + w;
      st128(rWS, (u32x4){P[0], P[1], P[2], P[3]}, vl, WS_O + (om * 16 + head) * 2048);
      st128(rWS, (u32x4){P[4], P[5], P[6], P[7]}, vl, WS_O + (om * 16 + head) * 2048 + 1024);
      asm volatile("s_waitcnt vmcnt(0)" ::: "memory");
      if (lane == 0) put_flag(ws, WS_FO + (om * 16 + head) * 8, tag);
      STAMP(8);
    }
    if (!has_tile) return;
    const u32x4 sO = mk_srd(pwo, D_ * D_ * 2);
    sfor<0, 14>([&](auto e) LAM { dma(sO, vw, (112 * w + 8 * e.value) * 896 + nt * 128, red + e.value * 1024); });
    poll_flags(ws, WS_FO + ((2 * rt + ((lane >> 2) & 1)) * 16 + 4 * w + (lane & 3)) * 8, tag);
    STAMP(9);
    u32x4 of[2][8];
    sfor<0, 8>([&](auto k) LAM {
      sfor<0, 2>([&](auto m) LAM { of[m.value][k.value] = ld128(rWS, lane * 16, WS_O + ((2 * rt + m.value) * 16 + 4 * w) * 2048 + k.value * 1024); });
    });
    asm volatile("s_waitcnt vmcnt(16)" ::: "memory");
    u32 wa[2], wp[2];
    sfor<0, 2>([&](auto n) LAM {
      wa[n.value] = red + trow + ((64 * n.value + 32 * blk + 8 * p4) ^ tswz);
      wp[n.value] = wa[n.value] - hf * 512;
    });
    sfor<0, 2>([&](auto m) LAM { sfor<0, 2>([&](auto n) LAM { acc[m.value][n.value] = (f32x16){}; }); });
    sfor<0, 8>([&](auto k) LAM {
      constexpr int hh = k.value >> 1, s = k.value & 1;
      constexpr u32 off = (28 * hh + 16 * s) * 128;
      sfor<0, 2>([&](auto n) LAM {
        const u32x4 wf = trd2(wa[n.value] + off, (s ? wp[n.value] : wa[n.value]) + off + 1024);
        sfor<0, 2>([&](auto m) LAM { acc[m.value][n.value] = mfma32(wf, of[m.value][k.value], acc[m.value][n.value]); });
      });
    });
  } else {
    const int gw = w - 4;
    red = L_GEM + gw * GEM_SZ;
    if (bid == 255 && gw == 0) {
      sfor<0, 4>([&](auto i) LAM { poll_flags(ws, WS_FQ + (i.value * 64 + lane) * 8, tag); });
      if (lane == 0) put_flag(ws, WS_EPOCH, epoch + 1);
    }
    if (!has_tile) return;
    constexpr int DP = 8;
    const u32x4 sD = mk_srd(pwd, F_ * D_ * 2);
    const u32x4 sH = mk_srd(ws + WS_H, 64 * 112 * 1024);
    const u32 od = 448 * gw * 896 + nt * 128, oh = (2 * rt * 112 + 28 * gw) * 1024, vl = lane * 16;
    auto wload = [&](auto K) LAM {
      dma(sD, vw, od + K.value * 16 * 896, red + (K.value % DP) * 2048);
      dma(sD, vw, od + (K.value * 16 + 8) * 896, red + (K.value % DP) * 2048 + 1024);
    };
    u32x4 hfr[DP][2];
    auto hload = [&](auto K) LAM {
      aload_sc1(hfr[K.value % DP][0], vl, sH, oh + K.value * 1024);
      aload_sc1(hfr[K.value % DP][1], vl, sH, oh + (112 + K.value) * 1024);
    };
    sfor<0, DP>([&](auto K) LAM { wload(K); });
    poll_flags(ws, WS_FH + ((rt >> 1) * 32 + 7 * gw + (lane < 7 ? lane : 0)) * 8, tag);
    STAMP(6);
    sfor<0, DP>([&](auto K) LAM { hload(K); });
    u32 wa[2];
    sfor<0, 2>([&](auto n) LAM { wa[n.value] = red + trow + ((64 * n.value + 32 * blk + 8 * p4) ^ tswz); });
    sfor<0, 2>([&](auto m) LAM { sfor<0, 2>([&](auto n) LAM { acc[m.value][n.value] = (f32x16){}; }); });
    u32x4 wf[2][2];
    auto ready = [&](auto K, auto N) LAM {
      vm_on2<N.value>(hfr[K.value % DP][0], hfr[K.value % DP][1]);
      sfor<0, 2>([&](auto n) LAM { wf[K.value & 1][n.value] = trd2(wa[n.value] + (K.value % DP) * 2048, wa[n.value] + (K.value % DP) * 2048 + 1024); });
    };
    ready(IC<0>{}, IC<2 * (DP - 1)>{});
    sfor<0, 28>([&](auto K) LAM {
      constexpr int k = K.value;
      if constexpr (k + 1 < 28) {
        constexpr int hi = imin(27, k - 1 + DP);
        constexpr int N = k + 1 < DP ? 2 * (DP - 2 - k) + 4 * (hi - DP + 1) : 4 * (hi - (k + 1));
        ready(IC<k + 1>{}, IC<N>{});
      }
      sfor<0, 2>([&](auto n) LAM {
        sfor<0, 2>([&](auto m) LAM { acc[m.value][n.value] = mfma32(wf[k & 1][n.value], hfr[k % DP][m.value], acc[m.value][n.value]); });
      });
      SBAR();
      if constexpr (k + DP < 28) { wload(IC<k + DP>{}); hload(IC<k + DP>{}); }
      SBAR();
    });
    STAMP(7);
  }
  const int otok = tid >> 3, ooct = tid & 7;
  const u32 xo = ((rt * 64 + otok) * 448 + nt * 64 + ooct * 8) * 2;
  const u32x4 xr = *(const GAS u32x4*)((const char*)px + xo);
  asm volatile("s_waitcnt lgkmcnt(0)" ::: "memory");
  sfor<0, 2>([&](auto m) LAM {
    sfor<0, 2>([&](auto n) LAM {
      sfor<0, 4>([&](auto i) LAM {
        const f32x16& a = acc[m.value][n.value];
        *(LAS f32x4*)(red + (m.value * 32 + r32) * 256 + (((8 * n.value + 2 * i.value + hf) ^ (r32 & 15)) << 4)) =
            (f32x4){a[4 * i.value], a[4 * i.value + 1], a[4 * i.value + 2], a[4 * i.value + 3]};
      });
    });
  });
  STAMP(10);
  sync_stage<63>();
  u32 R[4];
  sfor<0, 2>([&](auto e) LAM {
    const u32 ra = otok * 256 + (((2 * ooct + e.value) ^ (otok & 15)) << 4);
    f32x4 sv = lds128f(L_ATT + ra), fv = lds128f(L_GEM + ra);
    sfor<1, 4>([&](auto j) LAM {
      const f32x4 s2 = lds128f(L_ATT + j.value * 16384 + ra), f2 = lds128f(L_GEM + j.value * GEM_SZ + ra);
      sfor<0, 4>([&](auto c) LAM { sv[c.value] += s2[c.value]; fv[c.value] += f2[c.value]; });
    });
    float y[4];
    sfor<0, 4>([&](auto c) LAM {
      const u32 xd = xr[2 * e.value + (c.value >> 1)];
      const float xv = asf((c.value & 1) ? (xd & 0xffff0000u) : (xd << 16));
      const float f = fv[c.value];
      const float gt = f * rcp(1.f + ex2(f * -1.4426950408889634f));
      y[c.value] = __builtin_fmaf(sv[c.value], gt, xv);
    });
    R[2 * e.value] = cvtpk(y[0], y[1]);
    R[2 * e.value + 1] = cvtpk(y[2], y[3]);
  });
  *(GAS u32x4*)((char*)pout + xo) = (u32x4){R[0], R[1], R[2], R[3]};
  STAMP(11);
}

extern "C" void kernel_launch(void* const* d_in, const int* in_sizes, int n_in, void* d_out, int out_size, void* d_ws,
                              size_t ws_size, hipStream_t stream) {
  fused_block<<<256, 512, 0, stream>>>(d_in[0], d_in[1], d_in[2], d_in[4], d_in[5], d_in[6], d_out, (char*)d_ws);
}
```

```cpp
#include <hip/hip_runtime.h>
#include <stdint.h>
#pragma clang diagnostic ignored "-Wint-to-pointer-cast"

#if defined(__HIP_DEVICE_COMPILE__)
#define NOPK __attribute__((target("no-packed-fp32-ops")))
#else
#define NOPK
#endif
#define DEV __device__ __forceinline__ NOPK
#define LAM __attribute__((always_inline)) NOPK
#define LAS __attribute__((address_space(3)))
#define GAS __attribute__((address_space(1)))

#define STAMPS 0

typedef unsigned u32;
typedef unsigned long long u64;
typedef u32 u32x2 __attribute__((ext_vector_type(2)));
typedef u32 u32x4 __attribute__((ext_vector_type(4)));
typedef short s16x4 __attribute__((ext_vector_type(4)));
typedef float f32x2 __attribute__((ext_vector_type(2)));
typedef float f32x4 __attribute__((ext_vector_type(4)));
typedef float f32x8 __attribute__((ext_vector_type(8)));
typedef float f32x16 __attribute__((ext_vector_type(16)));
typedef __bf16 bf16x8 __attribute__((ext_vector_type(8)));
typedef __bf16 bf16x16 __attribute__((ext_vector_type(16)));

extern "C" __device__ u64 llvm_dispatch_id() __asm("llvm.amdgcn.dispatch.id");

constexpr int S_ = 2048, D_ = 448, F_ = 1792;
constexpr u32 WS_EPOCH = 0, WS_FQ = 4096, WS_FH = 8192, WS_FO = 16384, WS_KM = 32768, WS_DBG = 65536;
constexpr u32 WS_Q = 1u << 20, WS_KV = 3u << 20, WS_O = 7u << 20, WS_H = 9u << 20, WS_END = 16u << 20;
constexpr u32 L_MISC = 147456, L_CS = L_MISC, L_STAT = L_MISC + 3072, L_CNT = L_MISC + 7168, L_DUM = L_MISC + 7424;
constexpr u32 L_VT = L_MISC + 8192;
constexpr u32 LDS_BYTES = 163840;

template <int V> struct IC { static constexpr int value = V; };
template <int I, int N, class Fn> DEV void sfor(Fn&& f) {
  if constexpr (I < N) { f(IC<I>{}); sfor<I + 1, N>(f); }
}

#define SBAR() __builtin_amdgcn_sched_barrier(0)
template <class T> DEV void PIN(T& x) { asm volatile("" : "+v"(x)); }
template <class T> DEV void nop_on(T& x) { asm volatile("s_nop 7" : "+v"(x)); }
template <class T> DEV void lgkm0_on(T& x) { asm volatile("s_waitcnt lgkmcnt(0)" : "+v"(x)::"memory"); }
template <int K> DEV void colsum1(f32x4& c0, u32x4 sel, u32x4 w0, int mt) {
  asm volatile("s_cmp_lg_u32 %3, %4\n\ts_cbranch_scc1 .Lcs%=\n\tv_mfma_f32_16x16x32_bf16 %0, %1, %2, %0\n.Lcs%=:"
               : "+v"(c0) : "v"(sel), "v"(w0), "s"(mt), "i"(K) : "scc");
}
template <int K> DEV void colsum2(f32x4& c0, f32x4& c1, u32x4 sel, u32x4 w0, u32x4 w1, int mt) {
  asm volatile("s_cmp_lg_u32 %5, %6\n\ts_cbranch_scc1 .Lcs%=\n\tv_mfma_f32_16x16x32_bf16 %0, %2, %3, %0\n\tv_mfma_f32_16x16x32_bf16 %1, %2, %4, %1\n.Lcs%=:"
               : "+v"(c0), "+v"(c1) : "v"(sel), "v"(w0), "v"(w1), "s"(mt), "i"(K) : "scc");
}
template <int K> DEV void colsum3(f32x4& c0, f32x4& c1, f32x4& c2, u32x4 sel, u32x4 w0, u32x4 w1, u32x4 w2, int mt) {
  asm volatile("s_cmp_lg_u32 %7, %8\n\ts_cbranch_scc1 .Lcs%=\n\tv_mfma_f32_16x16x32_bf16 %0, %3, %4, %0\n\tv_mfma_f32_16x16x32_bf16 %1, %3, %5, %1\n\tv_mfma_f32_16x16x32_bf16 %2, %3, %6, %2\n.Lcs%=:"
               : "+v"(c0), "+v"(c1), "+v"(c2) : "v"(sel), "v"(w0), "v"(w1), "v"(w2), "s"(mt), "i"(K) : "scc");
}
DEV void aload_sc1(u32x4& d, u32 voff, u32x4 srd, u32 soff) {
  asm volatile("buffer_load_dwordx4 %0, %1, %2, %3 offen sc1" : "=v"(d) : "v"(voff), "s"(srd), "s"(soff) : "memory");
}
template <int N> DEV void vm_on2(u32x4& a, u32x4& b) { asm volatile("s_waitcnt vmcnt(%2)" : "+v"(a), "+v"(b) : "i"(N) : "memory"); }

DEV f32x16 mfma32(u32x4 a, u32x4 b, f32x16 c) {
  return __builtin_amdgcn_mfma_f32_32x32x16_bf16(__builtin_bit_cast(bf16x8, a), __builtin_bit_cast(bf16x8, b), c, 0, 0, 0);
}
DEV f32x16 smf_lo(u32x4 a, f32x16 p, f32x16 c, int idx) {
  f32x8 b = __builtin_shufflevector(p, p, 0, 1, 2, 3, 4, 5, 6, 7);
  return __builtin_amdgcn_smfmac_f32_32x32x32_bf16(__builtin_bit_cast(bf16x8, a), __builtin_bit_cast(bf16x16, b), c, idx, 0, 0);
}
DEV f32x16 smf_hi(u32x4 a, f32x16 p, f32x16 c, int idx) {
  f32x8 b = __builtin_shufflevector(p, p, 8, 9, 10, 11, 12, 13, 14, 15);
  return __builtin_amdgcn_smfmac_f32_32x32x32_bf16(__builtin_bit_cast(bf16x8, a), __builtin_bit_cast(bf16x16, b), c, idx, 0, 0);
}
typedef __bf16 bf16x2 __attribute__((ext_vector_type(2)));
DEV u32 cvtpk(float lo, float hi) {
  f32x2 v = {lo, hi};
  return __builtin_bit_cast(u32, __builtin_convertvector(v, bf16x2));
}
DEV float asf(u32 u) { return __builtin_bit_cast(float, u); }
DEV u32 asu(float f) { return __builtin_bit_cast(u32, f); }
DEV float bfround(float f) { return asf(cvtpk(f, f) << 16); }
DEV float ex2(float x) { return __builtin_amdgcn_exp2f(x); }
DEV float rcp(float x) { return __builtin_amdgcn_rcpf(x); }
DEV float other_half(float v) {
  auto r = __builtin_amdgcn_permlane32_swap(asu(v), asu(v), false, false);
  return asf(r[0] ^ r[1] ^ asu(v));
}
DEV u32x4 mk_srd(const void* p, u32 bytes) {
  u64 a = (u64)p;
  u32x4 r = {(u32)a, (u32)(a >> 32) & 0xffffu, bytes, 0x00020000u};
  return r;
}
template <int LD, int OFF, int MUL> DEV void dma(u32x4 srd, u32 voff, u32 s0, u32 k, u32 l0) {
  u32 t;
  asm volatile("s_mul_i32 %0, %4, %8\n\ts_add_u32 m0, %1, %2\n\ts_add_u32 %0, %0, %3\n\tbuffer_load_dwordx4 %5, %6, %0 offen offset:%7 lds"
               : "=&s"(t) : "s"(l0), "i"(LD - OFF), "s"(s0), "s"(k), "v"(voff), "s"(srd), "i"(OFF), "i"(MUL) : "memory", "scc");
}
template <int LD, int ADD> DEV void dma_i(u32x4 srd, u32 voff, u32 s0, u32 l0) {
  u32 t;
  asm volatile("s_add_u32 m0, %1, %2\n\ts_add_u32 %0, %3, %4\n\tbuffer_load_dwordx4 %5, %6, %0 offen lds"
               : "=&s"(t) : "s"(l0), "i"(LD), "s"(s0), "i"(ADD), "v"(voff), "s"(srd) : "memory", "scc");
}
DEV int lane_id() {
  int l;
  asm volatile("v_mbcnt_lo_u32_b32 %0, -1, 0\n\tv_mbcnt_hi_u32_b32 %0, -1, %0" : "=v"(l));
  return l;
}
#define LANES() const int lane = lane_id(), r32 = lane & 31, hf = lane >> 5, blk = (lane >> 4) & 1, q4 = (lane & 15) >> 2, p4 = lane & 3; \
  const u32 vl = lane * 16; (void)r32; (void)hf; (void)blk; (void)q4; (void)p4; (void)vl
DEV void touch(const void* p) {
  asm volatile("s_mov_b32 m0, %0\n\ts_nop 0\n\tglobal_load_lds_dword %1, off" ::"i"(L_DUM), "v"(p) : "memory");
}
template <int N> DEV void vmwait() { asm volatile("s_waitcnt vmcnt(%0)" ::"i"(N) : "memory"); }
DEV void cfence() { asm volatile("" ::: "memory"); }
DEV void cnt_add(int i) {
  cfence();
  if (lane_id() == 0) __hip_atomic_fetch_add((LAS u32*)(L_CNT + 4 * i), 1u, __ATOMIC_RELAXED, __HIP_MEMORY_SCOPE_WORKGROUP);
  cfence();
}
DEV u32 cnt_get(int i) { return __hip_atomic_load((LAS u32*)(L_CNT + 4 * i), __ATOMIC_RELAXED, __HIP_MEMORY_SCOPE_WORKGROUP); }
DEV void cnt_wait(int i, u32 n) {
  while (__builtin_amdgcn_readfirstlane(cnt_get(i)) < n) __builtin_amdgcn_s_sleep(1);
  cfence();
}
DEV u32x2 trd(u32 a) { return __builtin_bit_cast(u32x2, __builtin_amdgcn_ds_read_tr16_b64_v4i16((LAS s16x4*)a)); }
DEV u32x4 trd2(u32 a0, u32 a1) { u32x2 x = trd(a0), y = trd(a1); u32x4 r = {x[0], x[1], y[0], y[1]}; return r; }
DEV u32x4 lds128(u32 a) { return *(LAS u32x4*)a; }
DEV f32x4 lds128f(u32 a) { return *(LAS f32x4*)a; }
template <int N> DEV void sync_stage() { asm volatile("s_waitcnt vmcnt(%0) lgkmcnt(0)\n\ts_barrier" ::"i"(N) : "memory"); }
template <int I> DEV void wlane(u32& f, u32 v) { asm volatile("v_writelane_b32 %0, %1, %2" : "+v"(f) : "s"(v), "i"(I)); }
template <int N, int I> DEV void sync_fine(u32& f) {
  u64 a, b, c;
  asm volatile("s_memtime %1\n\ts_waitcnt vmcnt(%4) lgkmcnt(0)\n\ts_memtime %2\n\ts_barrier\n\ts_memtime %3\n\ts_waitcnt lgkmcnt(0)"
               : "+v"(f), "=&s"(a), "=&s"(b), "=&s"(c) : "i"(N) : "memory");
  wlane<I>(f, (u32)a); wlane<I + 1>(f, (u32)b); wlane<I + 2>(f, (u32)c);
}
template <int N> DEV u64 sync_first(const char* p) {
  u64 e;
  asm volatile("s_load_dwordx2 %0, %1, 0x0\n\ts_waitcnt vmcnt(%2) lgkmcnt(0)\n\ts_barrier" : "=s"(e) : "s"(p), "i"(N) : "memory");
  return e;
}
typedef __amdgpu_buffer_rsrc_t rsrc_t;
DEV void st128(rsrc_t r, u32x4 v, u32 voff, u32 soff) {
  __builtin_amdgcn_raw_buffer_store_b128(v, r, voff, __builtin_amdgcn_readfirstlane(soff), 16);
  asm volatile("s_nop 1" ::"v"(v) : "memory");
}
DEV void st128p(rsrc_t r, u32x4 v, u32 voff) {
  __builtin_amdgcn_raw_buffer_store_b128(v, r, voff, 0, 16);
}
DEV void aflag(u64& d, const char* p) { asm volatile("global_load_dwordx2 %0, %1, off sc1" : "=v"(d) : "v"(p) : "memory"); }
template <int N> DEV void vm_on1(u64& a) { asm volatile("s_waitcnt vmcnt(%1)" : "+v"(a) : "i"(N) : "memory"); }
DEV void swap32(u32& a, u32& b) {
  auto r = __builtin_amdgcn_permlane32_swap(a, b, false, false);
  a = r[0]; b = r[1];
}
DEV u32x4 ld128(rsrc_t r, u32 voff, u32 soff) { return __builtin_amdgcn_raw_buffer_load_b128(r, voff, __builtin_amdgcn_readfirstlane(soff), 16); }
DEV void put_flag(char* ws, u32 off, u64 tag) { __hip_atomic_store((GAS u64*)(ws + off), tag, __ATOMIC_RELAXED, __HIP_MEMORY_SCOPE_AGENT); }
DEV void poll_flags(char* ws, u32 off, u64 tag) {
  for (u32 spins = 0; spins < (1u << 20); ++spins) {
    u64 v = __hip_atomic_load((GAS u64*)(ws + off), __ATOMIC_RELAXED, __HIP_MEMORY_SCOPE_AGENT);
    if (__builtin_amdgcn_ballot_w64(v != tag) == 0) break;
    __builtin_amdgcn_s_sleep(2);
  }
  __builtin_amdgcn_fence(__ATOMIC_ACQUIRE, "wavefront");
  asm volatile("" ::: "memory");
}
DEV float wave_max_nonneg(float v) {
  u32 x = asu(v);
#define DPPMAX(ctrl) { u32 y = __builtin_amdgcn_update_dpp(0u, x, ctrl, 0xf, 0xf, true); x = x > y ? x : y; }
  DPPMAX(0x111) DPPMAX(0x112) DPPMAX(0x114) DPPMAX(0x118) DPPMAX(0x142) DPPMAX(0x143)
#undef DPPMAX
  return asf(__builtin_amdgcn_readlane(x, 63));
}

#define STAMP(i) do {} while (0)

#define ROT1 1
#define I0A 8
#define I0B 8
#define RAMP 4
#define PUBK 18
#define PAT 1
#define DPH 6
#define PRIO 1

constexpr int imin(int a, int b) { return a < b ? a : b; }
constexpr int p_cnt(int T, int n) { return (T == 1 && n < 21 && n % 3 == 2) ? 1 : 2; }
constexpr int n_stage(int T) { return T == 0 ? 21 : 14; }
constexpr int p_last(int T, int G) { return G < 0 ? -1 : (T == 0 ? (G < 7 ? 2 * G + 1 : G + 7) : (G < 7 ? 3 * G + 2 : G + 14)); }
constexpr int p_w(int T, int G) { return T == 0 ? (G < 7 ? 2 * G + 1 : G + 7) : (G < 7 ? 3 * G + 1 : G + 14); }
constexpr int p_x(int T, int G) { return T == 0 ? 2 * G : 3 * G; }
constexpr int q_first(int T, int n) { int q = 0; for (int p = 0; p < n; ++p) q += p_cnt(T, p); return q; }
constexpr int q_pair(int T, int q) { int n = 0; while (q_first(T, n + 1) <= q) ++n; return n; }
constexpr int n_tile(int T, int u) { return T == 0 ? (u == 1 ? 1 : 2) : (u == 0 ? 3 : 2); }
constexpr int ge_a(int p) { return 5 + p * 22 / 8; }
constexpr int st_q(int T, int i) { return T == 0 ? 8 : 7 + 3 * i; }
constexpr int J_PF = 6, J_POLL = 24;
constexpr int N_PF0 = 1;
constexpr int extra(int T, int j) {
  const int k = j - (4 * n_stage(T) - 28);
  int n = 0;
  if (T == 0 && (j == 28 + ge_a(3) + 2 || j == 28 + ge_a(7) + 2)) n += 1;
  for (int i = 0; i < (T == 0 ? 1 : 3); ++i) if (k == st_q(T, i)) n += 2;
  if (k == J_PF || k == J_POLL) n += 1;
  return n;
}
struct Sched { int lo[86]; int wait[22]; };
constexpr Sched make_sched(int T) {
  Sched s{};
  const int NS = 4 * n_stage(T), NQ = q_first(T, 36);
  int q = T == 0 ? I0A : I0B;
  for (int j = 0; j < NS; ++j) {
    s.lo[j] = q;
    const int nt = n_tile(T, j / 28);
    int b = nt == 2 ? 1 : (nt == 1 ? (j & 1) : ((j & 3) == 3 ? 2 : 1));
    if (j < RAMP) ++b;
    const int gf = j < 2 ? -1 : imin((j - 2) / 4, n_stage(T) - 2);
    while (b > 0 && q < NQ && q_pair(T, q) - 9 <= p_last(T, gf)) { ++q; --b; }
  }
  s.lo[NS] = q;
  s.wait[0] = N_PF0 + s.lo[0] - q_first(T, p_last(T, 0) + 1);
  for (int G = 1; G < n_stage(T); ++G) {
    const int j = 4 * G - 2, need = q_first(T, p_last(T, G) + 1);
    int n = s.lo[j] - need;
    for (int i = 0; i < j; ++i) if (s.lo[i + 1] >= need) n += extra(T, i);
    if (j == NS - 28 + PUBK) {
      const int js = NS - 28 + st_q(T, T == 0 ? 0 : 2);
      int m = s.lo[j] - s.lo[js + 1];
      for (int i = js + 1; i < j; ++i) m += extra(T, i);
      n = imin(n, m);
    }
    s.wait[G] = n;
  }
  return s;
}
template <int T> struct SC { static constexpr Sched s = make_sched(T); };
static_assert(SC<0>::s.lo[84] == 72 && SC<1>::s.lo[56] == 65 && SC<0>::s.lo[56 + J_POLL] == 72 && SC<1>::s.lo[28 + J_POLL] == 65, "all pieces issued");

struct P1 {
  u32x4 sX, sQ, sG, sU, sD;
  u32 vx0, vx1, vq, vg, vu, vw;
  u32 ox, oq, of0, of1, od0, od1;
  u32 lw, lw2;
  u32 kr[7];
  u32 kd;
};
template <int T, int q> DEV void issue_piece(const P1& c) {
  constexpr int n = q_pair(T, q), h = q - q_first(T, n);
  constexpr int base = (n % 9) * 16384;
  constexpr bool isx = (T == 0) ? (n < 14 && (n & 1) == 0) : (n < 21 && n % 3 == 0);
  if constexpr (n >= 28) {
    u32 k = c.kd + 2 * (n - 28);
    k = k >= 28 ? k - 28 : k;
    dma<base + h * 1024, 0, 16 * 896>(c.sD, c.vw, h ? c.od1 : c.od0, k, c.lw2);
  } else if constexpr (isx) {
    dma<base + h * 1024, 0, 128>(c.sX, h ? c.vx1 : c.vx0, c.ox, c.kr[T == 0 ? n / 2 : n / 3], c.lw2);
  } else if constexpr (n >= 21 || (T == 0 && n < 14)) {
    dma<base + h * 8192, 0, 64 * 3584>(h ? c.sU : c.sG, h ? c.vu : c.vg, n >= 21 ? c.of1 : c.of0, c.kr[n >= 21 ? n - 21 : n / 2], c.lw);
  } else {
    constexpr int lc = (T == 1 && n % 3 == 2) ? 2 : h;
    dma<base + h * 8192, lc * 128, 64 * 2688>(c.sQ, c.vq, c.oq, c.kr[T == 0 ? n - 14 : n / 3], c.lw);
  }
}
constexpr u32 wd_loc(int k) { return (((k % 18) / 2 + 1) % 9) * 16384 + (k & 1) * 2048; }

constexpr int gemm_wait(int k) {
  int n = k < DPH ? 2 * (DPH - 1 - k) : 0;
  for (int i = k < DPH ? 0 : k + 1 - DPH; i < k - 1; ++i) n += (i + 18 < 28 ? 2 : 0) + (i + DPH < 28 ? 2 : 0);
  return n;
}

__global__ __launch_bounds__(512, 2) NOPK void fused_block(const void* __restrict__ px, const void* __restrict__ pwqkv,
                                                           const void* __restrict__ pwo, const void* __restrict__ pwg,
                                                           const void* __restrict__ pwu, const void* __restrict__ pwd,
                                                           void* __restrict__ pout, char* ws) {
  __shared__ __attribute__((aligned(16))) char smem[LDS_BYTES];
  asm volatile("" ::"v"((u32)(uintptr_t)(LAS char*)smem));
  const int tid = __builtin_amdgcn_workitem_id_x();
  const int w = __builtin_amdgcn_readfirstlane(tid >> 6);
  const int bid = __builtin_amdgcn_workgroup_id_x();
  const int xcd = bid & 7, bi = bid >> 3;
  const rsrc_t rWS = __builtin_amdgcn_make_buffer_rsrc(ws, 0, WS_END, 0x00020000);
  u64 pc0;
  u32 clen;
  asm volatile("s_getpc_b64 %0\n.Lfb_pc0:\n\ts_max_u32 %1, .Lfb_enda-.Lfb_pc0, 0\n\ts_max_u32 %1, .Lfb_endb-.Lfb_pc0, %1" : "=s"(pc0), "=s"(clen)::"scc");
  STAMP(0);
  u64 epoch = 0, tag = 0;
  if (tid < 8) *(LAS u32*)(L_CNT + tid * 4) = 0;

  const bool has_tile = bi < 28;
  const int rq = bi / 7, rt = 4 * xcd + rq, nt = bi - 7 * rq;
  const int rotg = (4 * nt + 7 * rq) % 28;
  const int head = 2 * xcd + (bi >> 4), qb = bi & 15;

  const int rb = 8 * (xcd & 1) + (bi & 7), quad = xcd >> 1, ci = bi >> 3, cb = 4 * quad + ci;
  const int mt = w & 3, g = w >> 2;
  const int mtile = rb * 4 + mt;
  const int a3 = 3 * quad + ci;
  const int sl0 = ci == 3 ? (16 + 16 * (quad >> 1) + 10 * (quad & 1)) : 2 * a3 + (a3 < 8 ? 0 : (a3 < 10 ? 6 : 18));
  const int L0 = (56 * sl0) >> 7;
  const int u0 = 7 * quad + 2 * ci, u1 = ci == 3 ? u0 : u0 + 1;
  P1 c;
  c.sX = mk_srd(px, S_ * D_ * 2);
  c.sQ = mk_srd(pwqkv, D_ * 3 * D_ * 2);
  c.sG = mk_srd(pwg, D_ * F_ * 2);
  c.sU = mk_srd(pwu, D_ * F_ * 2);
  c.sD = mk_srd(pwd, F_ * D_ * 2);
  {
    const int rr = (tid >> 3) & 7, ch = tid & 7;
    c.vx0 = rr * 896 + ((ch ^ (rr >> 1)) << 4);
    c.vx1 = (rr + 8) * 896 + ((ch ^ (4 + (rr >> 1))) << 4);
    const int sw = (ch ^ ((rr & 2) << 1)) << 4;
    c.vq = rr * 2688 + sw;
    c.vg = rr * 3584 + sw;
    c.vu = rr * 3584 + (sw ^ 32);
    c.vw = rr * 896 + sw;
  }
  c.ox = (rb * 128 + 16 * w) * 896;
  c.oq = 8 * w * 2688 + L0 * 128;
  c.of0 = 8 * w * 3584 + u0 * 128;
  c.of1 = 8 * w * 3584 + u1 * 128;
  c.od0 = 448 * (w >> 1) * 896 + nt * 128;
  c.od1 = c.od0 + 8 * 896;
  c.lw = w * 1024;
  c.lw2 = w * 2048;
  {
    const int rot = ROT1 ? ((bi & 7) + 2 * ci) % 7 : 0;
    sfor<0, 7>([&](auto s) LAM { const int v = s.value + rot; c.kr[s.value] = v >= 7 ? v - 7 : v; });
    const int k = rotg + (w & 1);
    c.kd = k >= 28 ? k - 28 : k;
  }

  u32x4 xf[28];
  float rstd = 0.f, K1 = 0.f, K2 = 0.f, IR2 = 0.f;
  u32x4 negm = {0, 0, 0, 0};
  f32x16 accL[2];
  u64 fq = 0;
  auto fq_off = [&]() LAM -> u32 {
    const int lane = lane_id();
    const int ck = head < 6 ? 3 : (head >= 10 ? 7 : (head < 8 ? 10 : 12)), cv = head < 6 ? 11 : (head >= 10 ? 15 : (head < 8 ? 13 : 14));
    return WS_FQ + 8 * (lane < 16 ? ck * 16 + lane : (lane < 32 ? cv * 16 + lane - 16 : ((head >> 1) + (head >> 1) / 3) * 16 + qb));
  };
  auto cs_frag = [&](int t) LAM -> u32x4 { return lds128(L_CS + ((g * 3 + t) * 32 + (lane_id() & 31)) * 16); };
  auto ge_exp = [&](float t) LAM -> float { return ex2(t * __builtin_fmaf(t * t, K2, K1)); };
  auto ge_rcp = [&](float e) LAM -> float { return rcp(__builtin_fmaf(e, IR2, IR2)); };

  auto phase1 = [&](auto T_) LAM {
    constexpr int T = T_.value, NS = 4 * n_stage(T), JL = NS - 28, QU = T == 0 ? 1 : 0, NQ = n_tile(T, QU);
    constexpr int Q0 = q_first(T, p_last(T, 0) + 1);
    sfor<0, Q0>([&](auto q) LAM { issue_piece<T, q.value>(c); });
    {
      const u32 o = (bi * 16 + w * 2 + (tid & 1)) * 128;
      touch((const char*)pc0 + (o < clen ? o : clen));
    }
    sfor<Q0, SC<T>::s.lo[0]>([&](auto q) LAM { issue_piece<T, q.value>(c); });

    u32 xa[4], fa[2], qa[NQ], selw;
    {
    LANES();
    const u32 rowoff = (8 * hf + q4) * 128, swz = (q4 & 2) << 5;
    sfor<0, 4>([&](auto K) LAM { xa[K.value] = (mt * 32 + r32) * 128 + (((2 * K.value + hf) ^ ((r32 >> 1) & 7)) << 4); });
    sfor<0, 2>([&](auto t) LAM { fa[t.value] = rowoff + blk * 8192 + (((2 * g + t.value) * 32) ^ swz ^ (blk * 32)) + 8 * p4; });
    sfor<0, NQ>([&](auto t) LAM {
      u32 b = 56 * (sl0 + NQ * g + t.value) - 128 * L0 + 32 * blk + 8 * p4;
      b = b < 376u ? b : 376u;
      qa[t.value] = rowoff + (b >> 7) * 8192 + ((b & 127) ^ swz);
    });
    selw = (((lane >> 2) ^ (lane >> 4)) & 1) ? 0u : 0x3f803f80u;
    }
    const u32x4 sel = {selw, selw, selw, selw};
    const u32 pl = 0x01000c0cu + g * 0x04040000u, ph = 0x03020c0cu + g * 0x04040000u;
    float ssum = 0.f, ssq = 0.f;
    u32x4 wf[3][3];
    f32x4 cs[3];
    f32x16 accF[2], accQ[NQ];
    u32x4 cf;
    f32x4 s0, s1;
    float ea, eb, ta, tb, nrm = 0.f;
    u32 hp[4], P[8];
    u32x4 f0, f1;

    epoch = sync_first<SC<T>::s.wait[0]>(ws);
    STAMP(1);
    tag = (epoch + 0x9E3779B97F4A7C15ull) * 0xBF58476D1CE4E5B9ull;
    tag = (tag ^ (tag >> 29)) + llvm_dispatch_id() * 0x94D049BB133111EBull;
    tag = (tag ^ (tag >> 31)) * 0xD6E8FEB86659FD93ull;

    auto rd_x = [&](auto J) LAM {
      constexpr int j = J.value, G = j / 4;
      constexpr int o = (p_x(T, G) % 9) * 16384, op = G ? (p_x(T, G - 1) % 9) * 16384 : 0;
      constexpr int d = (o & ~65535) - (op & ~65535);
      if constexpr (d != 0) { xa[j & 3] += d; PIN(xa[j & 3]); }
      xf[j] = lds128(xa[j & 3] + (o & 65535));
    };
    auto rd_w = [&](auto J, auto t_) LAM {
      constexpr int j = J.value, t = t_.value, G = j / 4, u = j / 28;
      constexpr int Gp = (j % 28) ? (j - 1) / 4 : ((T == 0 && u == 2) ? 6 : -1);
      constexpr int o = (p_w(T, G) % 9) * 16384, op = Gp >= 0 ? (p_w(T, Gp) % 9) * 16384 : 0;
      constexpr int d = (o & ~65535) - (op & ~65535);
      u32& ad = u == QU ? qa[t < NQ ? t : 0] : fa[t & 1];
      if constexpr (d != 0) { ad += d; PIN(ad); }
      constexpr u32 im = (o & 65535) + (j & 3) * 2048;
      wf[j % 3][t] = trd2(ad + im, ad + im + 512);
    };
    auto rd_part = [&](auto J, auto lo, auto hi) LAM {
      constexpr int j = J.value;
      if constexpr (j < NS) {
        if constexpr (lo.value == 0 && j < 28) rd_x(J);
        sfor<lo.value, imin(hi.value, n_tile(T, j / 28))>([&](auto t) LAM { rd_w(J, t); });
      }
    };
    auto qkv_part = [&](auto I, auto part) LAM {
      constexpr int i = I.value;
      const int sl = sl0 + NQ * g + i, sec = sl >> 4, hd = sl & 15;
      f32x16& acc = accQ[i];
      const int lane = part.value ? lane_id() : 0, r32 = lane & 31, hf = lane >> 5, blk = (lane >> 4) & 1, q4 = (lane & 15) >> 2, p4 = lane & 3;
      const u32 vl = lane * 16;
      if constexpr (part.value < 2) {
        constexpr int b = 8 * part.value;
        const float sc = sec == 0 ? rstd * (0.18898223650461362f * 1.4426950408889634f) : rstd;
        float v[8];
        sfor<0, 8>([&](auto e) LAM { v[e.value] = acc[b + e.value] * sc; });
        sfor<0, 4>([&](auto e) LAM { P[b / 2 + e.value] = cvtpk(v[2 * e.value], v[2 * e.value + 1]); PIN(P[b / 2 + e.value]); });
        if (sec < 2) {
          if constexpr (b == 0) {
            nrm = v[0] * v[0];
            sfor<1, 8>([&](auto e) LAM { nrm = __builtin_fmaf(v[e.value], v[e.value], nrm); });
          } else {
            float n1 = v[4] * v[4];
            sfor<0, 4>([&](auto e) LAM { nrm = __builtin_fmaf(v[e.value], v[e.value], nrm); });
            sfor<5, 8>([&](auto e) LAM { n1 = __builtin_fmaf(v[e.value], v[e.value], n1); });
            nrm += hf ? 0.f : n1;
          }
          PIN(nrm);
        } else if constexpr (b == 8) {
          P[6] = hf ? 0x3f80u : P[6];
          P[7] = hf ? 0u : P[7];
          const u32 vt = L_VT + w * 1024;
          const u32 wa = vt + (r32 & 15) * 64 + hf * 8, ra = vt + (8 * hf + q4) * 64 + 32 * blk + 8 * p4;
          if (r32 < 16) sfor<0, 4>([&](auto e) LAM { *(LAS u32x2*)(wa + 16 * e.value) = (u32x2){P[2 * e.value], P[2 * e.value + 1]}; });
          cfence();
          f0 = trd2(ra, ra + 256);
          cfence();
          if (r32 >= 16) sfor<0, 4>([&](auto e) LAM { *(LAS u32x2*)(wa + 16 * e.value) = (u32x2){P[2 * e.value], P[2 * e.value + 1]}; });
          cfence();
          f1 = trd2(ra, ra + 256);
          cfence();
        }
      } else {
        if (sec < 2) {
          u32 pad = 0x3f80u;
          const float n = nrm + other_half(nrm);
          if (sec == 0) pad = cvtpk(__builtin_amdgcn_sqrtf(n), 0.f);
          else {
            const float km = wave_max_nonneg(n);
            if (lane == 0) __hip_atomic_store((GAS float*)(ws + WS_KM + (hd * 64 + mtile) * 4), km, __ATOMIC_RELAXED, __HIP_MEMORY_SCOPE_AGENT);
          }
          f0 = (u32x4){P[0], P[1], P[2], P[3]};
          f1 = (u32x4){P[4], P[5], hf ? pad : P[6], hf ? 0u : P[7]};
        }
        const u32 so = sec == 0 ? WS_Q + (hd * 64 + mtile) * 2048 : WS_KV + (hd * 64 + mtile) * 4096 + (sec - 1) * 2048;
        st128(rWS, f0, vl, so);
        st128(rWS, f1, vl + 1024, so);
      }
    };

    rd_part(IC<0>{}, IC<0>{}, IC<3>{});
    rd_part(IC<1>{}, IC<0>{}, IC<3>{});
    SBAR();
    sfor<0, NS>([&](auto J) LAM {
      constexpr int j = J.value, u = j / 28, kk = j % 28, NT = n_tile(T, u);
      constexpr bool sy = (j & 3) == 2 && j / 4 + 1 < n_stage(T);
      f32x16* acc = j >= JL ? accL : (u == QU ? accQ : accF);
      sfor<0, NT>([&](auto t_) LAM {
        constexpr int t = t_.value;
        if constexpr (kk == 0) { acc[t] = mfma32(wf[j % 3][t], xf[kk], (f32x16){}); cs[t] = (f32x4){0.f, 0.f, 0.f, 0.f}; }
        else acc[t] = mfma32(wf[j % 3][t], xf[kk], acc[t]);
        SBAR();
        if constexpr (!sy) { rd_part(IC<j + 2>{}, IC<t>{}, IC<(t == NT - 1 ? 3 : t + 1)>{}); SBAR(); }
      });
      if constexpr (NT == 1) colsum1<kk & 3>(cs[0], sel, wf[j % 3][0], mt);
      else if constexpr (NT == 2) colsum2<kk & 3>(cs[0], cs[1], sel, wf[j % 3][0], wf[j % 3][1], mt);
      else colsum3<kk & 3>(cs[0], cs[1], cs[2], sel, wf[j % 3][0], wf[j % 3][1], wf[j % 3][2], mt);
      if constexpr (sy) {
        sync_stage<SC<T>::s.wait[j / 4 + 1]>();
        rd_part(IC<j + 2>{}, IC<0>{}, IC<3>{});
        SBAR();
        if constexpr (j == JL + PUBK) {
          if (w == 0 && lane_id() == 0) {
            put_flag(ws, WS_FQ + (cb * 16 + rb) * 8, tag);
            if (T == 0) put_flag(ws, WS_FH + (rb * 32 + u0) * 8, tag);
          }
        }
      }
      sfor<SC<T>::s.lo[j], SC<T>::s.lo[j + 1]>([&](auto q) LAM { issue_piece<T, q.value>(c); });
      if constexpr (u == 0) {
        const float a0 = asf(__builtin_amdgcn_perm(xf[kk][2], xf[kk][0], pl)), a1 = asf(__builtin_amdgcn_perm(xf[kk][2], xf[kk][0], ph));
        const float a2 = asf(__builtin_amdgcn_perm(xf[kk][3], xf[kk][1], pl)), a3_ = asf(__builtin_amdgcn_perm(xf[kk][3], xf[kk][1], ph));
        ssum += (a0 + a1) + (a2 + a3_);
        ssq = __builtin_fmaf(a0, a0, ssq); ssq = __builtin_fmaf(a1, a1, ssq);
        ssq = __builtin_fmaf(a2, a2, ssq); ssq = __builtin_fmaf(a3_, a3_, ssq);
        PIN(ssum); PIN(ssq);
      }
      if constexpr (u >= 1) {
        constexpr int NP = n_tile(T, u - 1);
        f32x16* pa = u - 1 == QU ? accQ : accF;
        if constexpr (kk == 2 && u == 1) {
          const u32 sa = L_STAT + (mt * 32 + (lane_id() & 31)) * 32;
          s0 = lds128f(sa); s1 = lds128f(sa + 16);
        }
        if constexpr (kk == 3 && u == 1) {
          const float mean = ((s0[0] + s0[2]) + (s1[0] + s1[2])) * (1.f / 448.f);
          const u32 nm = lane_id() >= 32 ? 0u : cvtpk(-mean, -mean);
          negm = (u32x4){nm, nm, nm, nm};
          float var = ((s0[1] + s0[3]) + (s1[1] + s1[3])) * (1.f / 448.f) - mean * mean;
          var = var > 0.f ? var : 0.f;
          rstd = rcp(bfround(__builtin_amdgcn_sqrtf(bfround(bfround(var) + 1e-5f))));
        }
        sfor<0, NP>([&](auto t) LAM {
          if constexpr (kk == 2 + 3 * t.value) cf = cs_frag(t.value);
          if constexpr (kk == 3 + 3 * t.value) pa[t.value] = mfma32(cf, negm, pa[t.value]);
        });
        if constexpr (kk == 4 && u == 1) {
          const float k1 = -2.f * 0.7978845608028654f * 1.4426950408889634f;
          K1 = k1 * rstd; K2 = k1 * 0.044715f * rstd * rstd * rstd; IR2 = rcp(rstd * rstd);
          PIN(K1); PIN(K2); PIN(IR2);
        }
        if constexpr (T == 0 && u == 1) {
          sfor<0, 8>([&](auto p_) LAM {
            constexpr int p = p_.value, t = p >> 2, e = 2 * (p & 3);
            if constexpr (kk == ge_a(p) + 2) {
              hp[p & 3] = cvtpk(ta * ea, tb * eb); PIN(hp[p & 3]);
              if constexpr ((p & 3) == 3) st128(rWS, (u32x4){hp[0], hp[1], hp[2], hp[3]}, lane_id() * 16 + t * 1024, WS_H + (mtile * 112 + u0 * 4 + 2 * g) * 1024);
            }
          });
          sfor<0, 8>([&](auto p_) LAM {
            constexpr int p = p_.value, t = p >> 2, e = 2 * (p & 3);
            if constexpr (kk == ge_a(p)) { ea = ge_exp(accF[t][e]); eb = ge_exp(accF[t][e + 1]); PIN(ea); PIN(eb); }
            if constexpr (kk == ge_a(p) + 1) {
              ea = ge_rcp(ea); eb = ge_rcp(eb); ta = accF[t][e] * accF[t][e + 8]; tb = accF[t][e + 1] * accF[t][e + 9];
              PIN(ea); PIN(eb); PIN(ta); PIN(tb);
            }
          });
        }
        if constexpr (j >= JL) {
          sfor<0, NQ>([&](auto i) LAM {
            if constexpr (kk >= st_q(T, i.value) - 2 && kk <= st_q(T, i.value)) qkv_part(i, IC<kk - st_q(T, i.value) + 2>{});
          });
          if constexpr (kk == J_PF) { const int lane = lane_id(); touch((const char*)pwo + ((rq & 3) * 112 + w * 14 + (lane < 14 ? lane : 0)) * 896 + nt * 128); }
          if constexpr (kk == J_POLL) aflag(fq, ws + fq_off());
        }
      }
      if constexpr (kk == 27) {
        LANES();
        sfor<0, NT>([&](auto t) LAM { if constexpr (t.value == 0) nop_on(cs[t.value]); else PIN(cs[t.value]); });
        sfor<0, NT>([&](auto t) LAM {
          const float v = cs[t.value][0];
          const float hi = bfround(v);
          *(LAS u32*)(L_CS + ((g * 3 + t.value) * 32 + r32) * 16 + mt * 4) = cvtpk(hi, v - hi);
        });
        if constexpr (u == 0) *(LAS f32x2*)(L_STAT + ((mt * 32 + r32) * 4 + g * 2 + hf) * 8) = (f32x2){ssum, ssq};
        if constexpr (j == 27) STAMP(2);
        if constexpr (j == JL - 1) STAMP(3);
      }
      SBAR();
    });
  };
  if (ci < 3) phase1(IC<0>{}); else phase1(IC<1>{});
  STAMP(4);
  sync_stage<63>();
  LANES();
  auto last_ffn = [&]() LAM {
    sfor<0, 2>([&](auto t) LAM { accL[t.value] = mfma32(cs_frag(t.value), negm, accL[t.value]); });
    sfor<0, 2>([&](auto t) LAM {
      u32 hp[4];
      const f32x16& a = accL[t.value];
      sfor<0, 4>([&](auto j) LAM {
        constexpr int e = 2 * j.value;
        hp[j.value] = cvtpk(a[e] * a[e + 8] * ge_rcp(ge_exp(a[e])), a[e + 1] * a[e + 9] * ge_rcp(ge_exp(a[e + 1])));
      });
      st128(rWS, (u32x4){hp[0], hp[1], hp[2], hp[3]}, vl + t.value * 1024, WS_H + (mtile * 112 + u1 * 4 + 2 * g) * 1024);
    });
  };
  auto h_done = [&]() LAM {
    cfence();
    if (lane_id() == 0) {
      const u32 n = __hip_atomic_fetch_add((LAS u32*)(L_CNT), 1u, __ATOMIC_RELAXED, __HIP_MEMORY_SCOPE_WORKGROUP);
      if (n == 7) put_flag(ws, WS_FH + (rb * 32 + u1) * 8, tag);
    }
    cfence();
  };

  if (w < 4) {
    if (PRIO) __builtin_amdgcn_s_setprio(2);
    vm_on1<0>(fq);
    if (__builtin_amdgcn_ballot_w64(fq != tag) != 0) poll_flags(ws, fq_off(), tag);
    STAMP(5);
    const u32 kvb = WS_KV + (head * 64 + w * 16) * 4096;
    auto kvo = [&](int kt) LAM -> u32 { return kvb + ((kt + qb) & 15) * 4096; };
    u32x4 Q[4][2], KV[4][4];
    const float km = asf(__builtin_amdgcn_raw_buffer_load_b32(rWS, lane * 4, WS_KM + head * 256, 16));
    sfor<0, 4>([&](auto t) LAM {
      const u32 so = WS_Q + (head * 64 + qb * 4 + ((w + 1 + t.value) & 3)) * 2048;
      Q[t.value][1] = ld128(rWS, vl + 1024, so);
      Q[t.value][0] = ld128(rWS, vl, so);
    });
    sfor<0, 3>([&](auto b) LAM { sfor<0, 4>([&](auto i) LAM { KV[b.value][i.value] = ld128(rWS, vl + i.value * 1024, kvo(b.value)); }); });
    SBAR();
    last_ffn();
    SBAR();
    STAMP(6);
    const float kn = __builtin_amdgcn_sqrtf(wave_max_nonneg(km)) * 1.01f;
    bool safe = true;
    sfor<0, 4>([&](auto t) LAM {
      const float m = asf(Q[t.value][1][2] << 16) * kn;
      safe = safe && (hf == 0 || m <= 48.f);
      Q[t.value][1][2] = hf ? cvtpk(-m, 0.f) : Q[t.value][1][2];
    });
    const int idx = 0xDDDD;
    f32x16 O[4] = {};
    const f32x16 Z = {};
    auto rgn = [&](int t) LAM -> u32 { return t == 3 ? 0u : (6 + t) * 16384u; };
    auto put_part = [&](auto t, auto i) LAM {
      const f32x16& o = O[t.value];
      *(LAS f32x4*)(rgn((w + 1 + t.value) & 3) + t.value * 4096 + i.value * 1024 + vl) =
          (f32x4){o[4 * i.value], o[4 * i.value + 1], o[4 * i.value + 2], o[4 * i.value + 3]};
    };
    if (__builtin_amdgcn_ballot_w64(!safe) == 0) {
      f32x16 Sx[4];
      u32 cb1 = 0;
      Sx[0] = mfma32(KV[0][1], Q[0][1], mfma32(KV[0][0], Q[0][0], Z));
      SBAR();
      sfor<0, 64>([&](auto I) LAM {
        constexpr int kt = I.value >> 2, b = kt & 3, t = I.value & 3, tn = (t + 1) & 3, tp = (t + 3) & 3;
        constexpr int bn = t == 3 ? (b + 1) & 3 : b, bp = t == 0 ? (b + 3) & 3 : b;
        constexpr int E1 = PAT ? 3 : 4, E2 = 2 * E1, E3 = 3 * E1;
        auto exps = [&](auto lo, auto hi) LAM {
          SBAR();
          sfor<lo.value, hi.value>([&](auto e) LAM { Sx[t][e.value] = ex2(Sx[t][e.value]); });
          PIN(Sx[t]);
          SBAR();
        };
        if constexpr (I.value < 63) Sx[tn] = mfma32(KV[bn][0], Q[tn][0], Z);
        exps(IC<0>{}, IC<E1>{});
        if constexpr (I.value < 63) Sx[tn] = mfma32(KV[bn][1], Q[tn][1], Sx[tn]);
        exps(IC<E1>{}, IC<E2>{});
        if constexpr (I.value > 0) O[tp] = smf_hi(KV[bp][3], Sx[tp], O[tp], idx);
        exps(IC<E2>{}, IC<E3>{});
        O[t] = smf_lo(KV[b][2], Sx[t], O[t], idx);
        exps(IC<E3>{}, IC<E3 + 2>{});
        if constexpr (kt + 3 < 16) KV[(b + 3) & 3][t] = ld128(rWS, vl + t * 1024, kvo(kt + 3));
        if constexpr (I.value == 8) { vmwait<8>(); h_done(); }
        if constexpr (I.value == 58) cb1 = cnt_get(1);
        if constexpr (I.value == 60) { if (has_tile && __builtin_amdgcn_readfirstlane(cb1) < 4u) cnt_wait(1, 4); cfence(); }
        if constexpr (I.value >= 62) sfor<0, 4>([&](auto i) LAM { put_part(IC<I.value - 62>{}, i); });
        exps(IC<E3 + 2>{}, IC<16>{});
      });
      O[3] = smf_hi(KV[3][3], Sx[3], O[3], idx);
    } else {
      vmwait<0>();
      h_done();
      sfor<0, 4>([&](auto t) LAM { Q[t.value][1][2] = hf ? 0u : Q[t.value][1][2]; });
      float mx[4] = {-3.0e38f, -3.0e38f, -3.0e38f, -3.0e38f};
      for (int kt = 0; kt < 16; ++kt) {
        const u32x4 k0 = ld128(rWS, vl, kvb + kt * 4096), k1 = ld128(rWS, vl + 1024, kvb + kt * 4096);
        sfor<0, 4>([&](auto t) LAM {
          const f32x16 s = mfma32(k1, Q[t.value][1], mfma32(k0, Q[t.value][0], Z));
          sfor<0, 16>([&](auto i) LAM { mx[t.value] = __builtin_fmaxf(mx[t.value], s[i.value]); });
        });
      }
      sfor<0, 4>([&](auto t) LAM {
        mx[t.value] = __builtin_fmaxf(mx[t.value], other_half(mx[t.value]));
        *(LAS float*)(L_VT + ((((w + 1 + t.value) & 3) * 32 + r32) * 4 + w) * 4) = mx[t.value];
      });
      cnt_add(3);
      cnt_wait(3, 4);
      sfor<0, 4>([&](auto t) LAM {
        const f32x4 m4 = lds128f(L_VT + (((w + 1 + t.value) & 3) * 32 + r32) * 16);
        mx[t.value] = __builtin_fmaxf(__builtin_fmaxf(m4[0], m4[1]), __builtin_fmaxf(m4[2], m4[3]));
      });
      for (int kt = 0; kt < 16; ++kt) {
        const u32x4 k0 = ld128(rWS, vl, kvb + kt * 4096), k1 = ld128(rWS, vl + 1024, kvb + kt * 4096);
        const u32x4 v0 = ld128(rWS, vl + 2048, kvb + kt * 4096), v1 = ld128(rWS, vl + 3072, kvb + kt * 4096);
        sfor<0, 4>([&](auto t) LAM {
          f32x16 s = mfma32(k1, Q[t.value][1], mfma32(k0, Q[t.value][0], Z));
          sfor<0, 16>([&](auto i) LAM { s[i.value] = ex2(s[i.value] - mx[t.value]); });
          O[t.value] = smf_hi(v1, s, smf_lo(v0, s, O[t.value], idx), idx);
        });
      }
      if (has_tile) cnt_wait(1, 4);
      sfor<0, 2>([&](auto t) LAM { sfor<0, 4>([&](auto i) LAM { put_part(t, i); }); });
    }
    STAMP(7);
    sfor<0, 4>([&](auto i) LAM { put_part(IC<2>{}, i); });
    cnt_add(2);
    cnt_wait(2, 4);
    const u32 red = rgn(w);
    {
      LANES();
      float o[16];
      sfor<0, 4>([&](auto i) LAM {
        const u32 ra = red + i.value * 1024 + vl;
        const f32x4 a0 = lds128f(ra), a1 = lds128f(ra + 4096), a2 = lds128f(ra + 8192);
        sfor<0, 4>([&](auto j) LAM { o[4 * i.value + j.value] = (a0[j.value] + a1[j.value]) + (a2[j.value] + O[3][4 * i.value + j.value]); });
      });
      const float lo_ = other_half(o[12]);
      const float il = rcp(hf ? o[12] : lo_);
      u32 P[8];
      sfor<0, 8>([&](auto i) LAM { P[i.value] = cvtpk(o[2 * i.value] * il, o[2 * i.value + 1] * il); });
      const int om = qb * 4 + w;
      st128(rWS, (u32x4){P[0], P[1], P[2], P[3]}, vl, WS_O + (om * 16 + head) * 2048);
      st128(rWS, (u32x4){P[4], P[5], hf ? 0u : P[6], hf ? 0u : P[7]}, vl + 1024, WS_O + (om * 16 + head) * 2048);
      if (has_tile) {
        const u32x4 sO = mk_srd(pwo, D_ * D_ * 2);
        const u32 vw = (lane >> 3) * 896 + (((lane & 7) ^ ((lane >> 2) & 4)) << 4);
        sfor<0, 14>([&](auto e) LAM { dma_i<e.value * 1024, 8 * e.value * 896>(sO, vw, 112 * w * 896 + nt * 128, red); });
        vmwait<14>();
      } else vmwait<0>();
      if (lane == 0) put_flag(ws, WS_FO + (om * 16 + head) * 8, tag);
    }
    STAMP(8);
    if (!has_tile) return;
    {
    LANES();
    const u32 trow = (4 * hf + q4) * 128, swz = (q4 & 2) << 5;
    const int m = w & 1, kh = w >> 1;
    const rsrc_t rX = __builtin_amdgcn_make_buffer_rsrc((void*)px, 0, S_ * D_ * 2, 0x00020000);
    const u32 xo = ((rt * 64 + m * 32 + r32) * 448 + nt * 64 + kh * 32 + hf * 8) * 2;
    u32x4 xr[2];
    xr[0] = __builtin_amdgcn_raw_buffer_load_b128(rX, xo, 0, 0);
    xr[1] = __builtin_amdgcn_raw_buffer_load_b128(rX, xo + 32, 0, 0);
    PIN(xr[0]); PIN(xr[1]);
    vmwait<0>();
    cnt_add(6);
    poll_flags(ws, WS_FO + ((2 * rt + m) * 16 + 8 * kh + (lane & 7)) * 8, tag);
    STAMP(9);
    u32x4 of[16];
    sfor<0, 16>([&](auto k) LAM { of[k.value] = ld128(rWS, vl + (k.value & 3) * 1024, WS_O + ((2 * rt + m) * 16 + 8 * kh) * 2048 + (k.value >> 2) * 4096); });
    SBAR();
    cnt_wait(6, 4);
    cnt_wait(5, 4);
    f32x4 gt[4];
    sfor<0, 4>([&](auto i) LAM { gt[i.value] = lds128f(4 * 16384 + w * 4096 + i.value * 1024 + vl); });
    u32 xs[8] = {xr[0][0], xr[0][1], xr[0][2], xr[0][3], xr[1][0], xr[1][1], xr[1][2], xr[1][3]};
    swap32(xs[0], xs[2]); swap32(xs[1], xs[3]); swap32(xs[4], xs[6]); swap32(xs[5], xs[7]);
    f32x16 acc[2] = {};
    sfor<0, 2>([&](auto r) LAM {
      const u32 rg = rgn(2 * kh + r.value);
      u32 wa[2], wp[2];
      sfor<0, 2>([&](auto n) LAM {
        wa[n.value] = rg + trow + ((64 * (n.value ^ kh) + 32 * blk + 8 * p4) ^ swz);
        wp[n.value] = wa[n.value] - hf * 512;
      });
      sfor<0, 8>([&](auto k) LAM {
        constexpr int hh = k.value >> 1, s = k.value & 1;
        constexpr u32 off = (28 * hh + 16 * s) * 128;
        sfor<0, 2>([&](auto n) LAM {
          constexpr int nn = 1 - n.value;
          const u32x4 wf = trd2(wa[nn] + off, (s ? wp[nn] : wa[nn]) + off + 1024);
          acc[nn] = mfma32(wf, of[8 * r.value + k.value], acc[nn]);
        });
      });
    });
    sfor<0, 4>([&](auto i) LAM {
      *(LAS f32x4*)(5 * 16384 + w * 4096 + i.value * 1024 + vl) =
          (f32x4){acc[1][4 * i.value], acc[1][4 * i.value + 1], acc[1][4 * i.value + 2], acc[1][4 * i.value + 3]};
    });
    STAMP(10);
    cnt_add(7);
    cnt_wait(7, 4);
    u32 R[8];
    sfor<0, 4>([&](auto i) LAM {
      const f32x4 a = lds128f(5 * 16384 + (w ^ 2) * 4096 + i.value * 1024 + vl);
      float y[4];
      sfor<0, 4>([&](auto j) LAM {
        const u32 xd = xs[2 * i.value + (j.value >> 1)];
        const float xv = asf((j.value & 1) ? (xd & 0xffff0000u) : (xd << 16));
        y[j.value] = __builtin_fmaf(acc[0][4 * i.value + j.value] + a[j.value], gt[i.value][j.value], xv);
      });
      R[2 * i.value] = cvtpk(y[0], y[1]);
      R[2 * i.value + 1] = cvtpk(y[2], y[3]);
    });
    swap32(R[0], R[2]); swap32(R[1], R[3]); swap32(R[4], R[6]); swap32(R[5], R[7]);
    const rsrc_t rOut = __builtin_amdgcn_make_buffer_rsrc(pout, 0, S_ * D_ * 2, 0x00020000);
    st128p(rOut, (u32x4){R[0], R[1], R[2], R[3]}, xo);
    st128p(rOut, (u32x4){R[4], R[5], R[6], R[7]}, xo + 32);
    asm volatile(".Lfb_enda:");
    STAMP(11);
    }
  } else {
    const u32 trow = (4 * hf + q4) * 128, swz = (q4 & 2) << 5;
    const int gw = w - 4;
    last_ffn();
    const u32 od = 448 * gw * 896 + nt * 128, lg = gw * 4096;
    auto krot = [&](int k) LAM -> u32 { const int v = k + rotg; return v >= 28 ? v - 28 : v; };
    auto wload = [&](auto K) LAM {
      dma<wd_loc(K.value), 0, 16 * 896>(c.sD, c.vw, od, krot(K.value), lg);
      dma<wd_loc(K.value) + 1024, 0, 16 * 896>(c.sD, c.vw, od + 8 * 896, krot(K.value), lg);
    };
    if (has_tile) { wload(IC<16>{}); wload(IC<17>{}); vmwait<4>(); } else vmwait<0>();
    PIN(fq);
    h_done();
    STAMP(5);
    if (bid == 255 && gw == 0) {
      sfor<0, 4>([&](auto i) LAM { poll_flags(ws, WS_FQ + (i.value * 64 + lane) * 8, tag); });
      if (lane == 0) put_flag(ws, WS_EPOCH, epoch + 1);
    }
    if (!has_tile) return;
    const u32x4 sH = mk_srd(ws + WS_H, 64 * 112 * 1024);
    const u32 oh0 = ((2 * rt + (gw & 1)) * 112 + 28 * gw) * 1024, oh1 = ((2 * rt + 1 - (gw & 1)) * 112 + 28 * gw) * 1024;
    u32x4 hfr[DPH][2];
    auto hload = [&](auto K) LAM {
      aload_sc1(hfr[K.value % DPH][0], vl, sH, oh0 + krot(K.value) * 1024);
      aload_sc1(hfr[K.value % DPH][1], vl, sH, oh1 + krot(K.value) * 1024);
    };
    cnt_wait(0, 8);
    poll_flags(ws, WS_FH + ((rt >> 1) * 32 + 7 * gw + (lane < 7 ? lane : 0)) * 8, tag);
    STAMP(6);
    sfor<0, DPH>([&](auto K) LAM { hload(K); });
    u32 wa[2];
    sfor<0, 2>([&](auto n) LAM { wa[n.value] = lg + trow + ((64 * (n.value ^ (gw >> 1)) + 32 * blk + 8 * p4) ^ swz); });
    f32x16 acc[2][2] = {};
    u32x4 wf[2][2];
    auto ready = [&](auto K, auto N) LAM {
      constexpr int k = K.value;
      constexpr int o = wd_loc(k), d = (o & ~65535) - (k ? (int)(wd_loc(k - 1) & ~65535) : 0);
      vm_on2<N.value>(hfr[k % DPH][0], hfr[k % DPH][1]);
      sfor<0, 2>([&](auto n) LAM {
        if constexpr (d != 0) { wa[n.value] += d; PIN(wa[n.value]); }
        wf[k & 1][n.value] = trd2(wa[n.value] + (o & 65535), wa[n.value] + (o & 65535) + 1024);
      });
    };
    ready(IC<0>{}, IC<2 * (DPH - 1)>{});
    sfor<0, 28>([&](auto K) LAM {
      constexpr int k = K.value;
      if constexpr (k + 1 < 28) ready(IC<k + 1>{}, IC<gemm_wait(k + 1)>{});
      sfor<0, 2>([&](auto n) LAM {
        sfor<0, 2>([&](auto m) LAM { acc[m.value][n.value] = mfma32(wf[k & 1][n.value], hfr[k % DPH][m.value], acc[m.value][n.value]); });
      });
      SBAR();
      if constexpr (k == 17) cnt_add(1);
      if constexpr (k + 18 < 28) wload(IC<k + 18>{});
      if constexpr (k + DPH < 28) hload(IC<k + DPH>{});
      SBAR();
    });
    STAMP(7);
    sfor<1, 4>([&](auto x) LAM {
      const f32x16& a = acc[x.value & 1][x.value >> 1];
      sfor<0, 4>([&](auto i) LAM {
        *(LAS f32x4*)(x.value * 16384 + lg + i.value * 1024 + vl) = (f32x4){a[4 * i.value], a[4 * i.value + 1], a[4 * i.value + 2], a[4 * i.value + 3]};
      });
    });
    cnt_add(4);
    cnt_wait(4, 4);
    sfor<0, 4>([&](auto i) LAM {
      const u32 ra = i.value * 1024 + vl;
      const f32x4 a1 = lds128f(16384 + (gw ^ 1) * 4096 + ra), a2 = lds128f(2 * 16384 + (gw ^ 2) * 4096 + ra), a3_ = lds128f(3 * 16384 + (gw ^ 3) * 4096 + ra);
      f32x4 gv;
      sfor<0, 4>([&](auto j) LAM {
        const float f = (acc[0][0][4 * i.value + j.value] + a1[j.value]) + (a2[j.value] + a3_[j.value]);
        gv[j.value] = f * rcp(1.f + ex2(f * -1.4426950408889634f));
      });
      *(LAS f32x4*)(4 * 16384 + lg + ra) = gv;
    });
    asm volatile(".Lfb_endb:");
    cnt_add(5);
    STAMP(10);
  }
}

extern "C" void kernel_launch(void* const* d_in, const int* in_sizes, int n_in, void* d_out, int out_size, void* d_ws,
                              size_t ws_size, hipStream_t stream) {
  fused_block<<<256, 512, 0, stream>>>(d_in[0], d_in[1], d_in[2], d_in[4], d_in[5], d_in[6], d_out, (char*)d_ws);
}
```

```cpp
#include <hip/hip_runtime.h>
#include <stdint.h>
#pragma clang diagnostic ignored "-Wint-to-pointer-cast"

#if defined(__HIP_DEVICE_COMPILE__)
#define NOPK __attribute__((target("no-packed-fp32-ops")))
#else
#define NOPK
#endif
#define DEV __device__ __forceinline__ NOPK
#define LAM __attribute__((always_inline)) NOPK
#define LAS __attribute__((address_space(3)))
#define GAS __attribute__((address_space(1)))

#define STAMPS 0

typedef unsigned u32;
typedef unsigned long long u64;
typedef u32 u32x2 __attribute__((ext_vector_type(2)));
typedef u32 u32x4 __attribute__((ext_vector_type(4)));
typedef short s16x4 __attribute__((ext_vector_type(4)));
typedef float f32x2 __attribute__((ext_vector_type(2)));
typedef float f32x4 __attribute__((ext_vector_type(4)));
typedef float f32x8 __attribute__((ext_vector_type(8)));
typedef float f32x16 __attribute__((ext_vector_type(16)));
typedef __bf16 bf16x8 __attribute__((ext_vector_type(8)));
typedef __bf16 bf16x16 __attribute__((ext_vector_type(16)));

extern "C" __device__ u64 llvm_dispatch_id() __asm("llvm.amdgcn.dispatch.id");

constexpr int S_ = 2048, D_ = 448, F_ = 1792;
#define V3 1
#define V4 1
constexpr u32 WS_EPOCH = 0, WS_FO = 4096, WS_FH = 16384, WS_ST = 32768, WS_DBG = 65536;
constexpr u32 WS_Q = 1u << 20, WS_KV = 3u << 20, WS_O = 7u << 20, WS_H = 9u << 20;
constexpr u32 WS_FQ = 16u << 20, WS_QN = WS_FQ + (256u << 10), WS_END = 17u << 20;
constexpr u32 L_QK = 147456, L_CS = 159744, L_CNT = L_CS + 2560, L_DUM = L_CS + 2816;
constexpr u32 L_VT = L_QK;
constexpr u32 LDS_BYTES = 163840;

template <int V> struct IC { static constexpr int value = V; };
template <int I, int N, class Fn> DEV void sfor(Fn&& f) {
  if constexpr (I < N) { f(IC<I>{}); sfor<I + 1, N>(f); }
}

#define SBAR() __builtin_amdgcn_sched_barrier(0)
template <class T> DEV void PIN(T& x) { asm volatile("" : "+v"(x)); }
template <class T> DEV void nop_on(T& x) { asm volatile("s_nop 7" : "+v"(x)); }
template <class T> DEV void lgkm0_on(T& x) { asm volatile("s_waitcnt lgkmcnt(0)" : "+v"(x)::"memory"); }
template <int K> DEV void colsum5(f32x4* c, u32x4 one, const u32x4* w, int who) {
  asm volatile("s_cmp_lg_u32 %11, %12\n\ts_cbranch_scc1 .Lcs%=\n\tv_mfma_f32_16x16x32_bf16 %0, %5, %6, %0\n\tv_mfma_f32_16x16x32_bf16 %1, %5, %7, %1\n\t"
               "v_mfma_f32_16x16x32_bf16 %2, %5, %8, %2\n\tv_mfma_f32_16x16x32_bf16 %3, %5, %9, %3\n\tv_mfma_f32_16x16x32_bf16 %4, %5, %10, %4\n.Lcs%=:"
               : "+v"(c[0]), "+v"(c[1]), "+v"(c[2]), "+v"(c[3]), "+v"(c[4])
               : "v"(one), "v"(w[0]), "v"(w[1]), "v"(w[2]), "v"(w[3]), "v"(w[4]), "s"(who), "i"(K) : "scc");
}
DEV void aload(u32x4& d, u32 voff, u32x4 srd, u32 soff) {
  asm volatile("buffer_load_dwordx4 %0, %1, %2, %3 offen" : "=v"(d) : "v"(voff), "s"(srd), "s"(soff) : "memory");
}
template <int N> DEV void vm_on(u32x4& a) { asm volatile("s_waitcnt vmcnt(%1)" : "+v"(a) : "i"(N) : "memory"); }
DEV void aload_sc1(u32x4& d, u32 voff, u32x4 srd, u32 soff) {
  asm volatile("buffer_load_dwordx4 %0, %1, %2, %3 offen sc1" : "=v"(d) : "v"(voff), "s"(srd), "s"(soff) : "memory");
}
template <int N> DEV void vm_on2(u32x4& a, u32x4& b) { asm volatile("s_waitcnt vmcnt(%2)" : "+v"(a), "+v"(b) : "i"(N) : "memory"); }

DEV f32x16 mfma32(u32x4 a, u32x4 b, f32x16 c) {
  return __builtin_amdgcn_mfma_f32_32x32x16_bf16(__builtin_bit_cast(bf16x8, a), __builtin_bit_cast(bf16x8, b), c, 0, 0, 0);
}
DEV f32x4 mfma16(u32x4 a, u32x4 b, f32x4 c) {
  return __builtin_amdgcn_mfma_f32_16x16x32_bf16(__builtin_bit_cast(bf16x8, a), __builtin_bit_cast(bf16x8, b), c, 0, 0, 0);
}
DEV f32x16 smf_lo(u32x4 a, f32x16 p, f32x16 c, int idx) {
  f32x8 b = __builtin_shufflevector(p, p, 0, 1, 2, 3, 4, 5, 6, 7);
  return __builtin_amdgcn_smfmac_f32_32x32x32_bf16(__builtin_bit_cast(bf16x8, a), __builtin_bit_cast(bf16x16, b), c, idx, 0, 0);
}
DEV f32x16 smf_hi(u32x4 a, f32x16 p, f32x16 c, int idx) {
  f32x8 b = __builtin_shufflevector(p, p, 8, 9, 10, 11, 12, 13, 14, 15);
  return __builtin_amdgcn_smfmac_f32_32x32x32_bf16(__builtin_bit_cast(bf16x8, a), __builtin_bit_cast(bf16x16, b), c, idx, 0, 0);
}
typedef __bf16 bf16x2 __attribute__((ext_vector_type(2)));
DEV u32 cvtpk(float lo, float hi) {
  f32x2 v = {lo, hi};
  return __builtin_bit_cast(u32, __builtin_convertvector(v, bf16x2));
}
DEV float asf(u32 u) { return __builtin_bit_cast(float, u); }
DEV u32 asu(float f) { return __builtin_bit_cast(u32, f); }
DEV float bfround(float f) { return asf(cvtpk(f, f) << 16); }
DEV float ex2(float x) { return __builtin_amdgcn_exp2f(x); }
DEV float rcp(float x) { return __builtin_amdgcn_rcpf(x); }
DEV float other_half(float v) {
  auto r = __builtin_amdgcn_permlane32_swap(asu(v), asu(v), false, false);
  return asf(r[0] ^ r[1] ^ asu(v));
}
DEV u32x4 mk_srd(const void* p, u32 bytes) {
  u64 a = (u64)p;
  u32x4 r = {(u32)a, (u32)(a >> 32) & 0xffffu, bytes, 0x00020000u};
  return r;
}
template <int LD> DEV void dma_half(u32x4 srd, u32 voff, u32 so, u32 l0) {
#define EXH "0"
  asm volatile("s_add_u32 m0, %0, %1\n\ts_mov_b32 exec_hi, " EXH "\n\tbuffer_load_dwordx4 %2, %3, %4 offen lds\n\ts_mov_b32 exec_hi, -1"
               ::"s"(l0), "i"(LD), "v"(voff), "s"(srd), "s"(so) : "memory", "scc");
}
template <int LD, int OFF> DEV void dma1(u32x4 srd, u32 voff, u32 so, u32 l0) {
  asm volatile("s_add_u32 m0, %0, %1\n\ts_nop 0\n\tbuffer_load_dwordx4 %2, %3, %4 offen offset:%5 lds"
               ::"s"(l0), "i"(LD), "v"(voff), "s"(srd), "s"(so), "i"(OFF) : "memory", "scc");
}
template <int LD, int OFF> DEV void dma2(u32x4 s0, u32 v0, u32x4 s1, u32 v1, u32 so, u32 l0) {
  asm volatile("s_add_u32 m0, %0, %1\n\ts_nop 0\n\tbuffer_load_dwordx4 %2, %3, %6 offen offset:%7 lds\n\tbuffer_load_dwordx4 %4, %5, %6 offen offset:%8 lds"
               ::"s"(l0), "i"(LD), "v"(v0), "s"(s0), "v"(v1), "s"(s1), "s"(so), "i"(OFF), "i"(OFF + 1024) : "memory", "scc");
}
DEV u32 opaque(u32 x) { asm volatile("" : "+s"(x)); return x; }
template <int LD, int ADD> DEV void dma_i(u32x4 srd, u32 voff, u32 s0, u32 l0) {
  u32 t;
  asm volatile("s_add_u32 m0, %1, %2\n\ts_add_u32 %0, %3, %4\n\tbuffer_load_dwordx4 %5, %6, %0 offen lds"
               : "=&s"(t) : "s"(l0), "i"(LD), "s"(s0), "i"(ADD), "v"(voff), "s"(srd) : "memory", "scc");
}
DEV int lane_id() {
  u32 z = 0;
  asm volatile("" : "+s"(z));
  return __builtin_amdgcn_mbcnt_hi(~0u, __builtin_amdgcn_mbcnt_lo(~0u, z));
}
#define LANES() const int lane = lane_id(), r32 = lane & 31, hf = lane >> 5, blk = (lane >> 4) & 1, q4 = (lane & 15) >> 2, p4 = lane & 3; \
  const u32 vl = lane * 16; (void)r32; (void)hf; (void)blk; (void)q4; (void)p4; (void)vl
DEV void touch(const void* p) {
  asm volatile("s_mov_b32 m0, %0\n\ts_nop 0\n\tglobal_load_lds_dword %1, off" ::"i"(L_DUM), "v"(p) : "memory");
}
template <int N> DEV void vmwait() { asm volatile("s_waitcnt vmcnt(%0)" ::"i"(N) : "memory"); }
DEV void cfence() { asm volatile("" ::: "memory"); }
DEV void cnt_add(int i) {
  cfence();
  if (lane_id() == 0) __hip_atomic_fetch_add((LAS u32*)(L_CNT + 4 * i), 1u, __ATOMIC_RELAXED, __HIP_MEMORY_SCOPE_WORKGROUP);
  cfence();
}
DEV u32 cnt_get(int i) { return __hip_atomic_load((LAS u32*)(L_CNT + 4 * i), __ATOMIC_RELAXED, __HIP_MEMORY_SCOPE_WORKGROUP); }
DEV void cnt_wait(int i, u32 n) {
  while (__builtin_amdgcn_readfirstlane(cnt_get(i)) < n) __builtin_amdgcn_s_sleep(1);
  cfence();
}
DEV u32x2 trd(u32 a) { return __builtin_bit_cast(u32x2, __builtin_amdgcn_ds_read_tr16_b64_v4i16((LAS s16x4*)a)); }
DEV u32x4 trd2(u32 a0, u32 a1) { u32x2 x = trd(a0), y = trd(a1); u32x4 r = {x[0], x[1], y[0], y[1]}; return r; }
DEV u32x4 lds128(u32 a) { return *(LAS u32x4*)a; }
DEV f32x4 lds128f(u32 a) { return *(LAS f32x4*)a; }
template <int N> DEV void sync_stage() { asm volatile("s_waitcnt vmcnt(%0) lgkmcnt(0)\n\ts_barrier" ::"i"(N) : "memory"); }
template <int I> DEV void wlane(u32& f, u32 v) { asm volatile("v_writelane_b32 %0, %1, %2" : "+v"(f) : "s"(v), "i"(I)); }
template <int N, int I> DEV void sync_fine(u32& f) {
  u64 a, b, c;
  asm volatile("s_memtime %1\n\ts_waitcnt vmcnt(%4) lgkmcnt(0)\n\ts_memtime %2\n\ts_barrier\n\ts_memtime %3\n\ts_waitcnt lgkmcnt(0)"
               : "+v"(f), "=&s"(a), "=&s"(b), "=&s"(c) : "i"(N) : "memory");
  wlane<I>(f, (u32)a); wlane<I + 1>(f, (u32)b); wlane<I + 2>(f, (u32)c);
}
template <int H> DEV void bar_if(int w) {
  asm volatile("s_bitcmp%1_b32 %0, 2\n\ts_cbranch_scc0 .Lbi%=\n\ts_waitcnt lgkmcnt(0)\n\ts_barrier\n.Lbi%=:" ::"s"(w), "i"(H) : "memory", "scc");
}
template <int H, int I> DEV void bar_fine(int w, u32& f) {
  if ((w >> 2) == H) sync_fine<63, I>(f);
}
template <int N> DEV u64 sync_first(const char* p) {
  u64 e;
  asm volatile("s_load_dwordx2 %0, %1, 0x0\n\ts_waitcnt vmcnt(%2) lgkmcnt(0)\n\ts_barrier" : "=s"(e) : "s"(p), "i"(N) : "memory");
  return e;
}
typedef __amdgpu_buffer_rsrc_t rsrc_t;
DEV void st128(rsrc_t r, u32x4 v, u32 voff, u32 soff) {
  __builtin_amdgcn_raw_buffer_store_b128(v, r, voff, __builtin_amdgcn_readfirstlane(soff), 16);
  asm volatile("s_nop 1" ::"v"(v) : "memory");
}
DEV void st128p(rsrc_t r, u32x4 v, u32 voff) {
  __builtin_amdgcn_raw_buffer_store_b128(v, r, voff, 0, 16);
}
DEV void aflag(u64& d, const char* p) { asm volatile("global_load_dwordx2 %0, %1, off sc1" : "=v"(d) : "v"(p) : "memory"); }
template <int N> DEV void vm_on1(u64& a) { asm volatile("s_waitcnt vmcnt(%1)" : "+v"(a) : "i"(N) : "memory"); }
DEV void swap32(u32& a, u32& b) {
  auto r = __builtin_amdgcn_permlane32_swap(a, b, false, false);
  a = r[0]; b = r[1];
}
DEV void swap16(u32& a, u32& b) {
  auto r = __builtin_amdgcn_permlane16_swap(a, b, false, false);
  a = r[0]; b = r[1];
}
DEV u32 from_lane(int l, u32 v) { return __builtin_amdgcn_ds_bpermute(l * 4, v); }
DEV u32x4 ld128(rsrc_t r, u32 voff, u32 soff) { return __builtin_amdgcn_raw_buffer_load_b128(r, voff, __builtin_amdgcn_readfirstlane(soff), 16); }
DEV void put_flag(char* ws, u32 off, u64 tag) { __hip_atomic_store((GAS u64*)(ws + off), tag, __ATOMIC_RELAXED, __HIP_MEMORY_SCOPE_AGENT); }
template <int NAP> DEV void poll_flags(char* ws, u32 off, u64 tag) {
  for (u32 spins = 0; spins < (1u << 20); ++spins) {
    u64 v = __hip_atomic_load((GAS u64*)(ws + off), __ATOMIC_RELAXED, __HIP_MEMORY_SCOPE_AGENT);
    if (__builtin_amdgcn_ballot_w64(v != tag) == 0) break;
    __builtin_amdgcn_s_sleep(NAP);
  }
  __builtin_amdgcn_fence(__ATOMIC_ACQUIRE, "wavefront");
  asm volatile("" ::: "memory");
}
DEV float wave_max_nonneg(float v) {
  u32 x = asu(v);
#define DPPMAX(ctrl) { u32 y = __builtin_amdgcn_update_dpp(0u, x, ctrl, 0xf, 0xf, true); x = x > y ? x : y; }
  DPPMAX(0x111) DPPMAX(0x112) DPPMAX(0x114) DPPMAX(0x118) DPPMAX(0x142) DPPMAX(0x143)
#undef DPPMAX
  return asf(__builtin_amdgcn_readlane(x, 63));
}

DEV float wave_sum(float v) {
#define DPPADD(ctrl) v += asf(__builtin_amdgcn_update_dpp(0u, asu(v), ctrl, 0xf, 0xf, true));
  DPPADD(0x111) DPPADD(0x112) DPPADD(0x114) DPPADD(0x118) DPPADD(0x142) DPPADD(0x143)
#undef DPPADD
  return asf(__builtin_amdgcn_readlane(asu(v), 63));
}
DEV bool fresh(u32x4 f, u64 tag) { return __builtin_amdgcn_ballot_w64(f[1] != (u32)tag || f[3] != (u32)(tag >> 32)) == 0; }
DEV void await(u32x4& f, u32 voff, u32x4 srd, u32 soff, u64 tag) {
  for (u32 spins = 0; !fresh(f, tag) && spins < (1u << 20); ++spins) {
    __builtin_amdgcn_s_sleep(1);
    aload_sc1(f, voff, srd, soff);
    vm_on<0>(f);
  }
}

#define STAMP(i) do {} while (0)
#define STAMP_END() do {} while (0)

#define ROT1 1
#define PAT 0
#define DPH 10
#define PRIO 1
#define QF 1
#define KD 18
#define ID 60

constexpr int piece_at(int j, int i) {
  if (j == 0) return i == 2 ? 0 : i == 5 ? 1 : i == 8 ? 2 : i == 11 ? 3 : i == 14 ? 4 : i == 16 ? 5 : i == 18 ? 6 : -1;
  if (j & 1) return i == 6 ? 0 : i == 11 ? 1 : i == 16 ? 2 : i == 19 ? 3 : -1;
  return i == 5 ? 4 : i == 14 ? 5 : i == 18 ? 6 : -1;
}
constexpr int wd_blk(int b) { return (int)(0x610875432ull >> (4 * (b % 9))) & 15; }
constexpr u32 wd_loc(int k) { return wd_blk(k / 2) * 16384 + (k & 1) * 1024; }

constexpr int gemm_wait(int k) {
  int n = k < DPH ? 2 * (DPH - 1 - k) : 0;
  for (int i = k < DPH ? 0 : k + 1 - DPH; i < k - 1; ++i) n += ((i & 1) && i < 10 ? 4 : 0) + (i + DPH < 28 ? 2 : 0);
  return n;
}
typedef u32 u32x16 __attribute__((ext_vector_type(16)));
DEV u32x4 mk_srd2(u32 lo, u32 hi, u32 bytes) { return (u32x4){lo, hi & 0xffffu, bytes, 0x00020000u}; }

__global__ __launch_bounds__(512, 2) NOPK void fused_block(const void*, const void*, const void*, const void*, const void*, const void*,
                                                           void*, char*) {
  __shared__ __attribute__((aligned(16))) char smem[LDS_BYTES];
  u32x16 ka;
  asm volatile("s_load_dwordx16 %0, %1, 0x0" : "=s"(ka) : "s"(__builtin_amdgcn_kernarg_segment_ptr()));
  const int tid = __builtin_amdgcn_workitem_id_x();
  const int w = __builtin_amdgcn_readfirstlane(tid >> 6);
  const int bid = __builtin_amdgcn_workgroup_id_x();
  const int xcd = bid & 7, bi = bid >> 3;
  const int rb = 4 * (xcd & 1) + (bi & 3), quad = xcd >> 1, cl = bi >> 2, cb = 8 * quad + cl;
  const bool t1 = cl == 7;
  const int mg = w & 3, g = w >> 2, un = 7 * quad + cl;
  u32 rot = ROT1 ? cl + 2 * (bi & 3) : 0;
  rot = rot >= 7 ? rot - 7 : rot;
  const int rr = (tid >> 3) & 7, ch = tid & 7, odd = (tid >> 5) & 2;
  u32 vx0 = rr * 896 + ((ch ^ (rr >> 1)) << 4), vx1 = (vx0 ^ 64) + 6144;
  u32 ox = (rb * 8 + w) * (32 * 896), lw = w * 1024, so0 = ox + rot * 128;
  asm volatile("s_waitcnt lgkmcnt(0)" : "+s"(ka), "+v"(vx0), "+v"(vx1), "+s"(so0), "+s"(lw));
  const u32x4 sX = mk_srd2(ka[0], ka[1], S_ * D_ * 2);
  dma2<0, 0>(sX, vx0, sX, vx1, so0, 4 * lw);
  dma2<0, 2048>(sX, vx0, sX, vx1, so0 + 12288, 4 * lw);
  SBAR();
  const u32 wst = t1 ? 2688 : 3584, cw = ch ^ ((rr & 2) << 1);
  const u32 cv = (tid & 15) ^ ((tid >> 2) & 12) ^ odd;
  const u32 va = t1 ? (rr >> 1) * 2688 + (cv >= 14 ? cv - 2 : cv) * 16 : rr * 3584 + (cw ^ odd) * 16;
  const u32 ow = 8 * w * wst + (t1 ? 1792 + 224 * quad : un * 128);
  const u32 vq = ((tid >> 2) & 7) * 2688 + ((tid & 3) ^ odd) * 16;
  const u32 oq = w * (8 * 2688) + ((56 * cb) & ~15);
  const u64 pb_ = (t1 ? ((u64)ka[3] << 32 | ka[2]) + 4 * 2688 : ((u64)ka[9] << 32 | ka[8])) - 1024;
  const u32x4 sA = mk_srd2(t1 ? ka[2] : ka[6], t1 ? ka[3] : ka[7], 1u << 21), sB = mk_srd2((u32)pb_, (u32)(pb_ >> 32), 1u << 21);
  const u32x4 sQ = mk_srd2(ka[2], ka[3], D_ * 3 * D_ * 2);
  u32 kst = rot, so = 0;
  auto piece = [&](auto H_, auto I_) LAM {
    constexpr int H = H_.value, i = I_.value;
    if constexpr (i == 0) {
      if constexpr (H > 0) { kst = opaque(kst) + 1; kst = kst == 7 ? 0 : kst; }
      so = ow + kst * (64 * wst);
    }
    if constexpr (i < 2) dma1<(6 + H % 3) * 16384, i * 1024>(i ? sB : sA, va, so, 2 * lw);
    if constexpr (i == 2) dma_half<L_QK + (H % 3) * 4096>(sQ, vq, oq + opaque(kst) * (64 * 2688), lw >> 1);
    if constexpr (i == 3) so = ox + opaque(kst) * 128;
    if constexpr (i == 5) so = opaque(so) + 12288;
    if constexpr (i >= 3) dma1<(H % 3) * 32768, (i - 3) * 1024>(sX, (i & 1) ? vx0 : vx1, so, 4 * lw);
  };
  sfor<0, 3>([&](auto i) LAM { piece(IC<0>{}, i); });
  asm volatile("s_barrier" ::: "memory");
  u32x4 xr;
  aload(xr, (tid & 63) < 56 ? (tid & 63) * 16 : 0x7ffffff0, sX, (rb * 256 + cb * 8 + w) * 896);
  u64 pc0;
  u32 clen;
  asm volatile("s_getpc_b64 %0\n.Lfb_pc0:\n\ts_max_u32 %1, .Lfb_enda-.Lfb_pc0, 0\n\ts_max_u32 %1, .Lfb_endb-.Lfb_pc0, %1" : "=s"(pc0), "=s"(clen)::"scc");
  {
    const u32 o = (bi * 16 + w * 2 + (tid & 1)) * 128;
    touch((const char*)pc0 + (o < clen ? o : clen));
  }
  sfor<0, 7>([&](auto i) LAM { piece(IC<1>{}, i); });
  SBAR();
  asm volatile("" ::"v"((u32)(uintptr_t)(LAS char*)smem));
  char* const ws = (char*)((u64)ka[15] << 32 | ka[14]);
  const char* const px = (const char*)((u64)ka[1] << 32 | ka[0]);
  const char* const pwo = (const char*)((u64)ka[5] << 32 | ka[4]);
  char* const pout = (char*)((u64)ka[13] << 32 | ka[12]);
  const u32x4 sD = mk_srd2(ka[10], ka[11], F_ * D_ * 2), sWS = mk_srd2(ka[14], ka[15], WS_END);
  const rsrc_t rWS = __builtin_amdgcn_make_buffer_rsrc(ws, 0, WS_END, 0x00020000);
  if (tid < 16) *(LAS u32*)(L_CNT + tid * 4) = 0;
  const bool has_tile = !t1;
  const int rq = bi / 7, rt = 4 * xcd + rq, nt = bi - 7 * rq;
  const int rotg = (4 * nt + 8 * rq) % 28;
  const int head = 2 * xcd + (bi >> 4), qb = bi & 15;
  const u32 vw0 = rr * 896 + cw * 16, vw1 = vw0 + 6144;
  auto krot = [&](u32 k) LAM -> u32 { k += opaque(rotg); return k >= 28 ? k - 28 : k; };
  const u32 od = 448 * mg * 896 + nt * 128;

  u64 epoch = 0, tag = 0;
  float rs[4];
  f32x4 acc[5][4];
  auto geglu = [&](auto M) LAM -> u32x4 {
    constexpr int m = M.value;
    const float r2 = rs[m] * rs[m], K1 = (-2.f * 0.7978845608028654f * 1.4426950408889634f) * rs[m], K2 = K1 * 0.044715f * r2, IR2 = rcp(r2);
    u32x4 hp;
    sfor<0, 4>([&](auto j) LAM {
      constexpr int p = 1 + (j.value & 2), e = 2 * (j.value & 1);
      float y[2];
      sfor<0, 2>([&](auto i) LAM {
        const float t = acc[p][m][e + i.value];
        y[i.value] = t * acc[p + 1][m][e + i.value] * rcp(__builtin_fmaf(ex2(t * __builtin_fmaf(t * t, K2, K1)), IR2, IR2));
      });
      hp[j.value] = cvtpk(y[0], y[1]);
    });
    return hp;
  };
  auto frag_off = [&]() LAM -> u32 { const u32 l = lane_id(); return (l >> 4) * 512 + (l & 15) * 16; };
  auto h_store = [&](u32x4 hp, int m) LAM {
    st128(rWS, hp, frag_off() + (m & 1) * 256, WS_H + ((rb * 8 + mg * 2 + (m >> 1)) * 112 + un * 4 + g * 2) * 1024);
  };

  auto phase1 = [&](auto T_) LAM {
    constexpr int T = T_.value;
    u32 xa[2][2], fa[4], qa;
    {
      const int lane = lane_id(), g4 = lane >> 4, tok = lane & 15, q4 = tok >> 2, p4 = lane & 3;
      xa[0][0] = (mg * 64 + tok) * 128 + ((g4 ^ (tok >> 1)) << 4);
      xa[0][1] = xa[0][0] ^ 64;
      xa[1][0] = xa[0][0] + 65536;
      xa[1][1] = xa[0][1] + 65536;
      sfor<0, 4>([&](auto t) LAM {
        if constexpr (T == 0) fa[t.value] = 6 * 16384 + g4 * 2048 + q4 * 128 + ((64 * g + 16 * (t.value & 2)) ^ ((q4 & 2) << 5) ^ ((g4 & 1) << 5)) + 8 * p4;
        else fa[t.value] = 6 * 16384 + g4 * 2048 + q4 * 256 + ((112 * g + 28 * (t.value & 2) + 32 * (t.value & 1) + 8 * p4) ^ (q4 * 64) ^ ((g4 & 1) << 5));
      });
      qa = L_QK + g4 * 512 + q4 * 64 + ((((cb & 1) * 8 + 32 * g + 8 * p4) & 63) ^ ((g4 & 1) << 5));
      PIN(xa[0][0]); PIN(xa[0][1]); PIN(xa[1][0]); PIN(xa[1][1]); PIN(fa[0]); PIN(fa[2]); PIN(qa);
      if constexpr (T == 1) { PIN(fa[1]); PIN(fa[3]); }
    }
    u32x4 one = {0x3f803f80u, 0x3f803f80u, 0x3f803f80u, 0x3f803f80u};
    PIN(one);
    u32x4 xf[2][4], wf[2][5], cf[5], sg;
    f32x4 cs[5] = {};
    sfor<0, 5>([&](auto t) LAM { PIN(cs[t.value]); });
    u32 nm[4];
    float rv[16], rl;
    u32 nl;

    epoch = sync_first<9>(ws);
    STAMP(1);
    auto rd = [&](auto J, auto R) LAM {
      constexpr int j = J.value, r = R.value, sl = (j / 2) % 3, s = j & 1;
      if constexpr (r >= 2 && r < 6) xf[j & 1][r - 2] = lds128(xa[sl >> 1][s] + (sl & 1) * 32768 + (r - 2) * 2048);
      else {
        constexpr int t = r < 2 ? 0 : (r - 4) / 2, h = r & 1;
        u32x2 v;
        if constexpr (t == 0) v = trd(qa + sl * 4096 + s * 2048 + h * 256);
        else if constexpr (T == 0) v = trd(fa[(t - 1) & 2] + sl * 16384 + s * 8192 + ((t - 1) & 1) * 1024 + h * 512);
        else v = trd(fa[t - 1] + sl * 16384 + s * 8192 + h * 1024);
        wf[j & 1][t][2 * h] = v[0];
        wf[j & 1][t][2 * h + 1] = v[1];
      }
    };
    auto row_stats = [&]() LAM {
      PIN(xr);
      float a[8];
      sfor<0, 4>([&](auto i) LAM { a[2 * i.value] = asf(xr[i.value] << 16); a[2 * i.value + 1] = asf(xr[i.value] & 0xffff0000u); });
      const float mean = wave_sum(((a[0] + a[1]) + (a[2] + a[3])) + ((a[4] + a[5]) + (a[6] + a[7]))) * (1.f / 448.f);
      float q = 0.f;
      sfor<0, 8>([&](auto i) LAM { const float d = a[i.value] - mean; q = __builtin_fmaf(d, d, q); });
      const int lane = lane_id();
      const float var = wave_sum(lane < 56 ? q : 0.f) * (1.f / 448.f);
      const u32 d = cvtpk(-mean, __builtin_amdgcn_sqrtf(bfround(bfround(var) + 1e-5f)));
      if (lane == 0) __builtin_amdgcn_raw_buffer_store_b128((u32x4){d, (u32)tag, d, (u32)(tag >> 32)}, rWS, 0, WS_ST + (rb * 256 + cb * 8 + w) * 16, 16);
    };
    sfor<0, 14>([&](auto r) LAM { rd(IC<0>{}, r); });
    SBAR();
    sfor<0, 14>([&](auto J) LAM {
      constexpr int j = J.value, G = j / 2;
      sfor<0, 20>([&](auto I) LAM {
        constexpr int i = I.value, t = i / 4, m = i % 4;
        const f32x4 c = j == 0 ? (f32x4){} : acc[t][m];
        if constexpr (T == 1 && t > 0) acc[t][m] = mfma16(xf[j & 1][m], wf[j & 1][t], c);
        else acc[t][m] = mfma16(wf[j & 1][t], xf[j & 1][m], c);
        PIN(acc[t][m]);
        SBAR();
        if constexpr ((j & 1) && i == 1 && j < 13) sync_stage<(G < 4 ? 7 : (G == 4 ? 8 : (T == 0 ? 4 : 0)))>();
        constexpr int r = i - 2 * (j & 1);
        if constexpr (j < 13 && r >= 0 && r < 14) rd(IC<j + 1>{}, IC<(r >= 0 && r < 14 ? r : 0)>{});
        if constexpr (i == 3) colsum5<j & 3>(cs, one, wf[j & 1], mg);
        constexpr int pc = piece_at(j, i), H = (j + 5) / 2;
        if constexpr (pc >= 0 && H < 7) piece(IC<H>{}, IC<(pc >= 0 ? pc : 0)>{});
        if constexpr (T == 0 && (j == 9 || j == 11) && pc >= 0) {
          if constexpr (pc == 0) so = od + krot(2 * (j - 9) + 2 * g) * 14336;
          if constexpr (pc == 2) so = opaque(so) + 12288;
          dma1<(j - 7) * 16384, pc * 1024>(sD, (pc & 1) ? vw1 : vw0, so, 4 * lw);
        }
        if constexpr (T == 0 && j == 12 && i == 16) { const int lane = lane_id(); touch(pwo + ((rq & 3) * 112 + w * 14 + (lane < 14 ? lane : 0)) * 896 + nt * 128); }
        if constexpr (j == 0 && i == 19) {
          tag = (epoch + 0x9E3779B97F4A7C15ull) * 0xBF58476D1CE4E5B9ull;
          tag = (tag ^ (tag >> 29)) + llvm_dispatch_id() * 0x94D049BB133111EBull;
          tag = (tag ^ (tag >> 31)) * 0xD6E8FEB86659FD93ull;
          asm volatile("" : "+s"(tag));
        }
        if constexpr ((j == 1 || j == 2) && i == 17) { if (g == j - 1) row_stats(); }
        if constexpr (j == 9 && i == 0) aload_sc1(sg, lane_id() * 16, sWS, WS_ST + (rb * 256 + mg * 64) * 16);
        if constexpr (j == 11 && i == 17) {
          PIN(sg);
          await(sg, lane_id() * 16, sWS, WS_ST + (rb * 256 + mg * 64) * 16, tag);
          rl = rcp(asf(sg[0] & 0xffff0000u));
          nl = __builtin_amdgcn_perm(sg[0], sg[0], 0x01000100u);
        }
        if constexpr (j == 12 && i >= 14 && i < 18) {
          const int src = 16 * (i - 14) + (lane_id() & 15);
          nm[i - 14] = from_lane(src, nl);
          rs[i - 14] = asf(from_lane(src, asu(rl)));
        }
        if constexpr (j == 13) {
          if constexpr (T == 1 && i < 4) {
            const int src = 16 * i + ((lane_id() >> 2) & 12);
            sfor<0, 4>([&](auto e) LAM { rv[4 * i + e.value] = asf(from_lane(src + e.value, asu(rl))); });
          }
          if constexpr (i >= 6 && i < 11) {
            constexpr int u = i - 6;
            const float v = cs[u][0], hi = bfround(v);
            *(LAS u32*)(L_CS + u * 256 + (g * 80 + (lane_id() & 15)) * 16 + mg * 4) = cvtpk(hi, v - hi);
          }
          if constexpr (i == 13) {
            sync_stage<63>();
            const u32 ca = L_CS + (g * 80 + (lane_id() & 15)) * 16;
            sfor<0, 5>([&](auto u) LAM { cf[u.value] = lds128(ca + u.value * 256); });
          }
          if constexpr (i >= 14 && i < 18) { nm[i - 14] = lane_id() < 16 ? nm[i - 14] : 0u; PIN(nm[i - 14]); }
        }
        SBAR();
      });
    });
    STAMP(2);
    sfor<0, 20>([&](auto I) LAM {
      constexpr int t = I.value / 4, m = I.value % 4;
      const u32x4 negm = {nm[m], nm[m], nm[m], nm[m]};
      if constexpr (T == 1 && t > 0) acc[t][m] = mfma16(negm, cf[t], acc[t][m]);
      else acc[t][m] = mfma16(cf[t], negm, acc[t][m]);
    });
    LANES();
    float km;
    {
      const bool isq = cb < 16, pad = g == 1 && lane >= 48;
      const u32 so = (isq ? WS_Q + ((cb & 15) * 64 + rb * 8 + mg * 2) * 2048 : WS_KV + ((cb & 15) * 64 + rb * 8 + mg * 2) * 4096) + g * 1024;
      const u32 vo = isq ? vl : (vl & 0x230u) | ((vl & 0xc0u) << 1) | ((vl & 0x100u) >> 2);
      u32 n2[2];
      sfor<0, 2>([&](auto U) LAM {
        constexpr int u = U.value;
        u32 P[2][2], n[2];
        sfor<0, 2>([&](auto i) LAM {
          const float sc = isq ? rs[2 * u + i.value] * (0.18898223650461362f * 1.4426950408889634f) : rs[2 * u + i.value];
          const f32x4 a = acc[0][2 * u + i.value];
          const float v0 = a[0] * sc, v1 = a[1] * sc, v2 = a[2] * sc, v3 = a[3] * sc;
          P[i.value][0] = pad ? 0x3f80u : cvtpk(v0, v1);
          P[i.value][1] = pad ? 0u : cvtpk(v2, v3);
          n[i.value] = pad ? 0u : asu(__builtin_fmaf(v0, v0, v1 * v1) + __builtin_fmaf(v2, v2, v3 * v3));
        });
        swap16(P[0][0], P[1][0]);
        swap16(P[0][1], P[1][1]);
        st128(rWS, (u32x4){P[0][0], P[0][1], P[1][0], P[1][1]}, vo, so + (isq ? 2048 : 4096) * u);
        swap16(n[0], n[1]);
        n2[u] = asu(asf(n[0]) + asf(n[1]));
      });
      swap32(n2[0], n2[1]);
      const float n = asf(n2[0]) + asf(n2[1]);
      if (isq) __builtin_amdgcn_raw_buffer_store_b32(asu(n), rWS, lane * 4, WS_QN + ((cb * 2 + g) * 2048 + rb * 256 + mg * 64) * 4, 16);
      km = wave_max_nonneg(n);
    }
    auto qk_flag = [&]() LAM {
      u32 n = 0;
      cfence();
      if (lane == 0) {
        __hip_atomic_fetch_max((LAS u32*)(L_CNT + 40 + 4 * g), asu(km), __ATOMIC_RELAXED, __HIP_MEMORY_SCOPE_WORKGROUP);
        n = __hip_atomic_fetch_add((LAS u32*)L_CNT, 1u, __ATOMIC_RELAXED, __HIP_MEMORY_SCOPE_WORKGROUP);
      }
      cfence();
      if (__builtin_amdgcn_readfirstlane(n) == 7) {
        const u32 k2 = asu(asf(cnt_get(10)) + asf(cnt_get(11)));
        const u32x4 f = {k2, (u32)tag, k2, (u32)(tag >> 32)};
        if (lane < (cb < 16 ? 2 : 16)) st128(rWS, f, lane * 512, WS_FQ + (cb < 16 ? (cb * 16 + 2 * rb) * 512 + 256 : (cb - 16) * 8192 + rb * 16));
        if constexpr (T == 1) st128(rWS, f, lane * 512, WS_FQ + quad * 32768 + 128 + rb * 16);
      }
      STAMP(13);
    };
    if constexpr (T == 1) {
      const bool oc = (lane & 15) == 12;
      const u32 fo = frag_off();
      sfor<0, 8>([&](auto I) LAM {
        constexpr int t = 1 + I.value / 2, u = I.value & 1;
        u32x4 P;
        sfor<0, 4>([&](auto e) LAM {
          constexpr int m = 2 * u + e.value / 2, c = 2 * (e.value & 1);
          P[e.value] = cvtpk(acc[t][m][c] * rv[4 * m + c], acc[t][m][c + 1] * rv[4 * m + c + 1]);
          if constexpr ((t & 1) == 0) P[e.value] = oc ? 0x3f803f80u : P[e.value];
        });
        st128(rWS, P, fo + ((t - 1) & 1) * 256, WS_KV + ((4 * quad + 2 * g + (t - 1) / 2) * 64 + rb * 8 + mg * 2 + u) * 4096 + 2048);
      });
      STAMP(3);
      vmwait<0>();
      qk_flag();
    } else {
      STAMP(3);
      SBAR();
      u32x4 hp = geglu(IC<0>{});
      PIN(hp);
      if constexpr (QF == 1) { vmwait<0>(); qk_flag(); }
      h_store(hp, 0);
      SBAR();
      hp = geglu(IC<1>{});
      PIN(hp);
      if constexpr (QF == 2) { vmwait<1>(); qk_flag(); }
      h_store(hp, 1);
    }
  };
  if (!t1) phase1(IC<0>{}); else phase1(IC<1>{});
  STAMP(4);
  LANES();
  auto h_done = [&]() LAM {
    if (has_tile && lane_id() == 0) put_flag(ws, WS_FH + ((rb * 4 + mg) * 64 + un * 2 + g) * 8, tag);
    cfence();
  };

  if (w < 4) {
    if (PRIO) __builtin_amdgcn_s_setprio(2);
    u32x4 fk;
    const u32 vfo = (lane < 17 ? lane : 0) * 16, sfo = WS_FQ + (head * 16 + qb) * 512;
    aload_sc1(fk, vfo, sWS, sfo);
    u32x4 hp2;
    if (has_tile) { hp2 = geglu(IC<2>{}); PIN(hp2); }
    vm_on<0>(fk);
    await(fk, vfo, sWS, sfo, tag);
    STAMP(5);
    const u32 kvb = WS_KV + (head * 64 + w * 16) * 4096;
    auto kvo = [&](int kt) LAM -> u32 { return kvb + ((kt + qb) & 15) * 4096; };
    u32x4 Q[4][2], KV[4][4];
    const float km = lane < 8 ? asf(fk[0]) : 0.f;
    u32 qn[4];
    sfor<0, 4>([&](auto t) LAM {
      const u32 so = WS_Q + (head * 64 + qb * 4 + ((w + 1 + t.value) & 3)) * 2048;
      qn[t.value] = __builtin_amdgcn_raw_buffer_load_b32(rWS, hf * 8192 + r32 * 4, WS_QN + (head * 4096 + qb * 128 + ((w + 1 + t.value) & 3) * 32) * 4, 16);
      Q[t.value][1] = ld128(rWS, vl + 1024, so);
      Q[t.value][0] = ld128(rWS, vl, so);
    });
    sfor<0, 3>([&](auto b) LAM { sfor<0, 4>([&](auto i) LAM { KV[b.value][i.value] = ld128(rWS, vl + i.value * 1024, kvo(b.value)); }); });
    SBAR();
    cnt_add(3);
    if (has_tile) { h_store(hp2, 2); h_store(geglu(IC<3>{}), 3); }
    SBAR();
    STAMP(6);
    const float k2 = wave_max_nonneg(km) * 1.03f;
    bool safe = true;
    sfor<0, 4>([&](auto t) LAM {
      u32 qo = qn[t.value];
      swap32(qn[t.value], qo);
      const float m = __builtin_amdgcn_sqrtf((asf(qn[t.value]) + asf(qo)) * k2);
      safe = safe && m <= 48.f;
      Q[t.value][1][2] = hf ? cvtpk(-m, 0.f) : Q[t.value][1][2];
    });
    const int idx = 0xDDDD;
    f32x16 O[4] = {};
    const f32x16 Z = {};
    auto rgn = [&](int t) LAM -> u32 { return ((0x6108u >> (4 * t)) & 15) * 16384u; };
    auto put_part = [&](auto t, auto i) LAM {
      const f32x16& o = O[t.value];
      *(LAS f32x4*)(rgn((w + 1 + t.value) & 3) + t.value * 4096 + i.value * 1024 + vl) =
          (f32x4){o[4 * i.value], o[4 * i.value + 1], o[4 * i.value + 2], o[4 * i.value + 3]};
    };
    if (__builtin_amdgcn_ballot_w64(!safe) == 0) {
      f32x16 Sx[4];
      u32 cb1 = 0;
      Sx[0] = mfma32(KV[0][1], Q[0][1], mfma32(KV[0][0], Q[0][0], Z));
      SBAR();
      sfor<0, 64>([&](auto I) LAM {
        constexpr int kt = I.value >> 2, b = kt & 3, t = I.value & 3, tn = (t + 1) & 3, tp = (t + 3) & 3;
        constexpr int bn = t == 3 ? (b + 1) & 3 : b, bp = t == 0 ? (b + 3) & 3 : b;
        constexpr int E1 = PAT ? 3 : 4, E2 = 2 * E1, E3 = 3 * E1;
        auto exps = [&](auto lo, auto hi) LAM {
          SBAR();
          sfor<lo.value, hi.value>([&](auto e) LAM { Sx[t][e.value] = ex2(Sx[t][e.value]); });
          PIN(Sx[t]);
          SBAR();
        };
        if constexpr (I.value < 63) Sx[tn] = mfma32(KV[bn][0], Q[tn][0], Z);
        exps(IC<0>{}, IC<E1>{});
        if constexpr (I.value < 63) Sx[tn] = mfma32(KV[bn][1], Q[tn][1], Sx[tn]);
        exps(IC<E1>{}, IC<E2>{});
        if constexpr (I.value > 0) O[tp] = smf_hi(KV[bp][3], Sx[tp], O[tp], idx);
        exps(IC<E2>{}, IC<E3>{});
        O[t] = smf_lo(KV[b][2], Sx[t], O[t], idx);
        exps(IC<E3>{}, IC<E3 + 2>{});
        if constexpr (kt + 3 < 16) KV[(b + 3) & 3][t] = ld128(rWS, vl + t * 1024, kvo(kt + 3));
        if constexpr (I.value == 8) { vmwait<8>(); h_done(); }
        if constexpr (I.value == ID) cnt_add(9);
        if constexpr (I.value == 58) cb1 = cnt_get(1);
        if constexpr (I.value == 60) { if (has_tile && __builtin_amdgcn_readfirstlane(cb1) < 4u) cnt_wait(1, 4); cfence(); }
        if constexpr (I.value >= 62) sfor<0, 4>([&](auto i) LAM { put_part(IC<I.value - 62>{}, i); });
        exps(IC<E3 + 2>{}, IC<16>{});
      });
      O[3] = smf_hi(KV[3][3], Sx[3], O[3], idx);
    } else {
      vmwait<0>();
      h_done();
      sfor<0, 4>([&](auto t) LAM { Q[t.value][1][2] = hf ? 0u : Q[t.value][1][2]; });
      float mx[4] = {-3.0e38f, -3.0e38f, -3.0e38f, -3.0e38f};
      for (int kt = 0; kt < 16; ++kt) {
        const u32x4 k0 = ld128(rWS, vl, kvb + kt * 4096), k1 = ld128(rWS, vl + 1024, kvb + kt * 4096);
        sfor<0, 4>([&](auto t) LAM {
          const f32x16 s = mfma32(k1, Q[t.value][1], mfma32(k0, Q[t.value][0], Z));
          sfor<0, 16>([&](auto i) LAM { mx[t.value] = __builtin_fmaxf(mx[t.value], s[i.value]); });
        });
      }
      sfor<0, 4>([&](auto t) LAM {
        mx[t.value] = __builtin_fmaxf(mx[t.value], other_half(mx[t.value]));
        *(LAS float*)(L_VT + ((((w + 1 + t.value) & 3) * 32 + r32) * 4 + w) * 4) = mx[t.value];
      });
      cnt_add(8);
      cnt_wait(8, 4);
      sfor<0, 4>([&](auto t) LAM {
        const f32x4 m4 = lds128f(L_VT + (((w + 1 + t.value) & 3) * 32 + r32) * 16);
        mx[t.value] = __builtin_fmaxf(__builtin_fmaxf(m4[0], m4[1]), __builtin_fmaxf(m4[2], m4[3]));
      });
      for (int kt = 0; kt < 16; ++kt) {
        const u32x4 k0 = ld128(rWS, vl, kvb + kt * 4096), k1 = ld128(rWS, vl + 1024, kvb + kt * 4096);
        const u32x4 v0 = ld128(rWS, vl + 2048, kvb + kt * 4096), v1 = ld128(rWS, vl + 3072, kvb + kt * 4096);
        sfor<0, 4>([&](auto t) LAM {
          f32x16 s = mfma32(k1, Q[t.value][1], mfma32(k0, Q[t.value][0], Z));
          sfor<0, 16>([&](auto i) LAM { s[i.value] = ex2(s[i.value] - mx[t.value]); });
          O[t.value] = smf_hi(v1, s, smf_lo(v0, s, O[t.value], idx), idx);
        });
      }
      cnt_add(9);
      if (has_tile) cnt_wait(1, 4);
      sfor<0, 2>([&](auto t) LAM { sfor<0, 4>([&](auto i) LAM { put_part(t, i); }); });
    }
    STAMP(7);
    sfor<0, 4>([&](auto i) LAM { put_part(IC<2>{}, i); });
    cnt_add(2);
    cnt_wait(2, 4);
    const u32 red = rgn(w);
    {
      LANES();
      float o[16];
      sfor<0, 4>([&](auto i) LAM {
        const u32 ra = red + i.value * 1024 + vl;
        const f32x4 a0 = lds128f(ra), a1 = lds128f(ra + 4096), a2 = lds128f(ra + 8192);
        sfor<0, 4>([&](auto j) LAM { o[4 * i.value + j.value] = (a0[j.value] + a1[j.value]) + (a2[j.value] + O[3][4 * i.value + j.value]); });
      });
      const float lo_ = other_half(o[12]);
      const float il = rcp(hf ? o[12] : lo_);
      u32 P[8];
      sfor<0, 8>([&](auto i) LAM { P[i.value] = cvtpk(o[2 * i.value] * il, o[2 * i.value + 1] * il); });
      const int om = qb * 4 + w;
      st128(rWS, (u32x4){P[0], P[1], P[2], P[3]}, vl, WS_O + (om * 16 + head) * 2048);
      st128(rWS, (u32x4){P[4], P[5], hf ? 0u : P[6], hf ? 0u : P[7]}, vl + 1024, WS_O + (om * 16 + head) * 2048);
      if (has_tile) {
        const u32x4 sO = mk_srd(pwo, D_ * D_ * 2);
        const u32 vw = (lane >> 3) * 896 + (((lane & 7) ^ ((lane >> 2) & 4)) << 4);
        sfor<0, 14>([&](auto e) LAM { dma_i<e.value * 1024, 8 * e.value * 896>(sO, vw, 112 * w * 896 + nt * 128, red); });
        vmwait<14>();
      } else vmwait<0>();
      if (lane == 0) put_flag(ws, WS_FO + (om * 16 + head) * 8, tag);
    }
    STAMP(8);
    if (!has_tile) { STAMP_END(); return; }
    {
    LANES();
    const u32 trow = (4 * hf + q4) * 128, swz = (q4 & 2) << 5;
    const int m = w & 1, kh = w >> 1;
    const rsrc_t rX = __builtin_amdgcn_make_buffer_rsrc((void*)px, 0, S_ * D_ * 2, 0x00020000);
    const u32 xo = ((rt * 64 + m * 32 + r32) * 448 + nt * 64 + kh * 32 + hf * 8) * 2;
    u32x4 xr[2];
    xr[0] = __builtin_amdgcn_raw_buffer_load_b128(rX, xo, 0, 0);
    xr[1] = __builtin_amdgcn_raw_buffer_load_b128(rX, xo + 32, 0, 0);
    PIN(xr[0]); PIN(xr[1]);
    vmwait<0>();
    cnt_add(6);
    poll_flags<2>(ws, WS_FO + ((2 * rt + m) * 16 + 8 * kh + (lane & 7)) * 8, tag);
    STAMP(9);
    u32x4 of[16];
    sfor<0, 16>([&](auto k) LAM { of[k.value] = ld128(rWS, vl + (k.value & 3) * 1024, WS_O + ((2 * rt + m) * 16 + 8 * kh) * 2048 + (k.value >> 2) * 4096); });
    SBAR();
    cnt_wait(6, 4);
    cnt_wait(5, 4);
    f32x4 gt[4];
    sfor<0, 4>([&](auto i) LAM { gt[i.value] = lds128f(5 * 16384 + w * 4096 + i.value * 1024 + vl); });
    u32 xs[8] = {xr[0][0], xr[0][1], xr[0][2], xr[0][3], xr[1][0], xr[1][1], xr[1][2], xr[1][3]};
    swap32(xs[0], xs[2]); swap32(xs[1], xs[3]); swap32(xs[4], xs[6]); swap32(xs[5], xs[7]);
    f32x16 acc[2] = {};
    sfor<0, 2>([&](auto r) LAM {
      const u32 rg = rgn(2 * kh + r.value);
      u32 wa[2], wp[2];
      sfor<0, 2>([&](auto n) LAM {
        wa[n.value] = rg + trow + ((64 * (n.value ^ kh) + 32 * blk + 8 * p4) ^ swz);
        wp[n.value] = wa[n.value] - hf * 512;
      });
      sfor<0, 8>([&](auto k) LAM {
        constexpr int hh = k.value >> 1, s = k.value & 1;
        constexpr u32 off = (28 * hh + 16 * s) * 128;
        sfor<0, 2>([&](auto n) LAM {
          constexpr int nn = 1 - n.value;
          const u32x4 wf = trd2(wa[nn] + off, (s ? wp[nn] : wa[nn]) + off + 1024);
          acc[nn] = mfma32(wf, of[8 * r.value + k.value], acc[nn]);
        });
      });
    });
    sfor<0, 4>([&](auto i) LAM {
      *(LAS f32x4*)(7 * 16384 + w * 4096 + i.value * 1024 + vl) =
          (f32x4){acc[1][4 * i.value], acc[1][4 * i.value + 1], acc[1][4 * i.value + 2], acc[1][4 * i.value + 3]};
    });
    STAMP(10);
    cnt_add(7);
    cnt_wait(7, 4);
    u32 R[8];
    sfor<0, 4>([&](auto i) LAM {
      const f32x4 a = lds128f(7 * 16384 + (w ^ 2) * 4096 + i.value * 1024 + vl);
      float y[4];
      sfor<0, 4>([&](auto j) LAM {
        const u32 xd = xs[2 * i.value + (j.value >> 1)];
        const float xv = asf((j.value & 1) ? (xd & 0xffff0000u) : (xd << 16));
        y[j.value] = __builtin_fmaf(acc[0][4 * i.value + j.value] + a[j.value], gt[i.value][j.value], xv);
      });
      R[2 * i.value] = cvtpk(y[0], y[1]);
      R[2 * i.value + 1] = cvtpk(y[2], y[3]);
    });
    swap32(R[0], R[2]); swap32(R[1], R[3]); swap32(R[4], R[6]); swap32(R[5], R[7]);
    const rsrc_t rOut = __builtin_amdgcn_make_buffer_rsrc(pout, 0, S_ * D_ * 2, 0x00020000);
    st128p(rOut, (u32x4){R[0], R[1], R[2], R[3]}, xo);
    st128p(rOut, (u32x4){R[4], R[5], R[6], R[7]}, xo + 32);
    asm volatile(".Lfb_enda:");
    STAMP(11);
    STAMP_END();
    }
  } else {
    const u32 trow = (4 * hf + q4) * 128, swz = (q4 & 2) << 5;
    const int gw = w - 4;
    const u32 lg = gw * 4096;
    auto wload = [&](auto B) LAM {
      const u32 so = od + krot(2 * B.value) * 14336;
      dma2<wd_loc(2 * B.value), 0>(sD, vw0, sD, vw1, so, lg);
      dma2<wd_loc(2 * B.value), 2048>(sD, vw0, sD, vw1, so + 12288, lg);
    };
    if (has_tile) {
      sfor<4, 9>([&](auto B) LAM { wload(B); });
      h_store(geglu(IC<2>{}), 2);
      h_store(geglu(IC<3>{}), 3);
      vmwait<0>();
      h_done();
    }
    STAMP(5);
    if (bid == 255 && gw == 0) {
      for (u32 i = 0; i < 4; ++i) {
        u32x4 f = {0, 0, 0, (u32)~(tag >> 32)};
        await(f, lane * 128, sWS, WS_ST + i * 8192, tag);
      }
      if (lane == 0) put_flag(ws, WS_EPOCH, epoch + 1);
    }
    if (!has_tile) { STAMP_END(); return; }
    const u32x4 sH = mk_srd(ws + WS_H, 64 * 112 * 1024);
    const u32 oh0 = ((2 * rt + (gw & 1)) * 112 + 28 * gw) * 1024, oh1 = ((2 * rt + 1 - (gw & 1)) * 112 + 28 * gw) * 1024;
    u32x4 hfr[DPH][2];
    auto hload = [&](auto K) LAM {
      aload_sc1(hfr[K.value % DPH][0], vl, sH, oh0 + krot(K.value) * 1024);
      aload_sc1(hfr[K.value % DPH][1], vl, sH, oh1 + krot(K.value) * 1024);
    };
    cnt_wait(0, 8);
    cnt_wait(3, 4);
    poll_flags<8>(ws, WS_FH + (rt * 64 + 14 * gw + (lane < 14 ? lane : 0)) * 8, tag);
    STAMP(6);
    sfor<0, DPH>([&](auto K) LAM { hload(K); });
    u32 wa[2];
    sfor<0, 2>([&](auto n) LAM { wa[n.value] = lg + trow + ((64 * (n.value ^ (gw >> 1)) + 32 * blk + 8 * p4) ^ swz); });
    f32x16 acc[2][2] = {};
    u32x4 wf[2][2];
    auto ready = [&](auto K, auto N) LAM {
      constexpr int k = K.value;
      constexpr int o = wd_loc(k), d = (o & ~65535) - (k ? (int)(wd_loc(k - 1) & ~65535) : 0);
      vm_on2<N.value>(hfr[k % DPH][0], hfr[k % DPH][1]);
      sfor<0, 2>([&](auto n) LAM {
        if constexpr (d != 0) { wa[n.value] += d; PIN(wa[n.value]); }
        wf[k & 1][n.value] = trd2(wa[n.value] + (o & 65535), wa[n.value] + (o & 65535) + 2048);
      });
    };
    ready(IC<0>{}, IC<2 * (DPH - 1)>{});
    sfor<0, 28>([&](auto K) LAM {
      constexpr int k = K.value;
      if constexpr (k == KD) cnt_wait(9, 4);
      if constexpr (k + 1 < 28) ready(IC<k + 1>{}, IC<gemm_wait(k + 1)>{});
      sfor<0, 2>([&](auto n) LAM {
        sfor<0, 2>([&](auto m) LAM { acc[m.value][n.value] = mfma32(wf[k & 1][n.value], hfr[k % DPH][m.value], acc[m.value][n.value]); });
      });
      SBAR();
      if constexpr (k == 17) cnt_add(1);
      if constexpr ((k & 1) && k < 10) wload(IC<(k + 17) / 2>{});
      if constexpr (k + DPH < 28) hload(IC<k + DPH>{});
      SBAR();
    });
    STAMP(7);
    sfor<1, 4>([&](auto x) LAM {
      const f32x16& a = acc[x.value & 1][x.value >> 1];
      sfor<0, 4>([&](auto i) LAM {
        *(LAS f32x4*)(IC<wd_blk(x.value - 1)>::value * 16384 + lg + i.value * 1024 + vl) = (f32x4){a[4 * i.value], a[4 * i.value + 1], a[4 * i.value + 2], a[4 * i.value + 3]};
      });
    });
    cnt_add(4);
    cnt_wait(4, 4);
    sfor<0, 4>([&](auto i) LAM {
      const u32 ra = i.value * 1024 + vl;
      const f32x4 a1 = lds128f(2 * 16384 + (gw ^ 1) * 4096 + ra), a2 = lds128f(3 * 16384 + (gw ^ 2) * 4096 + ra), a3_ = lds128f(4 * 16384 + (gw ^ 3) * 4096 + ra);
      f32x4 gv;
      sfor<0, 4>([&](auto j) LAM {
        const float f = (acc[0][0][4 * i.value + j.value] + a1[j.value]) + (a2[j.value] + a3_[j.value]);
        gv[j.value] = f * rcp(1.f + ex2(f * -1.4426950408889634f));
      });
      *(LAS f32x4*)(5 * 16384 + lg + ra) = gv;
    });
    asm volatile(".Lfb_endb:");
    cnt_add(5);
    STAMP(10);
    STAMP_END();
  }
}

extern "C" void kernel_launch(void* const* d_in, const int* in_sizes, int n_in, void* d_out, int out_size, void* d_ws,
                              size_t ws_size, hipStream_t stream) {
  fused_block<<<256, 512, 0, stream>>>(d_in[0], d_in[1], d_in[2], d_in[4], d_in[5], d_in[6], d_out, (char*)d_ws);
}
```

```cpp
#include <hip/hip_runtime.h>
#include <stdint.h>
#pragma clang diagnostic ignored "-Wint-to-pointer-cast"

#if defined(__HIP_DEVICE_COMPILE__)
#define NOPK __attribute__((target("no-packed-fp32-ops")))
#else
#define NOPK
#endif
#define DEV __device__ __forceinline__ NOPK
#define LAM __attribute__((always_inline)) NOPK
#define LAS __attribute__((address_space(3)))
#define GAS __attribute__((address_space(1)))

#define STAMPS 0

typedef unsigned u32;
typedef unsigned long long u64;
typedef u32 u32x2 __attribute__((ext_vector_type(2)));
typedef u32 u32x4 __attribute__((ext_vector_type(4)));
typedef short s16x4 __attribute__((ext_vector_type(4)));
typedef float f32x2 __attribute__((ext_vector_type(2)));
typedef float f32x4 __attribute__((ext_vector_type(4)));
typedef float f32x8 __attribute__((ext_vector_type(8)));
typedef float f32x16 __attribute__((ext_vector_type(16)));
typedef __bf16 bf16x8 __attribute__((ext_vector_type(8)));
typedef __bf16 bf16x16 __attribute__((ext_vector_type(16)));

extern "C" __device__ u64 llvm_dispatch_id() __asm("llvm.amdgcn.dispatch.id");

constexpr int S_ = 2048, D_ = 448, F_ = 1792;
#define V3 1
#define V4 1
constexpr u32 WS_EPOCH = 0, WS_FO = 4096, WS_FH = 16384, WS_ST = 32768, WS_DBG = 65536;
constexpr u32 WS_Q = 1u << 20, WS_KV = 3u << 20, WS_O = 7u << 20, WS_H = 9u << 20;
constexpr u32 WS_FQ = 16u << 20, WS_QN = WS_FQ + (256u << 10), WS_END = 17u << 20;
constexpr u32 L_QK = 147456, L_CS = 159744, L_CNT = L_CS + 2560, L_DUM = L_CS + 2816, L_RV = L_CS + 3072;
constexpr u32 OOB = 0x7ffffff0;
constexpr u32 L_VT = L_QK;
constexpr u32 LDS_BYTES = 163840;

template <int V> struct IC { static constexpr int value = V; };
template <int I, int N, class Fn> DEV void sfor(Fn&& f) {
  if constexpr (I < N) { f(IC<I>{}); sfor<I + 1, N>(f); }
}

#define SBAR() __builtin_amdgcn_sched_barrier(0)
template <class T> DEV void PIN(T& x) { asm volatile("" : "+v"(x)); }
template <class T> DEV void nop_on(T& x) { asm volatile("s_nop 7" : "+v"(x)); }
template <class T> DEV void lgkm0_on(T& x) { asm volatile("s_waitcnt lgkmcnt(0)" : "+v"(x)::"memory"); }
template <int K> DEV void colsum5(f32x4* c, u32x4 one, const u32x4* w, int who) {
  asm volatile("s_cmp_lg_u32 %11, %12\n\ts_cbranch_scc1 .Lcs%=\n\tv_mfma_f32_16x16x32_bf16 %0, %5, %6, %0\n\tv_mfma_f32_16x16x32_bf16 %1, %5, %7, %1\n\t"
               "v_mfma_f32_16x16x32_bf16 %2, %5, %8, %2\n\tv_mfma_f32_16x16x32_bf16 %3, %5, %9, %3\n\tv_mfma_f32_16x16x32_bf16 %4, %5, %10, %4\n.Lcs%=:"
               : "+v"(c[0]), "+v"(c[1]), "+v"(c[2]), "+v"(c[3]), "+v"(c[4])
               : "v"(one), "v"(w[0]), "v"(w[1]), "v"(w[2]), "v"(w[3]), "v"(w[4]), "s"(who), "i"(K) : "scc");
}
template <int OFF> DEV void aload(u32x4& d, u32 voff, u32x4 srd, u32 soff) {
  asm volatile("buffer_load_dwordx4 %0, %1, %2, %3 offen offset:%4" : "=v"(d) : "v"(voff), "s"(srd), "s"(soff), "i"(OFF) : "memory");
}
template <int N> DEV void vm_on(u32x4& a) { asm volatile("s_waitcnt vmcnt(%1)" : "+v"(a) : "i"(N) : "memory"); }
DEV void aload_sc1(u32x4& d, u32 voff, u32x4 srd, u32 soff) {
  asm volatile("buffer_load_dwordx4 %0, %1, %2, %3 offen sc1" : "=v"(d) : "v"(voff), "s"(srd), "s"(soff) : "memory");
}
template <int N> DEV void vm_on2(u32x4& a, u32x4& b) { asm volatile("s_waitcnt vmcnt(%2)" : "+v"(a), "+v"(b) : "i"(N) : "memory"); }

DEV f32x16 mfma32(u32x4 a, u32x4 b, f32x16 c) {
  return __builtin_amdgcn_mfma_f32_32x32x16_bf16(__builtin_bit_cast(bf16x8, a), __builtin_bit_cast(bf16x8, b), c, 0, 0, 0);
}
DEV f32x4 mfma16(u32x4 a, u32x4 b, f32x4 c) {
  return __builtin_amdgcn_mfma_f32_16x16x32_bf16(__builtin_bit_cast(bf16x8, a), __builtin_bit_cast(bf16x8, b), c, 0, 0, 0);
}
DEV f32x4 mfma16h(u32x2 a, u32x2 b, f32x4 c) {
  return __builtin_amdgcn_mfma_f32_16x16x16bf16_1k(__builtin_bit_cast(s16x4, a), __builtin_bit_cast(s16x4, b), c, 0, 0, 0);
}
DEV f32x16 smf_lo(u32x4 a, f32x16 p, f32x16 c, int idx) {
  f32x8 b = __builtin_shufflevector(p, p, 0, 1, 2, 3, 4, 5, 6, 7);
  return __builtin_amdgcn_smfmac_f32_32x32x32_bf16(__builtin_bit_cast(bf16x8, a), __builtin_bit_cast(bf16x16, b), c, idx, 0, 0);
}
DEV f32x16 smf_hi(u32x4 a, f32x16 p, f32x16 c, int idx) {
  f32x8 b = __builtin_shufflevector(p, p, 8, 9, 10, 11, 12, 13, 14, 15);
  return __builtin_amdgcn_smfmac_f32_32x32x32_bf16(__builtin_bit_cast(bf16x8, a), __builtin_bit_cast(bf16x16, b), c, idx, 0, 0);
}
typedef __bf16 bf16x2 __attribute__((ext_vector_type(2)));
DEV u32 cvtpk(float lo, float hi) {
  f32x2 v = {lo, hi};
  return __builtin_bit_cast(u32, __builtin_convertvector(v, bf16x2));
}
DEV float asf(u32 u) { return __builtin_bit_cast(float, u); }
DEV u32 asu(float f) { return __builtin_bit_cast(u32, f); }
DEV float bfround(float f) { return asf(cvtpk(f, f) << 16); }
DEV float ex2(float x) { return __builtin_amdgcn_exp2f(x); }
DEV float rcp(float x) { return __builtin_amdgcn_rcpf(x); }
DEV float other_half(float v) {
  auto r = __builtin_amdgcn_permlane32_swap(asu(v), asu(v), false, false);
  return asf(r[0] ^ r[1] ^ asu(v));
}
DEV u32x4 mk_srd(const void* p, u32 bytes) {
  u64 a = (u64)p;
  u32x4 r = {(u32)a, (u32)(a >> 32) & 0xffffu, bytes, 0x00020000u};
  return r;
}
template <int LD> DEV void dma_half(u32x4 srd, u32 voff, u32 so, u32 l0) {
#define EXH "0"
  asm volatile("s_add_u32 m0, %0, %1\n\ts_mov_b32 exec_hi, " EXH "\n\tbuffer_load_dwordx4 %2, %3, %4 offen lds\n\ts_mov_b32 exec_hi, -1"
               ::"s"(l0), "i"(LD), "v"(voff), "s"(srd), "s"(so) : "memory", "scc");
}
template <int LD, int OFF> DEV void dma1(u32x4 srd, u32 voff, u32 so, u32 l0) {
  asm volatile("s_add_u32 m0, %0, %1\n\ts_nop 0\n\tbuffer_load_dwordx4 %2, %3, %4 offen offset:%5 lds"
               ::"s"(l0), "i"(LD), "v"(voff), "s"(srd), "s"(so), "i"(OFF) : "memory", "scc");
}
template <int LD, int OFF> DEV void dma2(u32x4 s0, u32 v0, u32x4 s1, u32 v1, u32 so, u32 l0) {
  asm volatile("s_add_u32 m0, %0, %1\n\ts_nop 0\n\tbuffer_load_dwordx4 %2, %3, %6 offen offset:%7 lds\n\tbuffer_load_dwordx4 %4, %5, %6 offen offset:%8 lds"
               ::"s"(l0), "i"(LD), "v"(v0), "s"(s0), "v"(v1), "s"(s1), "s"(so), "i"(OFF), "i"(OFF + 1024) : "memory", "scc");
}
DEV u32 opaque(u32 x) { asm volatile("" : "+s"(x)); return x; }
template <int LD, int ADD> DEV void dma_i(u32x4 srd, u32 voff, u32 s0, u32 l0) {
  u32 t;
  asm volatile("s_add_u32 m0, %1, %2\n\ts_add_u32 %0, %3, %4\n\tbuffer_load_dwordx4 %5, %6, %0 offen lds"
               : "=&s"(t) : "s"(l0), "i"(LD), "s"(s0), "i"(ADD), "v"(voff), "s"(srd) : "memory", "scc");
}
DEV int lane_id() {
  u32 z = 0;
  asm volatile("" : "+s"(z));
  return __builtin_amdgcn_mbcnt_hi(~0u, __builtin_amdgcn_mbcnt_lo(~0u, z));
}
#define LANES() const int lane = lane_id(), r32 = lane & 31, hf = lane >> 5, blk = (lane >> 4) & 1, q4 = (lane & 15) >> 2, p4 = lane & 3; \
  const u32 vl = lane * 16; (void)r32; (void)hf; (void)blk; (void)q4; (void)p4; (void)vl
DEV void touch(const void* p) {
  asm volatile("s_mov_b32 m0, %0\n\ts_nop 0\n\tglobal_load_lds_dword %1, off" ::"i"(L_DUM), "v"(p) : "memory");
}
template <int N> DEV void vmwait() { asm volatile("s_waitcnt vmcnt(%0)" ::"i"(N) : "memory"); }
DEV void cfence() { asm volatile("" ::: "memory"); }
DEV void cnt_add(int i) {
  asm volatile("s_mov_b64 exec, 1\n\tds_inc_u32 %0, %0\n\ts_mov_b64 exec, -1" ::"v"(L_CNT + 4 * i) : "memory");
}
DEV u32 cnt_get(int i) { return __hip_atomic_load((LAS u32*)(L_CNT + 4 * i), __ATOMIC_RELAXED, __HIP_MEMORY_SCOPE_WORKGROUP); }
DEV void cnt_wait(int i, u32 n) {
  while (__builtin_amdgcn_readfirstlane(cnt_get(i)) < n) __builtin_amdgcn_s_sleep(1);
  cfence();
}
DEV u32x2 trd(u32 a) { return __builtin_bit_cast(u32x2, __builtin_amdgcn_ds_read_tr16_b64_v4i16((LAS s16x4*)a)); }
DEV u32x4 trd2(u32 a0, u32 a1) { u32x2 x = trd(a0), y = trd(a1); u32x4 r = {x[0], x[1], y[0], y[1]}; return r; }
DEV u32x4 lds128(u32 a) { return *(LAS u32x4*)a; }
DEV f32x4 lds128f(u32 a) { return *(LAS f32x4*)a; }
template <int N> DEV void sync_stage() { asm volatile("s_waitcnt vmcnt(%0) lgkmcnt(0)\n\ts_barrier" ::"i"(N) : "memory"); }
template <int I> DEV void wlane(u32& f, u32 v) { asm volatile("v_writelane_b32 %0, %1, %2" : "+v"(f) : "s"(v), "i"(I)); }
typedef __amdgpu_buffer_rsrc_t rsrc_t;
DEV void st128(rsrc_t r, u32x4 v, u32 voff, u32 soff) {
  __builtin_amdgcn_raw_buffer_store_b128(v, r, voff, __builtin_amdgcn_readfirstlane(soff), 16);
  asm volatile("s_nop 1" ::"v"(v) : "memory");
}
DEV void st128p(rsrc_t r, u32x4 v, u32 voff) {
  __builtin_amdgcn_raw_buffer_store_b128(v, r, voff, 0, 16);
}
DEV void aflag(u64& d, const char* p) { asm volatile("global_load_dwordx2 %0, %1, off sc1" : "=v"(d) : "v"(p) : "memory"); }
template <int N> DEV void vm_on1(u64& a) { asm volatile("s_waitcnt vmcnt(%1)" : "+v"(a) : "i"(N) : "memory"); }
DEV void swap32(u32& a, u32& b) {
  auto r = __builtin_amdgcn_permlane32_swap(a, b, false, false);
  a = r[0]; b = r[1];
}
DEV void swap16(u32& a, u32& b) {
  auto r = __builtin_amdgcn_permlane16_swap(a, b, false, false);
  a = r[0]; b = r[1];
}
DEV u32 from_lane(int l, u32 v) { return __builtin_amdgcn_ds_bpermute(l * 4, v); }
DEV u32x4 ld128(rsrc_t r, u32 voff, u32 soff) { return __builtin_amdgcn_raw_buffer_load_b128(r, voff, __builtin_amdgcn_readfirstlane(soff), 16); }
DEV void put_flag(char* ws, u32 off, u64 tag) { __hip_atomic_store((GAS u64*)(ws + off), tag, __ATOMIC_RELAXED, __HIP_MEMORY_SCOPE_AGENT); }
template <int NAP> DEV void poll_flags(char* ws, u32 off, u64 tag) {
#pragma nounroll
  for (u32 spins = 0; spins < (1u << 20); ++spins) {
    u64 v = __hip_atomic_load((GAS u64*)(ws + off), __ATOMIC_RELAXED, __HIP_MEMORY_SCOPE_AGENT);
    if (__builtin_amdgcn_ballot_w64(v != tag) == 0) break;
    __builtin_amdgcn_s_sleep(NAP);
  }
  __builtin_amdgcn_fence(__ATOMIC_ACQUIRE, "wavefront");
  asm volatile("" ::: "memory");
}
DEV float wave_max_nonneg(float v) {
  u32 x = asu(v);
#define DPPMAX(ctrl) { u32 y = __builtin_amdgcn_update_dpp(0u, x, ctrl, 0xf, 0xf, true); x = x > y ? x : y; }
  DPPMAX(0x111) DPPMAX(0x112) DPPMAX(0x114) DPPMAX(0x118) DPPMAX(0x142) DPPMAX(0x143)
  return asf(__builtin_amdgcn_readlane(x, 63));
}
DEV float max8_nonneg(float v) {
  u32 x = asu(v);
  DPPMAX(0x111) DPPMAX(0x112) DPPMAX(0x114)
#undef DPPMAX
  return asf(__builtin_amdgcn_readlane(x, 7));
}

DEV void widen(float& lo, float& hi, u32 x) {
  asm volatile("v_lshlrev_b32 %0, 16, %2\n\tv_and_b32 %1, 0xffff0000, %2" : "=&v"(lo), "=v"(hi) : "v"(x));
}
DEV float half_sum(float v) {
#define DPPADD(ctrl) v += asf(__builtin_amdgcn_update_dpp(0u, asu(v), ctrl, 0xf, 0xf, true));
  DPPADD(0x111) DPPADD(0x112) DPPADD(0x114) DPPADD(0x118) DPPADD(0x142)
#undef DPPADD
  return v;
}
DEV bool fresh(u32x4 f, u64 tag) { return __builtin_amdgcn_ballot_w64(f[1] != (u32)tag || f[3] != (u32)(tag >> 32)) == 0; }
DEV void await(u32x4& f, u32 voff, u32x4 srd, u32 soff, u64 tag) {
#pragma nounroll
  for (u32 spins = 0; !fresh(f, tag) && spins < (1u << 20); ++spins) {
    __builtin_amdgcn_s_sleep(1);
    aload_sc1(f, voff, srd, soff);
    vm_on<0>(f);
  }
}

#define STAMP(i) do {} while (0)
#define STAMPF(i) do {} while (0)
#define STAMP_END() do {} while (0)

#define ROT1 2
#define PAT 0
#define DPH 10
#define PRIO 1
#define NR 4
#define NAPB 4
#define WE 5
#define HP 4
#define PRB 0
#define HCNT 4
#define UPF 2
#define WS 1
#define WT 2
#define KD 18
#define ID 60

constexpr int piece_at(int j, int i) {
  if (j == 0) return i == 2 ? 0 : i == 5 ? 1 : i == 8 ? 2 : i == 11 ? 3 : i == 14 ? 4 : i == 16 ? 5 : i == 18 ? 6 : -1;
  if (j & 1) return i == 6 ? 0 : i == 11 ? 1 : i == 16 ? 2 : i == 19 ? 3 : -1;
  return i == 5 ? 4 : i == 14 ? 5 : i == 18 ? 6 : -1;
}
constexpr int wd_blk(int b) { return (int)(0x610875432ull >> (4 * (b % 9))) & 15; }
constexpr u32 wd_loc(int k) { return wd_blk(k / 2) * 16384 + (k & 1) * 1024; }

constexpr int wd_at(int b) { return WS + WT * (b - 9); }
constexpr int gemm_wait(int k) {
  int n = 0, last = 0;
  const int end = k == 18 ? 18 : k - 1;
  for (int i = -DPH; i < end; ++i) {
    if (i + DPH < 28) { n += 2; if (i + DPH == k) last = n; }
    for (int b = 9; b < 14; ++b) if (wd_at(b) == i) { n += 4; if (k / 2 == b) last = n; }
  }
  return n - last < 63 ? n - last : 63;
}
static_assert(DPH == 10 && wd_at(13) <= 17 && wd_at(9) >= 1 && wd_at(13) >= 9);
typedef u32 u32x16 __attribute__((ext_vector_type(16)));
DEV u32x4 mk_srd2(u32 lo, u32 hi, u32 bytes) { return (u32x4){lo, hi & 0xffffu, bytes, 0x00020000u}; }

__global__ __launch_bounds__(512, 2) NOPK void fused_block(const void*, const void*, const void*, const void*, const void*, const void*,
                                                           void*, char*) {
  __shared__ __attribute__((aligned(16))) char smem[LDS_BYTES];
  u32x16 ka;
  asm volatile("s_load_dwordx16 %0, %1, 0x0" : "=s"(ka) : "s"(__builtin_amdgcn_kernarg_segment_ptr()));
  const int tid = __builtin_amdgcn_workitem_id_x();
  const int w = __builtin_amdgcn_readfirstlane(tid >> 6);
  const int bid = __builtin_amdgcn_workgroup_id_x();
  const int xcd = bid & 7, bi = bid >> 3;
  const int rb = 4 * (xcd & 1) + (bi & 3), quad = xcd >> 1, cl = bi >> 2, cb = 8 * quad + cl;
  const bool t1 = cl == 7;
  const int mg = w & 3, g = w >> 2, un = 7 * quad + cl;
  u32 rot = ROT1 == 1 ? cl + 2 * (bi & 3) : ROT1 == 2 ? 2 * (bi & 3) : ROT1 == 3 ? cl : ROT1 == 4 ? (cl & 6) + 2 * (bi & 3) : ROT1 == 5 ? 3 * ((cl ^ bi) & 1) : 0;
  rot = ROT1 == 4 ? rot & 6 : rot >= 7 ? rot - 7 : rot;
  const int rr = (tid >> 3) & 7, ch = tid & 7, odd = (tid >> 5) & 2;
  u32 vx0 = rr * 896 + ((ch ^ (rr >> 1)) << 4), vx1 = (vx0 ^ 64) + 6144;
  u32 ox = (rb * 8 + w) * (32 * 896), lw = w * 1024, so0 = ox + rot * 128;
  asm volatile("s_waitcnt lgkmcnt(0)" : "+s"(ka), "+v"(vx0), "+v"(vx1), "+s"(so0), "+s"(lw));
  STAMPF(3);
  const u32x4 sX = mk_srd2(ka[0], ka[1], S_ * D_ * 2);
  dma2<0, 0>(sX, vx0, sX, vx1, so0, 4 * lw);
  dma2<0, 2048>(sX, vx0, sX, vx1, so0 + 12288, 4 * lw);
  SBAR();
  const u32 wst = t1 ? 2688 : 3584, cw = ch ^ ((rr & 2) << 1);
  const u32 cv = (tid & 15) ^ ((tid >> 2) & 12) ^ odd;
  const u32 va = t1 ? (rr >> 1) * 2688 + (cv >= 14 ? cv - 2 : cv) * 16 : rr * 3584 + (cw ^ odd) * 16;
  const u32 ow = 8 * w * wst + (t1 ? 1792 + 224 * quad : un * 128);
  const u32 vq = ((tid >> 2) & 7) * 2688 + ((tid & 3) ^ odd) * 16;
  const u32 oq = w * (8 * 2688) + ((56 * cb) & ~15);
  const u64 pb_ = (t1 ? ((u64)ka[3] << 32 | ka[2]) + 4 * 2688 : ((u64)ka[9] << 32 | ka[8])) - 1024;
  const u32x4 sA = mk_srd2(t1 ? ka[2] : ka[6], t1 ? ka[3] : ka[7], 1u << 21), sB = mk_srd2((u32)pb_, (u32)(pb_ >> 32), 1u << 21);
  const u32x4 sQ = mk_srd2(ka[2], ka[3], D_ * 3 * D_ * 2);
  u32 kst = rot, so = 0;
  auto piece = [&](auto H_, auto I_) LAM {
    constexpr int H = H_.value, i = I_.value;
    if constexpr (i == 0) {
      if constexpr (H > 0) { kst = opaque(kst) + 1; kst = kst == 7 ? 0 : kst; }
      so = ow + kst * (64 * wst);
    }
    if constexpr (i < 2) dma1<(6 + H % 3) * 16384, i * 1024>(i ? sB : sA, va, so, 2 * lw);
    if constexpr (i == 2) dma_half<L_QK + (H % 3) * 4096>(sQ, vq, oq + opaque(kst) * (64 * 2688), lw >> 1);
    if constexpr (i == 3) so = ox + opaque(kst) * 128;
    if constexpr (i == 5) so = opaque(so) + 12288;
    if constexpr (i >= 3) dma1<(H % 3) * 32768, (i - 3) * 1024>(sX, (i & 1) ? vx0 : vx1, so, 4 * lw);
  };
  sfor<0, 3>([&](auto i) LAM { piece(IC<0>{}, i); });
  asm volatile("s_barrier" ::: "memory");
  STAMPF(4);
  u32x4 xr[2];
  const u32 row = rb * 256 + cb * 8 + 2 * mg;
  {
    const u32 vr = tid >= 256 && (tid & 31) < 28 ? (tid & 31) * 16 + (tid & 32) * 28 : OOB;
    aload<0>(xr[0], vr, sX, row * 896);
    aload<448>(xr[1], vr, sX, row * 896);
  }
  char* const ws = (char*)((u64)ka[15] << 32 | ka[14]);
  u64 epoch;
  asm volatile("s_load_dwordx2 %0, %1, 0x0" : "=s"(epoch) : "s"(ws));
  u64 pc0;
  u32 clen;
  asm volatile("s_getpc_b64 %0\n.Lfb_pc0:\n\ts_max_u32 %1, .Lfb_enda-.Lfb_pc0, 0\n\ts_max_u32 %1, .Lfb_endb-.Lfb_pc0, %1" : "=s"(pc0), "=s"(clen)::"scc");
  {
    const u32 o = (bi * 16 + w * 2 + (tid & 1)) * 128;
    touch((const char*)pc0 + (o < clen ? o : clen));
  }
  sfor<0, 7>([&](auto i) LAM { piece(IC<1>{}, i); });
  SBAR();
  asm volatile("" ::"v"((u32)(uintptr_t)(LAS char*)smem));
  const char* const px = (const char*)((u64)ka[1] << 32 | ka[0]);
  const char* const pwo = (const char*)((u64)ka[5] << 32 | ka[4]);
  char* const pout = (char*)((u64)ka[13] << 32 | ka[12]);
  const u32x4 sD = mk_srd2(ka[10], ka[11], F_ * D_ * 2), sWS = mk_srd2(ka[14], ka[15], WS_END);
  const rsrc_t rWS = __builtin_amdgcn_make_buffer_rsrc(ws, 0, WS_END, 0x00020000);
  if (tid < 16) *(LAS u32*)(L_CNT + tid * 4) = 0;
  const bool has_tile = !t1;
  const int rq = bi / 7, rt = 4 * xcd + rq, nt = bi - 7 * rq;
  const int rotg = (4 * nt + 8 * rq) % 28;
  const int head = 2 * xcd + (bi >> 4), qb = bi & 15;
  const u32 vw0 = rr * 896 + cw * 16, vw1 = vw0 + 6144;
  auto krot = [&](u32 k) LAM -> u32 { k += opaque(rotg); return k >= 28 ? k - 28 : k; };
  const u32 od = 448 * mg * 896 + nt * 128;

  asm volatile("s_waitcnt lgkmcnt(0)" : "+s"(epoch));
  u64 tag = (epoch + 0x9E3779B97F4A7C15ull) * 0xBF58476D1CE4E5B9ull;
  tag = (tag ^ (tag >> 29)) + llvm_dispatch_id() * 0x94D049BB133111EBull;
  tag = (tag ^ (tag >> 31)) * 0xD6E8FEB86659FD93ull;
  asm volatile("" : "+s"(tag));
  float rs[4];
  f32x4 acc[5][4];
  auto geglu = [&](auto M) LAM -> u32x4 {
    constexpr int m = M.value;
    const float r2 = rs[m] * rs[m], K1 = (-2.f * 0.7978845608028654f * 1.4426950408889634f) * rs[m], K2 = K1 * 0.044715f * r2, IR2 = rcp(r2);
    u32x4 hp;
    sfor<0, 4>([&](auto j) LAM {
      constexpr int p = 1 + (j.value & 2), e = 2 * (j.value & 1);
      float y[2];
      sfor<0, 2>([&](auto i) LAM {
        const float t = acc[p][m][e + i.value];
        y[i.value] = t * acc[p + 1][m][e + i.value] * rcp(__builtin_fmaf(ex2(t * __builtin_fmaf(t * t, K2, K1)), IR2, IR2));
      });
      hp[j.value] = cvtpk(y[0], y[1]);
    });
    return hp;
  };
  auto frag_off = [&]() LAM -> u32 { const u32 l = lane_id(); return (l >> 4) * 512 + (l & 15) * 16; };
  u32x4 hp[4];

  auto phase1 = [&](auto T_) LAM {
    constexpr int T = T_.value;
    u32 xa[2][2], fa[4], qa, vl_, cw4, cw0, cr, bt;
    {
      const int lane = lane_id(), g4 = lane >> 4, tok = lane & 15, q4 = tok >> 2, p4 = lane & 3;
      xa[0][0] = (mg * 64 + tok) * 128 + ((g4 ^ (tok >> 1)) << 4);
      xa[0][1] = xa[0][0] ^ 64;
      xa[1][0] = xa[0][0] + 65536;
      xa[1][1] = xa[0][1] + 65536;
      sfor<0, 4>([&](auto t) LAM {
        if constexpr (T == 0) fa[t.value] = 6 * 16384 + g4 * 2048 + q4 * 128 + ((64 * g + 16 * (t.value & 2)) ^ ((q4 & 2) << 5) ^ ((g4 & 1) << 5)) + 8 * p4;
        else fa[t.value] = 6 * 16384 + g4 * 2048 + q4 * 256 + ((112 * g + 28 * (t.value & 2) + 32 * (t.value & 1) + 8 * p4) ^ (q4 * 64) ^ ((g4 & 1) << 5));
      });
      qa = L_QK + g4 * 512 + q4 * 64 + ((((cb & 1) * 8 + 32 * g + 8 * p4) & 63) ^ ((g4 & 1) << 5));
      PIN(xa[0][0]); PIN(xa[0][1]); PIN(xa[1][0]); PIN(xa[1][1]); PIN(fa[0]); PIN(fa[2]); PIN(qa);
      if constexpr (T == 1) { PIN(fa[1]); PIN(fa[3]); }
      vl_ = lane * 16;
      bt = tok * 4;
      cw4 = L_CS + (g * 80 + tok) * 16 + mg * 4;
      cw0 = cw4 + g4 * 256;
      cr = L_CS + (g * 80 + tok) * 16 + (g4 & 1) * 8;
      PIN(vl_); PIN(bt); PIN(cw4); PIN(cw0); PIN(cr);
    }
    u32x4 one = {0x3f803f80u, 0x3f803f80u, 0x3f803f80u, 0x3f803f80u};
    PIN(one);
    u32x4 xf[2][4], wf[2][5], sg;
    u32x2 cf[5], nm[4];
    f32x4 cs[5] = {};
    sfor<0, 5>([&](auto t) LAM { PIN(cs[t.value]); });
    const int duty = (mg + 2 * g) & 3;
    f32x4 rv[4];
    float rl;
    u32 nl;

    sync_stage<8>();
    STAMP(1);
    auto rd = [&](auto J, auto R) LAM {
      constexpr int j = J.value, r = R.value, sl = (j / 2) % 3, s = j & 1;
      if constexpr (r >= 2 && r < 6) xf[j & 1][r - 2] = lds128(xa[sl >> 1][s] + (sl & 1) * 32768 + (r - 2) * 2048);
      else {
        constexpr int t = r < 2 ? 0 : (r - 4) / 2, h = r & 1;
        u32x2 v;
        if constexpr (t == 0) v = trd(qa + sl * 4096 + s * 2048 + h * 256);
        else if constexpr (T == 0) v = trd(fa[(t - 1) & 2] + sl * 16384 + s * 8192 + ((t - 1) & 1) * 1024 + h * 512);
        else v = trd(fa[t - 1] + sl * 16384 + s * 8192 + h * 1024);
        wf[j & 1][t][2 * h] = v[0];
        wf[j & 1][t][2 * h + 1] = v[1];
      }
    };
    sfor<0, 14>([&](auto r) LAM { rd(IC<0>{}, r); });
    SBAR();
    {
      u32 d = 0, vs = OOB;
      if (g != 0) {
        float a[16], t = 0.f, q = 0.f;
        sfor<0, 8>([&](auto i) LAM {
          widen(a[2 * i.value], a[2 * i.value + 1], xr[i.value >> 2][i.value & 3]);
          t += a[2 * i.value] + a[2 * i.value + 1];
        });
        t = half_sum(t) * (1.f / 448.f);
        const float m0 = asf(__builtin_amdgcn_readlane(asu(t), 31)), m1 = asf(__builtin_amdgcn_readlane(asu(t), 63)), mean = vl_ < 512 ? m0 : m1;
        sfor<0, 16>([&](auto i) LAM { const float e = a[i.value] - mean; q = __builtin_fmaf(e, e, q); });
        const float var = half_sum((vl_ & 511) < 448 ? q : 0.f) * (1.f / 448.f);
        d = cvtpk(-0.5f * mean, __builtin_amdgcn_sqrtf(bfround(bfround(var) + 1e-5f)));
        vs = (vl_ & 511) == 496 ? vl_ >> 5 & 16 : OOB;
      }
      st128(rWS, (u32x4){d, (u32)tag, d, (u32)(tag >> 32)}, vs, WS_ST + row * 16);
    }
    SBAR();
    sfor<0, 14>([&](auto J) LAM {
      constexpr int j = J.value, G = j / 2;
      sfor<0, 20>([&](auto I) LAM {
        constexpr int i = I.value, t = i / 4, m = i % 4;
        const f32x4 c = j == 0 ? (f32x4){} : acc[t][m];
        if constexpr (T == 1 && t > 0) acc[t][m] = mfma16(xf[j & 1][m], wf[j & 1][t], c);
        else acc[t][m] = mfma16(wf[j & 1][t], xf[j & 1][m], c);
        PIN(acc[t][m]);
        SBAR();
        if constexpr ((j & 1) && i == 1 && j < 13) sync_stage<(G == 0 || G == 4 ? 8 : (G < 4 ? 7 : (T == 0 ? 4 : 0)))>();
        if constexpr ((j & 1) && i == 1 && j < 13) STAMPF(5 + G);
        constexpr int r = i - 2 * (j & 1);
        if constexpr (j < 13 && r >= 0 && r < 14) rd(IC<j + 1>{}, IC<(r >= 0 && r < 14 ? r : 0)>{});
        if constexpr (i == (j < 13 ? 3 : 0)) colsum5<j & 3>(cs, one, wf[j & 1], duty);
        constexpr int pc = piece_at(j, i), H = (j + 5) / 2;
        if constexpr (pc >= 0 && H < 7) piece(IC<H>{}, IC<(pc >= 0 ? pc : 0)>{});
        if constexpr (T == 0 && (j == 9 || j == 11) && pc >= 0) {
          if constexpr (pc == 0) so = od + krot(2 * (j - 9) + 2 * g) * 14336;
          if constexpr (pc == 2) so = opaque(so) + 12288;
          dma1<(j - 7) * 16384, pc * 1024>(sD, (pc & 1) ? vw1 : vw0, so, 4 * lw);
        }
        if constexpr (j == 9 && i == 0) aload_sc1(sg, vl_, sWS, WS_ST + (rb * 256 + mg * 64) * 16);
        if constexpr (j == 11 && i == 17) {
          PIN(sg);
          await(sg, vl_, sWS, WS_ST + (rb * 256 + mg * 64) * 16, tag);
          rl = rcp(asf(sg[0] & 0xffff0000u));
          nl = __builtin_amdgcn_perm(sg[0], sg[0], 0x01000100u);
          if constexpr (T == 1) *(LAS float*)(L_RV + mg * 256 + (vl_ >> 2)) = rl;
        }
        if constexpr (j == 12 && i >= 14 && i < 18) {
          nm[i - 14][0] = __builtin_amdgcn_ds_bpermute(bt + 64 * (i - 14), nl);
          rs[i - 14] = asf(__builtin_amdgcn_ds_bpermute(bt + 64 * (i - 14), asu(rl)));
        }
        if constexpr (j == 13) {
          auto put = [&](u32 a, float v) LAM { const float hi = bfround(v); *(LAS u32*)a = cvtpk(hi, v - hi); };
          if constexpr (i == 3) {
            const float c0 = cs[0][0], c1 = cs[1][0], c2 = cs[2][0], c3 = cs[3][0];
            put(cw0, vl_ < 512 ? (vl_ < 256 ? c0 : c1) : (vl_ < 768 ? c2 : c3));
          }
          if constexpr (T == 1 && i == 1) sfor<0, 4>([&](auto m) LAM { rv[m.value] = lds128f(L_RV + mg * 256 + ((vl_ >> 4) & 48) + 64 * m.value); });
          if constexpr (i == 5) put(cw4 + 1024, cs[4][0]);
          if constexpr (i == 9) {
            sync_stage<63>();
            STAMPF(11);
            sfor<0, 5>([&](auto u) LAM { cf[u.value] = *(LAS u32x2*)(cr + u.value * 256); });
          }
          if constexpr (i >= 10 && i < 14) {
            nm[i - 10][1] = nm[i - 10][0];
            PIN(nm[i - 10]);
          }
        }
        SBAR();
      });
    });
    STAMP(2);
    __builtin_amdgcn_s_setprio(2);
    sfor<0, 20>([&](auto I) LAM {
      constexpr int t = I.value / 4, m = I.value % 4;
      if constexpr (T == 1 && t > 0) acc[t][m] = mfma16h(nm[m], cf[t], acc[t][m]);
      else acc[t][m] = mfma16h(cf[t], nm[m], acc[t][m]);
    });
    LANES();
    float km;
    {
      const bool isq = cb < 16, pad = g == 1 && lane >= 48;
      const u32 so = (isq ? WS_Q + ((cb & 15) * 64 + rb * 8 + mg * 2) * 2048 : WS_KV + ((cb & 15) * 64 + rb * 8 + mg * 2) * 4096) + g * 1024;
      const u32 vo = isq ? vl : (vl & 0x230u) | ((vl & 0xc0u) << 1) | ((vl & 0x100u) >> 2);
      u32 n2[2];
      sfor<0, 2>([&](auto U) LAM {
        constexpr int u = U.value;
        u32 P[2][2], n[2];
        sfor<0, 2>([&](auto i) LAM {
          const float sc = isq ? rs[2 * u + i.value] * (0.18898223650461362f * 1.4426950408889634f) : rs[2 * u + i.value];
          const f32x4 a = acc[0][2 * u + i.value];
          const float v0 = a[0] * sc, v1 = a[1] * sc, v2 = a[2] * sc, v3 = a[3] * sc;
          P[i.value][0] = pad ? 0u : cvtpk(v0, v1);
          P[i.value][1] = pad ? 0u : cvtpk(v2, v3);
          n[i.value] = pad ? 0u : asu(__builtin_fmaf(v0, v0, v1 * v1) + __builtin_fmaf(v2, v2, v3 * v3));
        });
        swap16(P[0][0], P[1][0]);
        swap16(P[0][1], P[1][1]);
        st128(rWS, (u32x4){P[0][0], P[0][1], P[1][0], P[1][1]}, vo, so + (isq ? 2048 : 4096) * u);
        swap16(n[0], n[1]);
        n2[u] = asu(asf(n[0]) + asf(n[1]));
      });
      swap32(n2[0], n2[1]);
      const float n = asf(n2[0]) + asf(n2[1]);
      if (isq) __builtin_amdgcn_raw_buffer_store_b32(asu(n), rWS, lane * 4, WS_QN + ((cb * 2 + g) * 2048 + rb * 256 + mg * 64) * 4, 16);
      km = wave_max_nonneg(n);
    }
    auto qk_flag = [&]() LAM {
      u32 n = 0;
      cfence();
      if (lane == 0) {
        __hip_atomic_fetch_max((LAS u32*)(L_CNT + 40 + 4 * g), asu(km), __ATOMIC_RELAXED, __HIP_MEMORY_SCOPE_WORKGROUP);
        n = __hip_atomic_fetch_add((LAS u32*)L_CNT, 1u, __ATOMIC_RELAXED, __HIP_MEMORY_SCOPE_WORKGROUP);
      }
      cfence();
      if (__builtin_amdgcn_readfirstlane(n) == 7) {
        const u32 k2 = asu(asf(cnt_get(10)) + asf(cnt_get(11)));
        const u32x4 f = {k2, (u32)tag, k2, (u32)(tag >> 32)};
        if (lane < (cb < 16 ? 2 : 16)) st128(rWS, f, lane * 512, WS_FQ + (cb < 16 ? (cb * 16 + 2 * rb) * 512 + 256 : (cb - 16) * 8192 + rb * 16));
        if constexpr (T == 1) st128(rWS, f, lane * 512, WS_FQ + quad * 32768 + 128 + rb * 16);
      }
      STAMP(13);
    };
    if constexpr (T == 1) {
      const bool oc = (lane & 15) == 12;
      const u32 fo = frag_off();
      sfor<0, 8>([&](auto I) LAM {
        constexpr int t = 1 + I.value / 2, u = I.value & 1;
        u32x4 P;
        sfor<0, 4>([&](auto e) LAM {
          constexpr int m = 2 * u + e.value / 2, c = 2 * (e.value & 1);
          P[e.value] = cvtpk(acc[t][m][c] * rv[m][c], acc[t][m][c + 1] * rv[m][c + 1]);
          if constexpr ((t & 1) == 0) P[e.value] = oc ? 0x3f803f80u : P[e.value];
        });
        st128(rWS, P, fo + ((t - 1) & 1) * 256, WS_KV + ((4 * quad + 2 * g + (t - 1) / 2) * 64 + rb * 8 + mg * 2 + u) * 4096 + 2048);
      });
      STAMP(3);
      vmwait<0>();
      qk_flag();
    } else {
      STAMP(3);
      PIN(km);
      __builtin_amdgcn_s_setprio(1);
      SBAR();
      hp[0] = geglu(IC<0>{});
      PIN(hp[0]);
      vmwait<0>();
      qk_flag();
      __builtin_amdgcn_s_setprio(0);
      SBAR();
      sfor<1, 4>([&](auto m) LAM { hp[m.value] = geglu(m); PIN(hp[m.value]); });
    }
  };
  if (!t1) phase1(IC<0>{}); else phase1(IC<1>{});
  STAMP(4);
  LANES();
  if (w < 4) {
    if (PRIO) __builtin_amdgcn_s_setprio(2);
    if (has_tile) {
      sfor<0, 4>([&](auto m) LAM { *(LAS u32x4*)(6 * 16384 + w * 4096 + m.value * 1024 + vl) = hp[m.value]; });
      cnt_add(12 + w);
    }
    u32x4 fk;
    const u32 vfo = (lane < 17 ? lane : 0) * 16, sfo = WS_FQ + (head * 16 + qb) * 512;
    u32 ord = 0;
#pragma nounroll
    for (u32 spins = 0; spins < (1u << 20); ++spins) {
      aload_sc1(fk, vfo, sWS, sfo);
      vm_on<0>(fk);
      const u32 m = (u32)__builtin_amdgcn_ballot_w64(fk[1] == (u32)tag && fk[3] == (u32)(tag >> 32));
      ord = m & (m >> 8) & 255;
      if ((m & 0x10000) && __builtin_popcount(ord) >= NR) break;
      __builtin_amdgcn_s_sleep(1);
    }
    STAMP(5);
    ord = ((ord | ord << 8) >> (qb >> 1)) & 255;
    ord |= (ord ^ 255) << 8;
    const u32 kvb = WS_KV + (head * 64 + 2 * w) * 4096;
    u32 kvc = 0;
    auto kv_next = [&](int kt) LAM {
      if (kt & 1) kvc = opaque(kvc) ^ 4096;
      else {
        ord = opaque(ord);
        kvc = kvb + (qb & 1) * 4096 + ((__builtin_ctz(ord) + (qb >> 1)) & 7) * 32768;
        ord &= ord - 1;
      }
    };
    u32x4 Q[4][2], KV[4][4];
    auto q_load = [&](auto t) LAM {
      const u32 so = WS_Q + (head * 64 + qb * 4 + ((w + 1 + t.value) & 3)) * 2048;
      Q[t.value][0] = ld128(rWS, vl, so);
      Q[t.value][1] = ld128(rWS, vl + 1024, so);
    };
    kv_next(0);
    KV[0][0] = ld128(rWS, vl, kvc);
    KV[0][1] = ld128(rWS, vl + 1024, kvc);
    q_load(IC<0>{});
    q_load(IC<1>{});
    KV[0][2] = ld128(rWS, vl + 2048, kvc);
    q_load(IC<2>{});
    KV[0][3] = ld128(rWS, vl + 3072, kvc);
    q_load(IC<3>{});
    SBAR();
    sfor<1, UPF>([&](auto b) LAM {
      kv_next(b.value);
      sfor<0, 4>([&](auto i) LAM { KV[b.value][i.value] = ld128(rWS, vl + i.value * 1024, kvc); });
    });
    SBAR();
    cnt_add(3);
    STAMP(6);
    const int idx = 0xDDDD;
    f32x16 O[4] = {};
    const f32x16 Z = {};
    auto rgn = [&](int t) LAM -> u32 { return ((0x6108u >> (4 * t)) & 15) * 16384u; };
    auto put_part = [&](auto t, auto i) LAM {
      const f32x16& o = O[t.value];
      *(LAS f32x4*)(rgn((w + 1 + t.value) & 3) + t.value * 4096 + i.value * 1024 + vl) =
          (f32x4){o[4 * i.value], o[4 * i.value + 1], o[4 * i.value + 2], o[4 * i.value + 3]};
    };
    f32x16 Sx[4];
    u32 cb1 = 0, qn[4];
    constexpr int CK = 8 * NR - 12, CP = CK - 10;
    auto tile = [&](auto I) LAM {
      constexpr int kt = I.value >> 2, b = kt & 3, t = I.value & 3, tn = (t + 1) & 3, tp = (t + 3) & 3;
      constexpr int bn = t == 3 ? (b + 1) & 3 : b, bp = t == 0 ? (b + 3) & 3 : b;
      constexpr int E1 = PAT ? 3 : 4, E2 = 2 * E1, E3 = 3 * E1;
      auto exps = [&](auto lo, auto hi) LAM {
        SBAR();
        sfor<lo.value, hi.value>([&](auto e) LAM { Sx[t][e.value] = ex2(Sx[t][e.value]); });
        PIN(Sx[t]);
        SBAR();
      };
      if constexpr (I.value < 63) Sx[tn] = mfma32(KV[bn][0], Q[tn][0], Z);
      exps(IC<0>{}, IC<E1>{});
      if constexpr (I.value < 63) Sx[tn] = mfma32(KV[bn][1], Q[tn][1], Sx[tn]);
      if constexpr (I.value < 63) PIN(Sx[tn]);
      exps(IC<E1>{}, IC<E2>{});
      if constexpr (I.value > 0) O[tp] = smf_hi(KV[bp][3], Sx[tp], O[tp], idx);
      PIN(O[tp]);
      exps(IC<E2>{}, IC<E3>{});
      O[t] = smf_lo(KV[b][2], Sx[t], O[t], idx);
      PIN(O[t]);
      exps(IC<E3>{}, IC<E3 + 2>{});
      if constexpr (I.value == CP) {
        LANES();
        sfor<0, 4>([&](auto u) LAM {
          qn[u.value] = __builtin_amdgcn_raw_buffer_load_b32(rWS, hf * 8192 + r32 * 4, WS_QN + (head * 4096 + qb * 128 + u.value * 32) * 4, 16);
        });
        aload_sc1(fk, vfo, sWS, sfo);
      }
      constexpr int NX = 4 * (3 - UPF);
      sfor<(I.value < NX ? 4 * UPF + 2 * I.value : I.value + 12), (I.value < NX ? 4 * UPF + 2 * I.value + 2 : I.value + 13)>([&](auto L) LAM {
        if constexpr (L.value < 64) {
          if constexpr ((L.value & 3) == 0) kv_next(L.value >> 2);
          KV[(L.value >> 2) & 3][L.value & 3] = ld128(rWS, vl + (L.value & 3) * 1024, kvc);
        }
      });
      if constexpr (I.value == ID) cnt_add(9);
      if constexpr (I.value == 58) cb1 = cnt_get(1);
      if constexpr (I.value == 60) { if (has_tile && __builtin_amdgcn_readfirstlane(cb1) < 4u) cnt_wait(1, 4); cfence(); }
      if constexpr (I.value >= 62) sfor<0, 4>([&](auto i) LAM { put_part(IC<I.value - 62>{}, i); });
      exps(IC<E3 + 2>{}, IC<16>{});
    };
    Sx[0] = mfma32(KV[0][1], Q[0][1], mfma32(KV[0][0], Q[0][0], Z));
    SBAR();
    sfor<0, CK>(tile);
    vm_on<CK - CP>(fk);
    await(fk, vfo, sWS, sfo, tag);
    u64 risky;
    {
      swap32(qn[0], qn[1]);
      swap32(qn[2], qn[3]);
      const u32 a = asu(asf(qn[0]) + asf(qn[1])), b = asu(asf(qn[2]) + asf(qn[3]));
      risky = __builtin_amdgcn_ballot_w64(!(asf(a > b ? a : b) * max8_nonneg(asf(fk[0])) <= 2200.f));
      asm volatile("" : "+s"(risky));
    }
    sfor<CK, 64>(tile);
    O[3] = smf_hi(KV[3][3], Sx[3], O[3], idx);
    if (risky != 0) {
      sfor<0, 4>([&](auto t) LAM { O[t.value] = Z; });
      auto kvo = [&](int kt) LAM -> u32 { return kvb + (kt >> 1) * 32768 + (kt & 1) * 4096; };
      float mx[4] = {-3.0e38f, -3.0e38f, -3.0e38f, -3.0e38f};
      for (int kt = 0; kt < 16; ++kt) {
        const u32x4 k0 = ld128(rWS, vl, kvo(kt)), k1 = ld128(rWS, vl + 1024, kvo(kt));
        sfor<0, 4>([&](auto t) LAM {
          const f32x16 s = mfma32(k1, Q[t.value][1], mfma32(k0, Q[t.value][0], Z));
          sfor<0, 16>([&](auto i) LAM { mx[t.value] = __builtin_fmaxf(mx[t.value], s[i.value]); });
        });
      }
      sfor<0, 4>([&](auto t) LAM {
        mx[t.value] = __builtin_fmaxf(mx[t.value], other_half(mx[t.value]));
        *(LAS float*)(L_VT + ((((w + 1 + t.value) & 3) * 32 + r32) * 4 + w) * 4) = mx[t.value];
      });
      cnt_add(8);
      cnt_wait(8, 4);
      sfor<0, 4>([&](auto t) LAM {
        const f32x4 m4 = lds128f(L_VT + (((w + 1 + t.value) & 3) * 32 + r32) * 16);
        mx[t.value] = __builtin_fmaxf(__builtin_fmaxf(m4[0], m4[1]), __builtin_fmaxf(m4[2], m4[3]));
      });
      for (int kt = 0; kt < 16; ++kt) {
        const u32x4 k0 = ld128(rWS, vl, kvo(kt)), k1 = ld128(rWS, vl + 1024, kvo(kt));
        const u32x4 v0 = ld128(rWS, vl + 2048, kvo(kt)), v1 = ld128(rWS, vl + 3072, kvo(kt));
        sfor<0, 4>([&](auto t) LAM {
          f32x16 s = mfma32(k1, Q[t.value][1], mfma32(k0, Q[t.value][0], Z));
          sfor<0, 16>([&](auto i) LAM { s[i.value] = ex2(s[i.value] - mx[t.value]); });
          O[t.value] = smf_hi(v1, s, smf_lo(v0, s, O[t.value], idx), idx);
        });
      }
      sfor<0, 2>([&](auto t) LAM { sfor<0, 4>([&](auto i) LAM { put_part(t, i); }); });
    }
    STAMP(7);
    sfor<0, 4>([&](auto i) LAM { put_part(IC<2>{}, i); });
    cnt_add(2);
    cnt_wait(2, 4);
    const u32 red = rgn(w);
    {
      LANES();
      float o[16];
      sfor<0, 4>([&](auto i) LAM {
        const u32 ra = red + i.value * 1024 + vl;
        const f32x4 a0 = lds128f(ra), a1 = lds128f(ra + 4096), a2 = lds128f(ra + 8192);
        sfor<0, 4>([&](auto j) LAM { o[4 * i.value + j.value] = (a0[j.value] + a1[j.value]) + (a2[j.value] + O[3][4 * i.value + j.value]); });
      });
      const float lo_ = other_half(o[12]);
      const float il = rcp(hf ? o[12] : lo_);
      u32 P[8];
      sfor<0, 8>([&](auto i) LAM { P[i.value] = cvtpk(o[2 * i.value] * il, o[2 * i.value + 1] * il); });
      const int om = qb * 4 + w;
      st128(rWS, (u32x4){P[0], P[1], P[2], P[3]}, vl, WS_O + (om * 16 + head) * 2048);
      st128(rWS, (u32x4){P[4], P[5], hf ? 0u : P[6], hf ? 0u : P[7]}, vl + 1024, WS_O + (om * 16 + head) * 2048);
      if (has_tile) {
        const u32x4 sO = mk_srd(pwo, D_ * D_ * 2);
        const u32 vw = (lane >> 3) * 896 + (((lane & 7) ^ ((lane >> 2) & 4)) << 4);
        sfor<0, 14>([&](auto e) LAM { dma_i<e.value * 1024, 8 * e.value * 896>(sO, vw, 112 * w * 896 + nt * 128, red); });
        vmwait<14>();
      } else vmwait<0>();
      if (lane == 0) put_flag(ws, WS_FO + (om * 16 + head) * 8, tag);
    }
    STAMP(8);
    if (!has_tile) { STAMP_END(); return; }
    {
    LANES();
    const u32 trow = (4 * hf + q4) * 128, swz = (q4 & 2) << 5;
    const int m = w & 1, kh = w >> 1;
    const rsrc_t rX = __builtin_amdgcn_make_buffer_rsrc((void*)px, 0, S_ * D_ * 2, 0x00020000);
    const u32 xo = ((rt * 64 + m * 32 + r32) * 448 + nt * 64 + kh * 32 + hf * 8) * 2;
    u32x4 xr[2];
    xr[0] = __builtin_amdgcn_raw_buffer_load_b128(rX, xo, 0, 0);
    xr[1] = __builtin_amdgcn_raw_buffer_load_b128(rX, xo + 32, 0, 0);
    PIN(xr[0]); PIN(xr[1]);
    vmwait<0>();
    cnt_add(6);
    poll_flags<2>(ws, WS_FO + ((2 * rt + m) * 16 + 8 * kh + (lane & 7)) * 8, tag);
    STAMP(9);
    u32x4 of[16];
    sfor<0, 16>([&](auto k) LAM { of[k.value] = ld128(rWS, vl + (k.value & 3) * 1024, WS_O + ((2 * rt + m) * 16 + 8 * kh) * 2048 + (k.value >> 2) * 4096); });
    SBAR();
    cnt_wait(6, 4);
    cnt_wait(5, 4);
    f32x4 gt[4];
    sfor<0, 4>([&](auto i) LAM { gt[i.value] = lds128f(5 * 16384 + w * 4096 + i.value * 1024 + vl); });
    u32 xs[8] = {xr[0][0], xr[0][1], xr[0][2], xr[0][3], xr[1][0], xr[1][1], xr[1][2], xr[1][3]};
    swap32(xs[0], xs[2]); swap32(xs[1], xs[3]); swap32(xs[4], xs[6]); swap32(xs[5], xs[7]);
    f32x16 acc[2] = {};
    sfor<0, 2>([&](auto r) LAM {
      const u32 rg = rgn(2 * kh + r.value);
      u32 wa[2], wp[2];
      sfor<0, 2>([&](auto n) LAM {
        wa[n.value] = rg + trow + ((64 * (n.value ^ kh) + 32 * blk + 8 * p4) ^ swz);
        wp[n.value] = wa[n.value] - hf * 512;
      });
      sfor<0, 8>([&](auto k) LAM {
        constexpr int hh = k.value >> 1, s = k.value & 1;
        constexpr u32 off = (28 * hh + 16 * s) * 128;
        sfor<0, 2>([&](auto n) LAM {
          constexpr int nn = 1 - n.value;
          const u32x4 wf = trd2(wa[nn] + off, (s ? wp[nn] : wa[nn]) + off + 1024);
          acc[nn] = mfma32(wf, of[8 * r.value + k.value], acc[nn]);
        });
      });
    });
    sfor<0, 4>([&](auto i) LAM {
      *(LAS f32x4*)(7 * 16384 + w * 4096 + i.value * 1024 + vl) =
          (f32x4){acc[1][4 * i.value], acc[1][4 * i.value + 1], acc[1][4 * i.value + 2], acc[1][4 * i.value + 3]};
    });
    STAMP(10);
    cnt_add(7);
    cnt_wait(7, 4);
    u32 R[8];
    sfor<0, 4>([&](auto i) LAM {
      const f32x4 a = lds128f(7 * 16384 + (w ^ 2) * 4096 + i.value * 1024 + vl);
      float y[4];
      sfor<0, 4>([&](auto j) LAM {
        const u32 xd = xs[2 * i.value + (j.value >> 1)];
        const float xv = asf((j.value & 1) ? (xd & 0xffff0000u) : (xd << 16));
        y[j.value] = __builtin_fmaf(acc[0][4 * i.value + j.value] + a[j.value], gt[i.value][j.value], xv);
      });
      R[2 * i.value] = cvtpk(y[0], y[1]);
      R[2 * i.value + 1] = cvtpk(y[2], y[3]);
    });
    swap32(R[0], R[2]); swap32(R[1], R[3]); swap32(R[4], R[6]); swap32(R[5], R[7]);
    const rsrc_t rOut = __builtin_amdgcn_make_buffer_rsrc(pout, 0, S_ * D_ * 2, 0x00020000);
    st128p(rOut, (u32x4){R[0], R[1], R[2], R[3]}, xo);
    st128p(rOut, (u32x4){R[4], R[5], R[6], R[7]}, xo + 32);
    asm volatile(".Lfb_enda:");
    STAMP(11);
    STAMP_END();
    }
  } else {
    const u32 trow = (4 * hf + q4) * 128, swz = (q4 & 2) << 5;
    const int gw = w - 4;
    const u32 lg = gw * 4096;
    auto wload = [&](auto B) LAM {
      const u32 so = od + krot(2 * B.value) * 14336;
      dma2<wd_loc(2 * B.value), 0>(sD, vw0, sD, vw1, so, lg);
      dma2<wd_loc(2 * B.value), 2048>(sD, vw0, sD, vw1, so + 12288, lg);
    };
    if (has_tile) {
      sfor<4, 4 + (WE < 4 ? WE : 4)>(wload);
      u32x4 ha[4];
      cnt_wait(12 + gw, 1);
      sfor<0, 4>([&](auto m) LAM { ha[m.value] = lds128(6 * 16384 + lg + m.value * 1024 + vl); PIN(ha[m.value]); });
      if constexpr (WE == 5) wload(IC<8>{});
      const u32 fo = frag_off(), ho = WS_H + ((rb * 8 + gw * 2) * 112 + un * 4) * 1024;
      cnt_wait(3, HCNT);
      STAMP(9);
      sfor<0, 8>([&](auto i) LAM {
        constexpr int m = i.value >> 1;
        st128(rWS, (i.value & 1) ? hp[m] : ha[m], fo + (m & 1) * 256 + (i.value & 1) * 2048, ho + (m >> 1) * (112 * 1024));
        if constexpr (i.value < 7) __builtin_amdgcn_s_sleep(HP);
      });
      vmwait<0>();
      if (lane == 0) put_flag(ws, WS_FH + ((rb * 4 + gw) * 32 + un) * 8, tag);
      STAMP(5);
      sfor<4 + WE, 9>(wload);
      touch(pwo + ((rq & 3) * 112 + gw * 28 + (lane < 28 ? lane : 0)) * 896 + nt * 128);
      STAMP(8);
    }
    if (bid == 255 && gw == 0) {
      for (u32 i = 0; i < 4; ++i) {
        u32x4 f = {0, 0, 0, (u32)~(tag >> 32)};
        await(f, lane * 128, sWS, WS_ST + i * 8192, tag);
      }
      if (lane == 0) put_flag(ws, WS_EPOCH, epoch + 1);
    }
    if (!has_tile) { STAMP_END(); return; }
    const u32x4 sH = mk_srd(ws + WS_H, 64 * 112 * 1024);
    const u32 oh0 = ((2 * rt + (gw & 1)) * 112 + 28 * gw) * 1024, oh1 = ((2 * rt + 1 - (gw & 1)) * 112 + 28 * gw) * 1024;
    u32x4 hfr[DPH][2];
    auto hload = [&](auto K) LAM {
      aload_sc1(hfr[K.value % DPH][0], vl, sH, oh0 + krot(K.value) * 1024);
      aload_sc1(hfr[K.value % DPH][1], vl, sH, oh1 + krot(K.value) * 1024);
    };
    cnt_wait(0, 8);
    poll_flags<NAPB>(ws, WS_FH + (rt * 32 + 7 * gw + (lane < 7 ? lane : 0)) * 8, tag);
    STAMP(6);
    sfor<0, DPH>([&](auto K) LAM { hload(K); });
    __builtin_amdgcn_s_setprio(PRB);
    u32 wa[2];
    sfor<0, 2>([&](auto n) LAM { wa[n.value] = lg + trow + ((64 * (n.value ^ (gw >> 1)) + 32 * blk + 8 * p4) ^ swz); });
    f32x16 acc[2][2] = {};
    u32x4 wf[2][2];
    auto ready = [&](auto K, auto N) LAM {
      constexpr int k = K.value;
      constexpr int o = wd_loc(k), d = (o & ~65535) - (k ? (int)(wd_loc(k - 1) & ~65535) : 0);
      vm_on2<N.value>(hfr[k % DPH][0], hfr[k % DPH][1]);
      sfor<0, 2>([&](auto n) LAM {
        if constexpr (d != 0) { wa[n.value] += d; PIN(wa[n.value]); }
        wf[k & 1][n.value] = trd2(wa[n.value] + (o & 65535), wa[n.value] + (o & 65535) + 2048);
      });
    };
    ready(IC<0>{}, IC<2 * (DPH - 1)>{});
    sfor<0, 28>([&](auto K) LAM {
      constexpr int k = K.value;
      if constexpr (k == KD) cnt_wait(9, 4);
      if constexpr (k + 1 < 28 && k != 17) ready(IC<k + 1>{}, IC<gemm_wait(k + 1)>{});
      sfor<0, 2>([&](auto n) LAM {
        sfor<0, 2>([&](auto m) LAM { acc[m.value][n.value] = mfma32(wf[k & 1][n.value], hfr[k % DPH][m.value], acc[m.value][n.value]); PIN(acc[m.value][n.value]); });
      });
      SBAR();
      if constexpr (k == 17) cnt_add(1);
      if constexpr (k + DPH < 28) hload(IC<k + DPH>{});
      sfor<9, 14>([&](auto B) LAM { if constexpr (wd_at(B.value) == k) wload(B); });
      if constexpr (k == 17) ready(IC<18>{}, IC<gemm_wait(18)>{});
      SBAR();
    });
    STAMP(7);
    sfor<1, 4>([&](auto x) LAM {
      const f32x16& a = acc[x.value & 1][x.value >> 1];
      sfor<0, 4>([&](auto i) LAM {
        *(LAS f32x4*)(IC<wd_blk(x.value - 1)>::value * 16384 + lg + i.value * 1024 + vl) = (f32x4){a[4 * i.value], a[4 * i.value + 1], a[4 * i.value + 2], a[4 * i.value + 3]};
      });
    });
    cnt_add(4);
    cnt_wait(4, 4);
    sfor<0, 4>([&](auto i) LAM {
      const u32 ra = i.value * 1024 + vl;
      const f32x4 a1 = lds128f(2 * 16384 + (gw ^ 1) * 4096 + ra), a2 = lds128f(3 * 16384 + (gw ^ 2) * 4096 + ra), a3_ = lds128f(4 * 16384 + (gw ^ 3) * 4096 + ra);
      f32x4 gv;
      sfor<0, 4>([&](auto j) LAM {
        const float f = (acc[0][0][4 * i.value + j.value] + a1[j.value]) + (a2[j.value] + a3_[j.value]);
        gv[j.value] = f * rcp(1.f + ex2(f * -1.4426950408889634f));
      });
      *(LAS f32x4*)(5 * 16384 + lg + ra) = gv;
    });
    asm volatile(".Lfb_endb:");
    cnt_add(5);
    STAMP(10);
    STAMP_END();
  }
}

extern "C" void kernel_launch(void* const* d_in, const int* in_sizes, int n_in, void* d_out, int out_size, void* d_ws,
                              size_t ws_size, hipStream_t stream) {
  fused_block<<<256, 512, 0, stream>>>(d_in[0], d_in[1], d_in[2], d_in[4], d_in[5], d_in[6], d_out, (char*)d_ws);
}
```

```cpp
#include <hip/hip_runtime.h>
#include <stdint.h>
#pragma clang diagnostic ignored "-Wint-to-pointer-cast"

#if defined(__HIP_DEVICE_COMPILE__)
#define NOPK __attribute__((target("no-packed-fp32-ops")))
#else
#define NOPK
#endif
#define DEV __device__ __forceinline__ NOPK
#define LAM __attribute__((always_inline)) NOPK
#define LAS __attribute__((address_space(3)))
#define GAS __attribute__((address_space(1)))

#define STAMPS 0

typedef unsigned u32;
typedef unsigned long long u64;
typedef u32 u32x2 __attribute__((ext_vector_type(2)));
typedef u32 u32x4 __attribute__((ext_vector_type(4)));
typedef short s16x4 __attribute__((ext_vector_type(4)));
typedef float f32x2 __attribute__((ext_vector_type(2)));
typedef float f32x4 __attribute__((ext_vector_type(4)));
typedef float f32x8 __attribute__((ext_vector_type(8)));
typedef float f32x16 __attribute__((ext_vector_type(16)));
typedef __bf16 bf16x8 __attribute__((ext_vector_type(8)));
typedef __bf16 bf16x16 __attribute__((ext_vector_type(16)));

extern "C" __device__ u64 llvm_dispatch_id() __asm("llvm.amdgcn.dispatch.id");

constexpr int S_ = 2048, D_ = 448, F_ = 1792;
#define V3 1
#define V4 1
#define FSQ 16
#define FSH 8
#define FSO 8
constexpr u32 WS_EPOCH = 0, WS_ST = 32768, WS_DBG = 65536;
constexpr u32 WS_Q = 1u << 20, WS_KV = 3u << 20, WS_O = 7u << 20, WS_H = 9u << 20;
constexpr u32 WS_CS = 16u << 20, WS_FO = WS_CS + (512u << 10), WS_FH = WS_FO + (256u << 10), WS_FQ = WS_FH + (256u << 10), WS_END = 18u << 20;
constexpr u32 BOX = FSQ == 16 ? 512 : 17 * FSQ;
constexpr u32 L_QK = 147456, L_CS = 159744, L_CNT = L_CS + 2560, L_DUM = L_CS + 2816, L_RV = L_CS + 3072;
constexpr u32 OOB = 0x7ffffff0;
constexpr u32 L_VT = L_QK;
constexpr u32 LDS_BYTES = 163840;

template <int V> struct IC { static constexpr int value = V; };
template <int I, int N, class Fn> DEV void sfor(Fn&& f) {
  if constexpr (I < N) { f(IC<I>{}); sfor<I + 1, N>(f); }
}

#define SBAR() __builtin_amdgcn_sched_barrier(0)
template <class T> DEV void PIN(T& x) { asm volatile("" : "+v"(x)); }
template <class T> DEV void nop_on(T& x) { asm volatile("s_nop 7" : "+v"(x)); }
template <class T> DEV void lgkm0_on(T& x) { asm volatile("s_waitcnt lgkmcnt(0)" : "+v"(x)::"memory"); }
DEV void colsum(f32x4& c0, f32x4& c1, u32x4 one, const u32x4* w, int who) {
#define CSM(k, c, r) "s_cmp_lg_u32 %8, " #k "\n\ts_cbranch_scc1 .Lcs" #r "_%=\n\tv_mfma_f32_16x16x32_bf16 %" #c ", %2, %" #r ", %" #c "\n.Lcs" #r "_%=:\n\t"
  asm volatile(CSM(0, 0, 3) CSM(1, 0, 4) CSM(2, 0, 5) CSM(3, 0, 6) CSM(0, 1, 7)
               : "+v"(c0), "+v"(c1) : "v"(one), "v"(w[0]), "v"(w[1]), "v"(w[2]), "v"(w[3]), "v"(w[4]), "s"(who) : "scc");
#undef CSM
}
DEV void mfma4_unless(f32x4* c, const u32x4* a, u32x4 b, int skip) {
  asm volatile("s_cmp_lg_u32 %9, 0\n\ts_cbranch_scc1 .Lm4_%=\n\tv_mfma_f32_16x16x32_bf16 %0, %4, %8, %0\n\tv_mfma_f32_16x16x32_bf16 %1, %5, %8, %1\n\t"
               "v_mfma_f32_16x16x32_bf16 %2, %6, %8, %2\n\tv_mfma_f32_16x16x32_bf16 %3, %7, %8, %3\n.Lm4_%=:"
               : "+v"(c[0]), "+v"(c[1]), "+v"(c[2]), "+v"(c[3]) : "v"(a[0]), "v"(a[1]), "v"(a[2]), "v"(a[3]), "v"(b), "s"(skip) : "scc");
}
template <int OFF> DEV void aload(u32x4& d, u32 voff, u32x4 srd, u32 soff) {
  asm volatile("buffer_load_dwordx4 %0, %1, %2, %3 offen offset:%4" : "=v"(d) : "v"(voff), "s"(srd), "s"(soff), "i"(OFF) : "memory");
}
template <int N> DEV void vm_on(u32x4& a) { asm volatile("s_waitcnt vmcnt(%1)" : "+v"(a) : "i"(N) : "memory"); }
DEV void aload_sc1(u32x4& d, u32 voff, u32x4 srd, u32 soff) {
  asm volatile("buffer_load_dwordx4 %0, %1, %2, %3 offen sc1" : "=v"(d) : "v"(voff), "s"(srd), "s"(soff) : "memory");
}
template <int N> DEV void vm_on2(u32x4& a, u32x4& b) { asm volatile("s_waitcnt vmcnt(%2)" : "+v"(a), "+v"(b) : "i"(N) : "memory"); }

DEV f32x16 mfma32(u32x4 a, u32x4 b, f32x16 c) {
  return __builtin_amdgcn_mfma_f32_32x32x16_bf16(__builtin_bit_cast(bf16x8, a), __builtin_bit_cast(bf16x8, b), c, 0, 0, 0);
}
template <bool FIRST> DEV void mac16(f32x4& c, u32x4 a, u32x4 b) {
  if constexpr (FIRST) asm volatile("v_mfma_f32_16x16x32_bf16 %0, %1, %2, 0" : "=&v"(c) : "v"(a), "v"(b));
  else asm volatile("v_mfma_f32_16x16x32_bf16 %0, %1, %2, %0" : "+v"(c) : "v"(a), "v"(b));
}
DEV f32x16 smf_lo(u32x4 a, f32x16 p, f32x16 c, int idx) {
  f32x8 b = __builtin_shufflevector(p, p, 0, 1, 2, 3, 4, 5, 6, 7);
  return __builtin_amdgcn_smfmac_f32_32x32x32_bf16(__builtin_bit_cast(bf16x8, a), __builtin_bit_cast(bf16x16, b), c, idx, 0, 0);
}
DEV f32x16 smf_hi(u32x4 a, f32x16 p, f32x16 c, int idx) {
  f32x8 b = __builtin_shufflevector(p, p, 8, 9, 10, 11, 12, 13, 14, 15);
  return __builtin_amdgcn_smfmac_f32_32x32x32_bf16(__builtin_bit_cast(bf16x8, a), __builtin_bit_cast(bf16x16, b), c, idx, 0, 0);
}
typedef __bf16 bf16x2 __attribute__((ext_vector_type(2)));
DEV u32 cvtpk(float lo, float hi) {
  f32x2 v = {lo, hi};
  return __builtin_bit_cast(u32, __builtin_convertvector(v, bf16x2));
}
DEV float asf(u32 u) { return __builtin_bit_cast(float, u); }
DEV u32 asu(float f) { return __builtin_bit_cast(u32, f); }
DEV float bfround(float f) { return asf(cvtpk(f, f) << 16); }
DEV float ex2(float x) { return __builtin_amdgcn_exp2f(x); }
DEV float rcp(float x) { return __builtin_amdgcn_rcpf(x); }
DEV float other_half(float v) {
  auto r = __builtin_amdgcn_permlane32_swap(asu(v), asu(v), false, false);
  return asf(r[0] ^ r[1] ^ asu(v));
}
DEV u32x4 mk_srd(const void* p, u32 bytes) {
  u64 a = (u64)p;
  u32x4 r = {(u32)a, (u32)(a >> 32) & 0xffffu, bytes, 0x00020000u};
  return r;
}
template <int LD> DEV void dma_half(u32x4 srd, u32 voff, u32 so, u32 l0) {
#define EXH "0"
  asm volatile("s_add_u32 m0, %0, %1\n\ts_mov_b32 exec_hi, " EXH "\n\tbuffer_load_dwordx4 %2, %3, %4 offen lds\n\ts_mov_b32 exec_hi, -1"
               ::"s"(l0), "i"(LD), "v"(voff), "s"(srd), "s"(so) : "memory", "scc");
}
template <int LD, int OFF> DEV void dma1(u32x4 srd, u32 voff, u32 so, u32 l0) {
  asm volatile("s_add_u32 m0, %0, %1\n\ts_nop 0\n\tbuffer_load_dwordx4 %2, %3, %4 offen offset:%5 lds"
               ::"s"(l0), "i"(LD), "v"(voff), "s"(srd), "s"(so), "i"(OFF) : "memory", "scc");
}
template <int LD, int OFF> DEV void dma2(u32x4 s0, u32 v0, u32x4 s1, u32 v1, u32 so, u32 l0) {
  asm volatile("s_add_u32 m0, %0, %1\n\ts_nop 0\n\tbuffer_load_dwordx4 %2, %3, %6 offen offset:%7 lds\n\tbuffer_load_dwordx4 %4, %5, %6 offen offset:%8 lds"
               ::"s"(l0), "i"(LD), "v"(v0), "s"(s0), "v"(v1), "s"(s1), "s"(so), "i"(OFF), "i"(OFF + 1024) : "memory", "scc");
}
DEV void dma_1280(u32x4 srd, u32 voff, u32 so, u32 l0) {
  asm volatile("s_mov_b32 m0, %0\n\ts_nop 0\n\tbuffer_load_dwordx4 %1, %2, %3 offen sc1 lds\n\ts_mov_b64 exec, 0xffff\n\t"
               "buffer_load_dwordx4 %1, %2, %3 offen offset:1024 sc1 lds\n\ts_mov_b64 exec, -1" ::"s"(l0), "v"(voff), "s"(srd), "s"(so) : "memory");
}
DEV u32 opaque(u32 x) { asm volatile("" : "+s"(x)); return x; }
template <int LD, int ADD> DEV void dma_i(u32x4 srd, u32 voff, u32 s0, u32 l0) {
  u32 t;
  asm volatile("s_add_u32 m0, %1, %2\n\ts_add_u32 %0, %3, %4\n\tbuffer_load_dwordx4 %5, %6, %0 offen lds"
               : "=&s"(t) : "s"(l0), "i"(LD), "s"(s0), "i"(ADD), "v"(voff), "s"(srd) : "memory", "scc");
}
DEV int lane_id() {
  u32 z = 0;
  asm volatile("" : "+s"(z));
  return __builtin_amdgcn_mbcnt_hi(~0u, __builtin_amdgcn_mbcnt_lo(~0u, z));
}
#define LANES() const int lane = lane_id(), r32 = lane & 31, hf = lane >> 5, blk = (lane >> 4) & 1, q4 = (lane & 15) >> 2, p4 = lane & 3; \
  const u32 vl = lane * 16; (void)r32; (void)hf; (void)blk; (void)q4; (void)p4; (void)vl
DEV void touch(const void* p) {
  asm volatile("s_mov_b32 m0, %0\n\ts_nop 0\n\tglobal_load_lds_dword %1, off" ::"i"(L_DUM), "v"(p) : "memory");
}
DEV void touch_b(u32x4 srd, u32 voff, u32 so) {
  asm volatile("s_mov_b32 m0, %0\n\ts_nop 0\n\tbuffer_load_dword %1, %2, %3 offen lds" ::"i"(L_DUM), "v"(voff), "s"(srd), "s"(so) : "memory");
}
template <int N> DEV void vmwait() { asm volatile("s_waitcnt vmcnt(%0)" ::"i"(N) : "memory"); }
DEV void cfence() { asm volatile("" ::: "memory"); }
DEV void cnt_add(int i) {
  asm volatile("s_mov_b64 exec, 1\n\tds_inc_u32 %0, %0\n\ts_mov_b64 exec, -1" ::"v"(L_CNT + 4 * i) : "memory");
}
DEV u32 cnt_get(int i) { return __hip_atomic_load((LAS u32*)(L_CNT + 4 * i), __ATOMIC_RELAXED, __HIP_MEMORY_SCOPE_WORKGROUP); }
DEV void cnt_wait(int i, u32 n) {
  while (__builtin_amdgcn_readfirstlane(cnt_get(i)) < n) __builtin_amdgcn_s_sleep(1);
  cfence();
}
DEV u32x2 trd(u32 a) { return __builtin_bit_cast(u32x2, __builtin_amdgcn_ds_read_tr16_b64_v4i16((LAS s16x4*)a)); }
DEV u32x4 trd2(u32 a0, u32 a1) { u32x2 x = trd(a0), y = trd(a1); u32x4 r = {x[0], x[1], y[0], y[1]}; return r; }
DEV u32x4 lds128(u32 a) { return *(LAS u32x4*)a; }
DEV f32x4 lds128f(u32 a) { return *(LAS f32x4*)a; }
template <int N> DEV void sync_stage() { asm volatile("s_waitcnt vmcnt(%0) lgkmcnt(0)\n\ts_barrier" ::"i"(N) : "memory"); }
template <int I> DEV void wlane(u32& f, u32 v) { asm volatile("v_writelane_b32 %0, %1, %2" : "+v"(f) : "s"(v), "i"(I)); }
typedef __amdgpu_buffer_rsrc_t rsrc_t;
DEV void st128(rsrc_t r, u32x4 v, u32 voff, u32 soff) {
  __builtin_amdgcn_raw_buffer_store_b128(v, r, voff, __builtin_amdgcn_readfirstlane(soff), 16);
  asm volatile("s_nop 1" ::"v"(v) : "memory");
}
DEV void st128p(rsrc_t r, u32x4 v, u32 voff) {
  __builtin_amdgcn_raw_buffer_store_b128(v, r, voff, 0, 16);
}
DEV void aflag(u64& d, const char* p) { asm volatile("global_load_dwordx2 %0, %1, off sc1" : "=v"(d) : "v"(p) : "memory"); }
template <int N> DEV void vm_on1(u64& a) { asm volatile("s_waitcnt vmcnt(%1)" : "+v"(a) : "i"(N) : "memory"); }
DEV void swap32(u32& a, u32& b) {
  auto r = __builtin_amdgcn_permlane32_swap(a, b, false, false);
  a = r[0]; b = r[1];
}
DEV void swap16(u32& a, u32& b) {
  auto r = __builtin_amdgcn_permlane16_swap(a, b, false, false);
  a = r[0]; b = r[1];
}
DEV u32 from_lane(int l, u32 v) { return __builtin_amdgcn_ds_bpermute(l * 4, v); }
DEV u32x4 ld128(rsrc_t r, u32 voff, u32 soff) { return __builtin_amdgcn_raw_buffer_load_b128(r, voff, __builtin_amdgcn_readfirstlane(soff), 16); }
DEV void put_flag(char* ws, u32 off, u64 tag) { __hip_atomic_store((GAS u64*)(ws + off), tag, __ATOMIC_RELAXED, __HIP_MEMORY_SCOPE_AGENT); }
template <int NAP> DEV void poll_flags(char* ws, u32 off, u64 tag) {
#pragma nounroll
  for (u32 spins = 0; spins < (1u << 20); ++spins) {
    u64 v = __hip_atomic_load((GAS u64*)(ws + off), __ATOMIC_RELAXED, __HIP_MEMORY_SCOPE_AGENT);
    if (__builtin_amdgcn_ballot_w64(v != tag) == 0) break;
    __builtin_amdgcn_s_sleep(NAP);
  }
  __builtin_amdgcn_fence(__ATOMIC_ACQUIRE, "wavefront");
  asm volatile("" ::: "memory");
}
DEV float wave_max_nonneg(float v) {
  u32 x = asu(v);
#define DPPMAX(ctrl) { u32 y = __builtin_amdgcn_update_dpp(0u, x, ctrl, 0xf, 0xf, true); x = x > y ? x : y; }
  DPPMAX(0x111) DPPMAX(0x112) DPPMAX(0x114) DPPMAX(0x118) DPPMAX(0x142) DPPMAX(0x143)
  return asf(__builtin_amdgcn_readlane(x, 63));
}
DEV float max8_nonneg(float v) {
  u32 x = asu(v);
  DPPMAX(0x111) DPPMAX(0x112) DPPMAX(0x114)
#undef DPPMAX
  return asf(__builtin_amdgcn_readlane(x, 7));
}

DEV void widen(float& lo, float& hi, u32 x) {
  asm volatile("v_lshlrev_b32 %0, 16, %2\n\tv_and_b32 %1, 0xffff0000, %2" : "=&v"(lo), "=v"(hi) : "v"(x));
}
DEV float half_sum(float v) {
#define DPPADD(ctrl) v += asf(__builtin_amdgcn_update_dpp(0u, asu(v), ctrl, 0xf, 0xf, true));
  DPPADD(0x111) DPPADD(0x112) DPPADD(0x114) DPPADD(0x118) DPPADD(0x142)
#undef DPPADD
  return v;
}
DEV u64 stale(u32x4 f, u64 tag) { return __builtin_amdgcn_ballot_w64(f[1] != (u32)tag) | __builtin_amdgcn_ballot_w64(f[3] != (u32)(tag >> 32)); }
DEV bool fresh(u32x4 f, u64 tag) { return stale(f, tag) == 0; }
DEV void await(u32x4& f, u32 voff, u32x4 srd, u32 soff, u64 tag) {
#pragma nounroll
  for (u32 spins = 0; !fresh(f, tag) && spins < (1u << 20); ++spins) {
    __builtin_amdgcn_s_sleep(1);
    aload_sc1(f, voff, srd, soff);
    vm_on<0>(f);
  }
}

#define STAMP(i) do {} while (0)
#define STAMPF(i) do {} while (0)
#define STAMP_END() do {} while (0)

#define ROT1 2
#define PAT 0
#define DPH 10
#define PRIO 1
#define NR 4
#define NAPB 4
#define WE 5
#define HP 4
#define PRB 0
#define HCNT 4
#define UPF 2
#define WS 1
#define WT 2
#define NAPA 1
#define NAPO 2
#define G0 1
#define WA 4
#define TJ1 1
#define TW 1
constexpr int TWM = (TW & 1 ? 4 << TJ1 : 0) | (TW & 2 ? 0x2a0 : 0);
#define KD 18
#define ID 60

constexpr int piece_at(int T, int j, int i) {
  constexpr int at[2][2][7] = {{{2, 5, 8, 11, 14, 16, 18}, {6, 11, 16, 19, 5, 14, 18}}, {{1, 3, 6, 8, 11, 13, 16}, {5, 9, 13, 16, 5, 11, 16}}};
  for (int p = j == 0 || (j & 1) ? 0 : 4; p < (j & 1 ? 4 : 7); ++p) if (at[T][j != 0][p] == i) return p;
  return -1;
}
constexpr int wd_blk(int b) { return (int)(0x610875432ull >> (4 * (b % 9))) & 15; }
constexpr u32 wd_loc(int k) { return wd_blk(k / 2) * 16384 + (k & 1) * 1024; }

constexpr int wd_at(int b) { return WS + WT * (b - 9); }
constexpr int gemm_wait(int k) {
  int n = 0, last = 0;
  const int end = k == 18 ? 18 : k - 1;
  for (int i = -DPH; i < end; ++i) {
    if (i + DPH < 28) { n += 2; if (i + DPH == k) last = n; }
    for (int b = 9; b < 14; ++b) if (wd_at(b) == i) { n += 4; if (k / 2 == b) last = n; }
  }
  return n - last < 63 ? n - last : 63;
}
static_assert(DPH == 10 && wd_at(13) <= 17 && wd_at(9) >= 1 && wd_at(13) >= 9);
typedef u32 u32x16 __attribute__((ext_vector_type(16)));
DEV u32x4 mk_srd2(u32 lo, u32 hi, u32 bytes) { return (u32x4){lo, hi & 0xffffu, bytes, 0x00020000u}; }

__global__ __launch_bounds__(512, 2) NOPK void fused_block(const void*, const void*, const void*, const void*, const void*, const void*,
                                                           void*, char*) {
  __shared__ __attribute__((aligned(16))) char smem[LDS_BYTES];
  u32x16 ka;
  asm volatile("s_load_dwordx16 %0, %1, 0x0" : "=s"(ka) : "s"(__builtin_amdgcn_kernarg_segment_ptr()));
  SBAR();
  const int tid = __builtin_amdgcn_workitem_id_x();
  const int w = __builtin_amdgcn_readfirstlane(tid >> 6);
  const int bid = __builtin_amdgcn_workgroup_id_x();
  const int xcd = bid & 7, bi = bid >> 3;
  u64 pc0;
  u32 clen;
  asm volatile("s_getpc_b64 %0\n.Lfb_pc0:\n\ts_max_u32 %1, .Lfb_enda-.Lfb_pc0, 0\n\ts_max_u32 %1, .Lfb_endb-.Lfb_pc0, %1" : "=s"(pc0), "=s"(clen)::"scc");
  {
    const u32 o = (bi * 16 + w * 2 + (tid & 1)) * 128;
    touch((const char*)pc0 + (o < clen ? o : clen));
  }
  SBAR();
  const int rb = 4 * (xcd & 1) + (bi & 3), quad = xcd >> 1, cl = bi >> 2, cb = 8 * quad + cl;
  const bool t1 = cl == 7;
  const int mg = w & 3, g = w >> 2, un = 7 * quad + cl;
  const u32 rot = 2 * (bi & 3);
  const int rr = (tid >> 3) & 7, ch = tid & 7, odd = (tid >> 5) & 2;
  u32 vx0 = rr * 896 + ((ch ^ (rr >> 1)) << 4), vx1 = (vx0 ^ 64) + 6144;
  u32 ox = (rb * 8 + w) * (32 * 896), lw = w * 1024, so0 = ox + rot * 128;
  asm volatile("s_waitcnt lgkmcnt(0)" : "+s"(ka), "+v"(vx0), "+v"(vx1), "+s"(so0), "+s"(lw));
  STAMPF(3);
  const u32x4 sX = mk_srd2(ka[0], ka[1], S_ * D_ * 2);
  dma2<0, 0>(sX, vx0, sX, vx1, so0, 4 * lw);
  dma2<0, 2048>(sX, vx0, sX, vx1, so0 + 12288, 4 * lw);
  SBAR();
  const u32 wst = t1 ? 2688 : 3584, cw = ch ^ ((rr & 2) << 1);
  const u32 cv = (tid & 15) ^ ((tid >> 2) & 12) ^ odd;
  const u32 va = t1 ? (rr >> 1) * 2688 + (cv >= 14 ? cv - 2 : cv) * 16 : rr * 3584 + (cw ^ odd) * 16;
  const u32 ow = 8 * w * wst + (t1 ? 1792 + 224 * quad : un * 128);
  const u32 vq = ((tid >> 2) & 7) * 2688 + ((tid & 3) ^ odd) * 16;
  const u32 oq = w * (8 * 2688) + ((56 * cb) & ~15);
  const u64 pb_ = (t1 ? ((u64)ka[3] << 32 | ka[2]) + 4 * 2688 : ((u64)ka[9] << 32 | ka[8])) - 1024;
  const u32x4 sA = mk_srd2(t1 ? ka[2] : ka[6], t1 ? ka[3] : ka[7], 1u << 21), sB = mk_srd2((u32)pb_, (u32)(pb_ >> 32), 1u << 21);
  const u32x4 sQ = mk_srd2(ka[2], ka[3], D_ * 3 * D_ * 2);
  u32 kst = rot, so = 0;
  auto piece = [&](auto H_, auto I_) LAM {
    constexpr int H = H_.value, i = I_.value;
    if constexpr (i == 0) {
      if constexpr (H > 0) { kst = opaque(kst) + 1; kst = kst == 7 ? 0 : kst; }
      so = ow + kst * (64 * wst);
    }
    if constexpr (i < 2) dma1<(6 + H % 3) * 16384, i * 1024>(i ? sB : sA, va, so, 2 * lw);
    if constexpr (i == 2) dma_half<L_QK + (H % 3) * 4096>(sQ, vq, oq + opaque(kst) * (64 * 2688), lw >> 1);
    if constexpr (i == 3) so = ox + opaque(kst) * 128;
    if constexpr (i == 5) so = opaque(so) + 12288;
    if constexpr (i >= 3) dma1<(H % 3) * 32768, (i - 3) * 1024>(sX, (i & 1) ? vx0 : vx1, so, 4 * lw);
  };
  sfor<0, 3>([&](auto i) LAM { piece(IC<0>{}, i); });
  asm volatile("s_barrier" ::: "memory");
  STAMPF(4);
  u32x4 xr[2];
  const u32 row = rb * 256 + cb * 8 + 2 * mg;
  {
    const u32 vr = (tid & 31) < 28 * g ? (tid & 31) * 16 + (tid & 32) * 28 : OOB;
    aload<0>(xr[0], vr, sX, row * 896);
    aload<448>(xr[1], vr, sX, row * 896);
  }
  char* const ws = (char*)((u64)ka[15] << 32 | ka[14]);
  u64 epoch;
  asm volatile("s_load_dwordx2 %0, %1, 0x0" : "=s"(epoch) : "s"(ws));
  asm volatile("" ::"v"((u32)(uintptr_t)(LAS char*)smem));
  const char* const px = (const char*)((u64)ka[1] << 32 | ka[0]);
  const char* const pwo = (const char*)((u64)ka[5] << 32 | ka[4]);
  char* const pout = (char*)((u64)ka[13] << 32 | ka[12]);
  const u32x4 sD = mk_srd2(ka[10], ka[11], F_ * D_ * 2), sWS = mk_srd2(ka[14], ka[15], WS_END);
  const rsrc_t rWS = __builtin_amdgcn_make_buffer_rsrc(ws, 0, WS_END, 0x00020000);
  if (tid < 16) *(LAS u32*)(L_CNT + tid * 4) = 0;
  const bool has_tile = !t1;
  const int rq = bi / 7, rt = 4 * xcd + rq, nt = bi - 7 * rq;
  const int rotg = (4 * nt + 8 * rq) % 28;
  const int head = 2 * xcd + (bi >> 4), qb = bi & 15;
  const u32 vw0 = rr * 896 + cw * 16, vw1 = vw0 + 6144;
  auto krot = [&](u32 k) LAM -> u32 { k += opaque(rotg); return k >= 28 ? k - 28 : k; };
  const u32 od = 448 * mg * 896 + nt * 128;

  asm volatile("s_waitcnt lgkmcnt(0)" : "+s"(epoch));
  u64 tag = (epoch + 0x9E3779B97F4A7C15ull) * 0xBF58476D1CE4E5B9ull;
  tag = (tag ^ (tag >> 29)) + llvm_dispatch_id() * 0x94D049BB133111EBull;
  tag = (tag ^ (tag >> 31)) * 0xD6E8FEB86659FD93ull & 0xBFFFBFFFBFFFBFFFull;
  asm volatile("" : "+s"(tag));
  float rs[4];
  f32x4 acc[5][4];
  auto geglu = [&](auto M) LAM -> u32x4 {
    constexpr int m = M.value;
    const float r2 = rs[m] * rs[m], K1 = (-2.f * 0.7978845608028654f * 1.4426950408889634f) * rs[m], K2 = K1 * 0.044715f * r2, IR2 = rcp(r2);
    u32x4 hp;
    sfor<0, 4>([&](auto j) LAM {
      constexpr int p = 1 + (j.value & 2), e = 2 * (j.value & 1);
      float y[2];
      sfor<0, 2>([&](auto i) LAM {
        const float t = acc[p][m][e + i.value];
        y[i.value] = t * acc[p + 1][m][e + i.value] * rcp(__builtin_fmaf(ex2(t * __builtin_fmaf(t * t, K2, K1)), IR2, IR2));
      });
      hp[j.value] = cvtpk(y[0], y[1]);
    });
    return hp;
  };
  auto frag_off = [&]() LAM -> u32 { const u32 l = lane_id(); return (l >> 4) * 512 + (l & 15) * 16; };
  u32x4 hp[4];

  auto phase1 = [&](auto T_) LAM {
    constexpr int T = T_.value;
    u32 xa[2][2], fa[4], qa, vl_, vcs, bt, vqk, vv[4];
    float qs;
    const bool isq = cb < 16;
    {
      const int lane = lane_id(), g4 = lane >> 4, tok = lane & 15, q4 = tok >> 2, p4 = lane & 3;
      xa[0][0] = (mg * 64 + tok) * 128 + ((g4 ^ (tok >> 1)) << 4);
      xa[0][1] = xa[0][0] ^ 64;
      xa[1][0] = xa[0][0] + 65536;
      xa[1][1] = xa[0][1] + 65536;
      sfor<0, 4>([&](auto t) LAM {
        if constexpr (T == 0) fa[t.value] = 6 * 16384 + g4 * 2048 + q4 * 128 + ((64 * g + 16 * (t.value & 2)) ^ ((q4 & 2) << 5) ^ ((g4 & 1) << 5)) + 8 * p4;
        else fa[t.value] = 6 * 16384 + g4 * 2048 + q4 * 256 + ((128 * g + 32 * t.value + 8 * p4) ^ (q4 * 64) ^ ((g4 & 1) << 5));
      });
      qa = L_QK + g4 * 512 + q4 * 64 + ((((cb & 1) * 8 + 32 * g + 8 * p4) & 63) ^ ((g4 & 1) << 5));
      PIN(xa[0][0]); PIN(xa[0][1]); PIN(xa[1][0]); PIN(xa[1][1]); PIN(fa[0]); PIN(fa[2]); PIN(qa);
      if constexpr (T == 1) { PIN(fa[1]); PIN(fa[3]); }
      vl_ = lane * 16;
      bt = tok * 4;
      vcs = mg * 1024 + g4 * 4096 + tok * 16;
      vcs = lane < (mg == 0 ? 32 : 16) ? vcs : OOB;
      vqk = isq ? vl_ : (vl_ & 0x230u) | ((vl_ & 0xc0u) << 1) | ((vl_ & 0x100u) >> 2);
      qs = g == 1 && lane >= 48 ? 0.f : isq ? 0.18898223650461362f * 1.4426950408889634f : 1.f;
      PIN(vl_); PIN(bt); PIN(vcs); PIN(vqk); PIN(qs);
      if constexpr (T == 1) sfor<0, 4>([&](auto t) LAM {
        const u32 c = 64 * g + 16 * t.value + tok, hd = (c * 147) >> 12;
        vv[t.value] = hd * (64 * 4096 - 28 * 16) + c * 16 + g4 * 512;
        PIN(vv[t.value]);
        acc[4][t.value] = (f32x4){};
        PIN(acc[4][t.value]);
      });
    }
    u32x4 one = {0x3f803f80u, 0x3f803f80u, 0x3f803f80u, 0x3f803f80u};
    PIN(one);
    u32x4 xf[2][4], wf[2][5], sg, cf[5], nm[4];
    f32x4 c0 = {}, c1 = {};
    PIN(c0); PIN(c1);
    const u32 scs = WS_CS + (cb * 2 + g) * 5120, sst = WS_ST + (rb * 256 + mg * 64) * 16;
    f32x4 rv[4];
    float rl;
    u32 nl;

    SBAR();
    sfor<0, 7>([&](auto i) LAM { piece(IC<1>{}, i); });
    sync_stage<7>();
    STAMP(1);
    auto rd = [&](auto J, auto R) LAM {
      constexpr int j = J.value, r = R.value, sl = (j / 2) % 3, s = j & 1;
      if constexpr (r >= 2 && r < 6) xf[j & 1][r - 2] = lds128(xa[sl >> 1][s] + (sl & 1) * 32768 + (r - 2) * 2048);
      else {
        constexpr int t = r < 2 ? 0 : (r - 4) / 2, h = r & 1;
        u32x2 v;
        if constexpr (t == 0) v = trd(qa + sl * 4096 + s * 2048 + h * 256);
        else if constexpr (T == 0) v = trd(fa[(t - 1) & 2] + sl * 16384 + s * 8192 + ((t - 1) & 1) * 1024 + h * 512);
        else v = trd(fa[t - 1] + sl * 16384 + s * 8192 + h * 1024);
        wf[j & 1][t][2 * h] = v[0];
        wf[j & 1][t][2 * h + 1] = v[1];
      }
    };
    sfor<0, 14>([&](auto r) LAM { rd(IC<0>{}, r); });
    SBAR();
    {
      u32 d = 0, vs = OOB;
      if (g != 0) {
        float a[16], t = 0.f, q = 0.f;
        sfor<0, 8>([&](auto i) LAM {
          widen(a[2 * i.value], a[2 * i.value + 1], xr[i.value >> 2][i.value & 3]);
          t += a[2 * i.value] + a[2 * i.value + 1];
        });
        t = half_sum(t) * (1.f / 448.f);
        const float m0 = asf(__builtin_amdgcn_readlane(asu(t), 31)), m1 = asf(__builtin_amdgcn_readlane(asu(t), 63)), mean = vl_ < 512 ? m0 : m1;
        sfor<0, 16>([&](auto i) LAM { const float e = a[i.value] - mean; q = __builtin_fmaf(e, e, q); });
        const float var = half_sum((vl_ & 511) < 448 ? q : 0.f) * (1.f / 448.f);
        d = cvtpk(-mean, __builtin_amdgcn_sqrtf(bfround(bfround(var) + 1e-5f)));
        vs = (vl_ & 511) == 496 ? vl_ >> 5 & 16 : OOB;
      }
      st128(rWS, (u32x4){d, (u32)tag, d, (u32)(tag >> 32)}, vs, WS_ST + row * 16);
    }
    SBAR();
    u32 P[4];
    auto qk_put = [&](auto M) LAM {
      constexpr int m = M.value;
      const float sc = rs[m] * qs;
      sfor<0, 4>([&](auto e) LAM { acc[0][m][e.value] *= sc; });
      P[2 * (m & 1)] = cvtpk(acc[0][m][0], acc[0][m][1]);
      P[2 * (m & 1) + 1] = cvtpk(acc[0][m][2], acc[0][m][3]);
      if constexpr (m & 1) {
        swap16(P[0], P[2]);
        swap16(P[1], P[3]);
        const u32 tl = (cb & 15) * 64 + rb * 8 + mg * 2 + (m >> 1);
        st128(rWS, (u32x4){P[0], P[1], P[2], P[3]}, vqk, (isq ? WS_Q + tl * 2048 : WS_KV + tl * 4096) + g * 1024);
      } else { PIN(P[0]); PIN(P[1]); }
    };
    auto v_put = [&](auto I) LAM {
      constexpr int t = 1 + I.value / 2, u = I.value & 1;
      u32x4 V;
      sfor<0, 4>([&](auto e) LAM {
        constexpr int m = 2 * u + e.value / 2, c = 2 * (e.value & 1);
        V[e.value] = cvtpk(acc[t][m][c] * rv[m][c], acc[t][m][c + 1] * rv[m][c + 1]);
      });
      st128(rWS, V, vv[t - 1], WS_KV + (quad * 256 + rb * 8 + mg * 2 + u) * 4096 + 2048);
    };
    sfor<0, 14>([&](auto J) LAM {
      constexpr int j = J.value, G = j / 2;
      sfor<0, 20>([&](auto I) LAM {
        constexpr int i = I.value, t = i / 4, m = i % 4;
        if constexpr (T == 1 && t == 4) {
          if constexpr (m == 0) mfma4_unless(acc[4], xf[j & 1], wf[j & 1][4], g);
        } else {
          if constexpr (T == 1 && t > 0) mac16<j == 0>(acc[t][m], xf[j & 1][m], wf[j & 1][t]);
          else mac16<j == 0>(acc[t][m], wf[j & 1][t], xf[j & 1][m]);
        }
        SBAR();
        if constexpr ((j & 1) && i == 1 && j < 13) sync_stage<(G == 5 ? 4 - 3 * T : 8 - (G & 1)) + (TWM >> j & 1)>();
        if constexpr ((j & 1) && i == 1 && j < 13) STAMPF(5 + G);
        if constexpr (i == 1 && (TWM >> (j + 2) & 1)) {
          if constexpr (j == TJ1) touch_b(sD, vl_ < 7 * 16 ? vl_ * 8 : OOB, (bid * 8 + w) * (7 * 128));
          else touch_b(sD, vl_ < 17 * 16 ? vl_ * 8 : OOB, ((bi * 8 + w) * 3 + (j - 3) / 2) * (17 * 128));
        }
        constexpr int r = i - 2 * (j & 1);
        if constexpr (j < 13 && r >= 0 && r < 14) rd(IC<j + 1>{}, IC<(r >= 0 && r < 14 ? r : 0)>{});
        if constexpr (j < 4 && i == 3) colsum(c0, c1, one, wf[j & 1], (0xf0700e0fu >> (8 * j + rb)) & 1 ? mg : 4);
        if constexpr (j == 3 && i == 8) {
          const float a0 = c0[0], a1 = c1[0], v = vl_ < 256 ? a0 : a1, hi = bfround(v);
          __builtin_amdgcn_raw_buffer_store_b64((u32x2){cvtpk(hi, v - hi), (u32)(tag >> (32 * (rb & 1)))}, rWS, vcs, scs + (rb >> 1) * 256 + (rb & 1) * 8, 16);
        }
        constexpr int pc = piece_at(T, j, i), H = (j + 5) / 2;
        if constexpr (pc >= 0 && H < 7) piece(IC<H>{}, IC<(pc >= 0 ? pc : 0)>{});
        if constexpr (T == 0 && (j == 9 || j == 11) && pc >= 0) {
          if constexpr (pc == 0) so = od + krot(2 * (j - 9) + 2 * g) * 14336;
          if constexpr (pc == 2) so = opaque(so) + 12288;
          dma1<(j - 7) * 16384, pc * 1024>(sD, (pc & 1) ? vw1 : vw0, so, 4 * lw);
        }
        if constexpr (j == 9 && i == 0) aload_sc1(sg, vl_, sWS, sst);
        if constexpr (j == 9 && i == 1) dma_1280(sWS, vl_, scs + mg * 1280, 7 * 16384 + w * 1280);
        if constexpr (T == 1 && j == 9 && i == 8) st128(rWS, one, (vl_ >> 8) * (64 * 4096) + (vl_ & 0xc0) * 8 + (vl_ & 48) + 448, WS_KV + (quad * 256 + rb * 8 + w) * 4096 + 2048);
        if constexpr (j == 11 && i == 0) { vcs = 7 * 16384 + g * 5120 + vl_; PIN(vcs); }
        if constexpr (j == 11 && i == 1) sfor<0, 5>([&](auto u) LAM { cf[u.value] = lds128(vcs + u.value * 1024); });
        if constexpr (j == 10 && i >= 14 && i < 18) { nm[i - 14] = (u32x4){}; PIN(nm[i - 14]); }
        if constexpr (j == 11 && i == 6) {
          __builtin_amdgcn_s_setprio(2);
          PIN(sg);
          sfor<0, 5>([&](auto u) LAM { PIN(cf[u.value]); });
#pragma nounroll
          for (u32 spins = 0; spins < (1u << 20); ++spins) {
            if ((stale(sg, tag) | stale(cf[0], tag) | stale(cf[1], tag) | stale(cf[2], tag) | stale(cf[3], tag) | stale(cf[4], tag)) == 0) break;
            aload_sc1(sg, vl_, sWS, sst);
            sfor<0, 5>([&](auto u) LAM { aload_sc1(cf[u.value], vl_, sWS, scs + u.value * 1024); });
            vm_on2<0>(sg, cf[0]); vm_on2<0>(cf[1], cf[2]); vm_on2<0>(cf[3], cf[4]);
          }
        }
        if constexpr (j == 11 && i == 9) {
          rl = rcp(asf(sg[0] & 0xffff0000u));
          nl = __builtin_amdgcn_perm(sg[0], sg[0], 0x01000100u);
          if constexpr (T == 1) *(LAS float*)(L_RV + mg * 256 + (vl_ >> 2)) = rl;
        }
        if constexpr (j == 11 && i >= 16) {
          nm[i - 16][0] = __builtin_amdgcn_ds_bpermute(bt + 64 * (i - 16), nl);
          PIN(bt);
          nm[i - 16][2] = __builtin_amdgcn_ds_bpermute(bt + 64 * (i - 16), nl);
        }
        if constexpr (j == 12) {
          constexpr int tc = (i + 10) % 20 / 4, mc = (i + 10) % 4;
          SBAR();
          if constexpr (T == 1 && tc > 0) mac16<false>(acc[tc][mc], nm[mc], cf[tc]);
          else mac16<false>(acc[tc][mc], cf[tc], nm[mc]);
          SBAR();
          if constexpr (i >= 14 && i < 18) {
            rs[i - 14] = asf(__builtin_amdgcn_ds_bpermute(bt + 64 * (i - 14), asu(rl)));
            if constexpr (T == 1 && i == 14) { vcs = L_RV + mg * 256 + ((vl_ >> 4) & 48); PIN(vcs); }
            if constexpr (T == 1) rv[i - 14] = lds128f(vcs + 64 * (i - 14));
          }
        }
        if constexpr (j == 13) {
          if constexpr (i == 0) STAMPF(11);
          if constexpr (T == 0 && i == 9) __builtin_amdgcn_s_setprio(1);
          if constexpr (i >= 5 && i < 9) qk_put(IC<(i >= 5 && i < 9 ? i - 5 : 0)>{});
          if constexpr (T == 1 && i >= 9 && (i & 1)) v_put(IC<(i >= 9 ? (i - 9) / 2 : 0)>{});
        }
        SBAR();
      });
    });
    asm volatile("s_nop 7");
    SBAR();
    STAMP(2);
    if constexpr (T == 1) {
      if (g == 0) { v_put(IC<6>{}); v_put(IC<7>{}); }
    }
    STAMP(3);
    __builtin_amdgcn_s_setprio(1);
    SBAR();
    LANES();
    float km;
    {
      u32 n[4];
      sfor<0, 4>([&](auto m) LAM {
        const f32x4 a = acc[0][m.value];
        n[m.value] = asu(__builtin_fmaf(a[0], a[0], a[1] * a[1]) + __builtin_fmaf(a[2], a[2], a[3] * a[3]));
      });
      swap16(n[0], n[1]);
      swap16(n[2], n[3]);
      n[0] = asu(asf(n[0]) + asf(n[1]));
      n[2] = asu(asf(n[2]) + asf(n[3]));
      swap32(n[0], n[2]);
      km = wave_max_nonneg(asf(n[0]) + asf(n[2]));
      PIN(km);
    }
    auto qk_flag = [&]() LAM {
      u32 n = 0;
      cfence();
      if (lane == 0) {
        __hip_atomic_fetch_max((LAS u32*)(L_CNT + 40 + 4 * g), asu(km), __ATOMIC_RELAXED, __HIP_MEMORY_SCOPE_WORKGROUP);
        n = __hip_atomic_fetch_add((LAS u32*)L_CNT, 1u, __ATOMIC_RELAXED, __HIP_MEMORY_SCOPE_WORKGROUP);
      }
      cfence();
      if (__builtin_amdgcn_readfirstlane(n) == 7) {
        const u32 k2 = asu(asf(cnt_get(10)) + asf(cnt_get(11)));
        const u32x4 f = {k2, (u32)tag, k2, (u32)(tag >> 32)};
        if (lane < (cb < 16 ? 2 : 16)) st128(rWS, f, lane * BOX, WS_FQ + (cb < 16 ? (cb * 16 + 2 * rb) * BOX + 16 * FSQ : (cb - 16) * (16 * BOX) + rb * FSQ));
        if constexpr (T == 1) st128(rWS, f, lane * BOX, WS_FQ + quad * (64 * BOX) + (8 + rb) * FSQ);
      }
      STAMP(13);
    };
    if constexpr (T == 0 && G0) { hp[0] = geglu(IC<0>{}); PIN(hp[0]); }
    vmwait<0>();
    qk_flag();
    __builtin_amdgcn_s_setprio(0);
    SBAR();
    if constexpr (T == 0) sfor<G0, 4>([&](auto m) LAM {
      if constexpr (m.value >= 2) {
        if constexpr (m.value == 2) cnt_wait(0, 8);
        const u32 so = od + krot(4 + 4 * g + 2 * m.value) * 14336, l0 = mg * 4096 + (g ? 0 : 7 * 16384);
        dma2<(m.value - 2) * 16384, 0>(sD, vw0, sD, vw1, so, l0);
        dma2<(m.value - 2) * 16384, 2048>(sD, vw0, sD, vw1, so + 12288, l0);
        SBAR();
      }
      hp[m.value] = geglu(m);
      PIN(hp[m.value]);
    });
  };
  if (!t1) phase1(IC<0>{}); else phase1(IC<1>{});
  STAMP(4);
  LANES();
  auto wload = [&](auto B) LAM {
    const u32 so = od + krot(2 * B.value) * 14336;
    dma2<wd_loc(2 * B.value), 0>(sD, vw0, sD, vw1, so, mg * 4096);
    dma2<wd_loc(2 * B.value), 2048>(sD, vw0, sD, vw1, so + 12288, mg * 4096);
  };
  if (w < 4) {
    if (PRIO) __builtin_amdgcn_s_setprio(2);
    if (has_tile) {
      sfor<0, 4>([&](auto m) LAM { *(LAS u32x4*)(6 * 16384 + w * 4096 + m.value * 1024 + vl) = hp[m.value]; });
      cnt_add(12 + w);
    }
    u32x4 fk;
    const u32 vfo = (lane < 17 ? lane : 0) * FSQ, sfo = WS_FQ + (head * 16 + qb) * BOX;
    u32 ord = 0, need = ((1u << NR) - 1) << (qb >> 1);
    need = ((need | need >> 8) & 255) * 257 + 0x10000;
#pragma nounroll
    for (u32 spins = 0; spins < (1u << 20); ++spins) {
      aload_sc1(fk, vfo, sWS, sfo);
      vm_on<0>(fk);
      const u32 m = ~(u32)stale(fk, tag);
      ord = m & (m >> 8) & 255;
      if ((m & need) == need) break;
      __builtin_amdgcn_s_sleep(NAPA);
    }
    STAMP(5);
    const u32 kvb = WS_KV + (head * 64 + 2 * w) * 4096;
    u32 kvc = 0;
    auto kv_next = [&](int kt) LAM {
      if (kt & 1) kvc = opaque(kvc) ^ 4096;
      else kvc = kvb + (qb & 1) * 4096 + ((opaque(qb >> 1) + (kt >> 1)) & 7) * 32768;
    };
    u32x4 Q[4][2], KV[4][4];
    auto q_load = [&](auto t) LAM {
      const u32 so = WS_Q + (head * 64 + qb * 4 + ((w + 1 + t.value) & 3)) * 2048;
      Q[t.value][0] = ld128(rWS, vl, so);
      Q[t.value][1] = ld128(rWS, vl + 1024, so);
    };
    kv_next(0);
    KV[0][0] = ld128(rWS, vl, kvc);
    KV[0][1] = ld128(rWS, vl + 1024, kvc);
    q_load(IC<0>{});
    q_load(IC<1>{});
    KV[0][2] = ld128(rWS, vl + 2048, kvc);
    q_load(IC<2>{});
    KV[0][3] = ld128(rWS, vl + 3072, kvc);
    q_load(IC<3>{});
    SBAR();
    sfor<1, UPF>([&](auto b) LAM {
      kv_next(b.value);
      sfor<0, 4>([&](auto i) LAM { KV[b.value][i.value] = ld128(rWS, vl + i.value * 1024, kvc); });
    });
    SBAR();
    cnt_add(3);
    STAMP(6);
    const int idx = 0xDDDD;
    f32x16 O[4] = {};
    const f32x16 Z = {};
    auto rgn = [&](int t) LAM -> u32 { return ((0x6108u >> (4 * t)) & 15) * 16384u; };
    auto put_part = [&](auto t, auto i) LAM {
      const f32x16& o = O[t.value];
      *(LAS f32x4*)(rgn((w + 1 + t.value) & 3) + t.value * 4096 + i.value * 1024 + vl) =
          (f32x4){o[4 * i.value], o[4 * i.value + 1], o[4 * i.value + 2], o[4 * i.value + 3]};
    };
    f32x16 Sx[4];
    u32 cb1 = 0;
    constexpr int CK = 8 * NR - 12, CP = CK - (NR > 3 ? 10 : 8);
    auto tile = [&](auto I) LAM {
      constexpr int kt = I.value >> 2, b = kt & 3, t = I.value & 3, tn = (t + 1) & 3, tp = (t + 3) & 3;
      constexpr int bn = t == 3 ? (b + 1) & 3 : b, bp = t == 0 ? (b + 3) & 3 : b;
      constexpr int E1 = PAT ? 3 : 4, E2 = 2 * E1, E3 = 3 * E1;
      auto exps = [&](auto lo, auto hi) LAM {
        SBAR();
        sfor<lo.value, hi.value>([&](auto e) LAM { Sx[t][e.value] = ex2(Sx[t][e.value]); });
        PIN(Sx[t]);
        SBAR();
      };
      if constexpr (I.value < 63) Sx[tn] = mfma32(KV[bn][0], Q[tn][0], Z);
      exps(IC<0>{}, IC<E1>{});
      if constexpr (I.value < 63) Sx[tn] = mfma32(KV[bn][1], Q[tn][1], Sx[tn]);
      if constexpr (I.value < 63) PIN(Sx[tn]);
      exps(IC<E1>{}, IC<E2>{});
      if constexpr (I.value > 0) O[tp] = smf_hi(KV[bp][3], Sx[tp], O[tp], idx);
      PIN(O[tp]);
      exps(IC<E2>{}, IC<E3>{});
      O[t] = smf_lo(KV[b][2], Sx[t], O[t], idx);
      PIN(O[t]);
      exps(IC<E3>{}, IC<E3 + 2>{});
      if constexpr (I.value == CP) aload_sc1(fk, vfo, sWS, sfo);
      constexpr int NX = 4 * (3 - UPF);
      sfor<(I.value < NX ? 4 * UPF + 2 * I.value : I.value + 12), (I.value < NX ? 4 * UPF + 2 * I.value + 2 : I.value + 13)>([&](auto L) LAM {
        if constexpr (L.value < 64) {
          if constexpr ((L.value & 3) == 0) kv_next(L.value >> 2);
          KV[(L.value >> 2) & 3][L.value & 3] = ld128(rWS, vl + (L.value & 3) * 1024, kvc);
        }
      });
      if constexpr (I.value == ID) cnt_add(9);
      if constexpr (I.value == 58) cb1 = cnt_get(1);
      if constexpr (I.value == 60) { if (has_tile && __builtin_amdgcn_readfirstlane(cb1) < 4u) cnt_wait(1, 4); cfence(); }
      if constexpr (I.value >= 62) sfor<0, 4>([&](auto i) LAM { put_part(IC<I.value - 62>{}, i); });
      exps(IC<E3 + 2>{}, IC<16>{});
    };
    Sx[0] = mfma32(KV[0][1], Q[0][1], mfma32(KV[0][0], Q[0][0], Z));
    SBAR();
    sfor<0, CK>(tile);
    vm_on<CK - CP>(fk);
    await(fk, vfo, sWS, sfo, tag);
    float bound = asf(__builtin_amdgcn_readlane(fk[0], 16)) * max8_nonneg(asf(fk[0]));
    PIN(bound);
    sfor<CK, 64>(tile);
    O[3] = smf_hi(KV[3][3], Sx[3], O[3], idx);
    if (__builtin_amdgcn_ballot_w64(!(bound <= 2200.f)) != 0) {
      sfor<0, 4>([&](auto t) LAM { O[t.value] = Z; });
      auto kvo = [&](int kt) LAM -> u32 { return kvb + (kt >> 1) * 32768 + (kt & 1) * 4096; };
      float mx[4] = {-3.0e38f, -3.0e38f, -3.0e38f, -3.0e38f};
      for (int kt = 0; kt < 16; ++kt) {
        const u32x4 k0 = ld128(rWS, vl, kvo(kt)), k1 = ld128(rWS, vl + 1024, kvo(kt));
        sfor<0, 4>([&](auto t) LAM {
          const f32x16 s = mfma32(k1, Q[t.value][1], mfma32(k0, Q[t.value][0], Z));
          sfor<0, 16>([&](auto i) LAM { mx[t.value] = __builtin_fmaxf(mx[t.value], s[i.value]); });
        });
      }
      sfor<0, 4>([&](auto t) LAM {
        mx[t.value] = __builtin_fmaxf(mx[t.value], other_half(mx[t.value]));
        *(LAS float*)(L_VT + ((((w + 1 + t.value) & 3) * 32 + r32) * 4 + w) * 4) = mx[t.value];
      });
      cnt_add(8);
      cnt_wait(8, 4);
      sfor<0, 4>([&](auto t) LAM {
        const f32x4 m4 = lds128f(L_VT + (((w + 1 + t.value) & 3) * 32 + r32) * 16);
        mx[t.value] = __builtin_fmaxf(__builtin_fmaxf(m4[0], m4[1]), __builtin_fmaxf(m4[2], m4[3]));
      });
      for (int kt = 0; kt < 16; ++kt) {
        const u32x4 k0 = ld128(rWS, vl, kvo(kt)), k1 = ld128(rWS, vl + 1024, kvo(kt));
        const u32x4 v0 = ld128(rWS, vl + 2048, kvo(kt)), v1 = ld128(rWS, vl + 3072, kvo(kt));
        sfor<0, 4>([&](auto t) LAM {
          f32x16 s = mfma32(k1, Q[t.value][1], mfma32(k0, Q[t.value][0], Z));
          sfor<0, 16>([&](auto i) LAM { s[i.value] = ex2(s[i.value] - mx[t.value]); });
          O[t.value] = smf_hi(v1, s, smf_lo(v0, s, O[t.value], idx), idx);
        });
      }
      sfor<0, 2>([&](auto t) LAM { sfor<0, 4>([&](auto i) LAM { put_part(t, i); }); });
    }
    STAMP(7);
    sfor<0, 4>([&](auto i) LAM { put_part(IC<2>{}, i); });
    cnt_add(2);
    cnt_wait(2, 4);
    const u32 red = rgn(w);
    {
      LANES();
      float o[16];
      sfor<0, 4>([&](auto i) LAM {
        const u32 ra = red + i.value * 1024 + vl;
        const f32x4 a0 = lds128f(ra), a1 = lds128f(ra + 4096), a2 = lds128f(ra + 8192);
        sfor<0, 4>([&](auto j) LAM { o[4 * i.value + j.value] = (a0[j.value] + a1[j.value]) + (a2[j.value] + O[3][4 * i.value + j.value]); });
      });
      const float lo_ = other_half(o[12]);
      const float il = rcp(hf ? o[12] : lo_);
      u32 P[8];
      sfor<0, 8>([&](auto i) LAM { P[i.value] = cvtpk(o[2 * i.value] * il, o[2 * i.value + 1] * il); });
      const int om = qb * 4 + w;
      st128(rWS, (u32x4){P[0], P[1], P[2], P[3]}, vl, WS_O + (om * 16 + head) * 2048);
      st128(rWS, (u32x4){P[4], P[5], hf ? 0u : P[6], hf ? 0u : P[7]}, vl + 1024, WS_O + (om * 16 + head) * 2048);
      if (has_tile) {
        const u32x4 sO = mk_srd(pwo, D_ * D_ * 2);
        const u32 vw = (lane >> 3) * 896 + (((lane & 7) ^ ((lane >> 2) & 4)) << 4);
        sfor<0, 14>([&](auto e) LAM { dma_i<e.value * 1024, 8 * e.value * 896>(sO, vw, 112 * w * 896 + nt * 128, red); });
        vmwait<14>();
      } else vmwait<0>();
      if (lane == 0) put_flag(ws, WS_FO + (om * 16 + head) * FSO, tag);
    }
    STAMP(8);
    if (!has_tile) { STAMP_END(); return; }
    {
    LANES();
    const u32 trow = (4 * hf + q4) * 128, swz = (q4 & 2) << 5;
    const int m = w & 1, kh = w >> 1;
    const rsrc_t rX = __builtin_amdgcn_make_buffer_rsrc((void*)px, 0, S_ * D_ * 2, 0x00020000);
    const u32 xo = ((rt * 64 + m * 32 + r32) * 448 + nt * 64 + kh * 32 + hf * 8) * 2;
    u32x4 xr[2];
    xr[0] = __builtin_amdgcn_raw_buffer_load_b128(rX, xo, 0, 0);
    xr[1] = __builtin_amdgcn_raw_buffer_load_b128(rX, xo + 32, 0, 0);
    PIN(xr[0]); PIN(xr[1]);
    vmwait<0>();
    cnt_add(6);
    poll_flags<NAPO>(ws, WS_FO + ((2 * rt + m) * 16 + 8 * kh + (lane & 7)) * FSO, tag);
    STAMP(9);
    u32x4 of[16];
    sfor<0, 16>([&](auto k) LAM { of[k.value] = ld128(rWS, vl + (k.value & 3) * 1024, WS_O + ((2 * rt + m) * 16 + 8 * kh) * 2048 + (k.value >> 2) * 4096); });
    SBAR();
    cnt_wait(6, 4);
    cnt_wait(5, 4);
    f32x4 gt[4];
    sfor<0, 4>([&](auto i) LAM { gt[i.value] = lds128f(5 * 16384 + w * 4096 + i.value * 1024 + vl); });
    u32 xs[8] = {xr[0][0], xr[0][1], xr[0][2], xr[0][3], xr[1][0], xr[1][1], xr[1][2], xr[1][3]};
    swap32(xs[0], xs[2]); swap32(xs[1], xs[3]); swap32(xs[4], xs[6]); swap32(xs[5], xs[7]);
    f32x16 acc[2] = {};
    sfor<0, 2>([&](auto r) LAM {
      const u32 rg = rgn(2 * kh + r.value);
      u32 wa[2], wp[2];
      sfor<0, 2>([&](auto n) LAM {
        wa[n.value] = rg + trow + ((64 * (n.value ^ kh) + 32 * blk + 8 * p4) ^ swz);
        wp[n.value] = wa[n.value] - hf * 512;
      });
      sfor<0, 8>([&](auto k) LAM {
        constexpr int hh = k.value >> 1, s = k.value & 1;
        constexpr u32 off = (28 * hh + 16 * s) * 128;
        sfor<0, 2>([&](auto n) LAM {
          constexpr int nn = 1 - n.value;
          const u32x4 wf = trd2(wa[nn] + off, (s ? wp[nn] : wa[nn]) + off + 1024);
          acc[nn] = mfma32(wf, of[8 * r.value + k.value], acc[nn]);
        });
      });
    });
    sfor<0, 4>([&](auto i) LAM {
      *(LAS f32x4*)(7 * 16384 + w * 4096 + i.value * 1024 + vl) =
          (f32x4){acc[1][4 * i.value], acc[1][4 * i.value + 1], acc[1][4 * i.value + 2], acc[1][4 * i.value + 3]};
    });
    STAMP(10);
    cnt_add(7);
    cnt_wait(7, 4);
    u32 R[8];
    sfor<0, 4>([&](auto i) LAM {
      const f32x4 a = lds128f(7 * 16384 + (w ^ 2) * 4096 + i.value * 1024 + vl);
      float y[4];
      sfor<0, 4>([&](auto j) LAM {
        const u32 xd = xs[2 * i.value + (j.value >> 1)];
        const float xv = asf((j.value & 1) ? (xd & 0xffff0000u) : (xd << 16));
        y[j.value] = __builtin_fmaf(acc[0][4 * i.value + j.value] + a[j.value], gt[i.value][j.value], xv);
      });
      R[2 * i.value] = cvtpk(y[0], y[1]);
      R[2 * i.value + 1] = cvtpk(y[2], y[3]);
    });
    swap32(R[0], R[2]); swap32(R[1], R[3]); swap32(R[4], R[6]); swap32(R[5], R[7]);
    const rsrc_t rOut = __builtin_amdgcn_make_buffer_rsrc(pout, 0, S_ * D_ * 2, 0x00020000);
    st128p(rOut, (u32x4){R[0], R[1], R[2], R[3]}, xo);
    st128p(rOut, (u32x4){R[4], R[5], R[6], R[7]}, xo + 32);
    asm volatile(".Lfb_enda:");
    STAMP(11);
    STAMP_END();
    }
  } else {
    const u32 trow = (4 * hf + q4) * 128, swz = (q4 & 2) << 5;
    const int gw = w - 4;
    const u32 lg = gw * 4096;
    if (has_tile) {
      u32x4 ha[4];
      cnt_wait(12 + gw, 1);
      sfor<0, 4>([&](auto m) LAM { ha[m.value] = lds128(6 * 16384 + lg + m.value * 1024 + vl); PIN(ha[m.value]); });
      if constexpr (WE == 5) wload(IC<8>{});
      const u32 fo = frag_off(), ho = WS_H + ((rb * 8 + gw * 2) * 112 + un * 4) * 1024;
      cnt_wait(3, HCNT);
      STAMP(9);
      sfor<0, 8>([&](auto i) LAM {
        constexpr int m = i.value >> 1;
        st128(rWS, (i.value & 1) ? hp[m] : ha[m], fo + (m & 1) * 256 + (i.value & 1) * 2048, ho + (m >> 1) * (112 * 1024));
        if constexpr (i.value < 7) __builtin_amdgcn_s_sleep(HP);
      });
      vmwait<0>();
      if (lane == 0) put_flag(ws, WS_FH + ((rb * 4 + gw) * 32 + un) * FSH, tag);
      STAMP(5);
      sfor<4 + WE, 9>(wload);
      touch(pwo + ((rq & 3) * 112 + gw * 28 + (lane < 28 ? lane : 0)) * 896 + nt * 128);
      STAMP(8);
    }
    if (bid == 255 && gw == 0) {
      for (u32 i = 0; i < 4; ++i) {
        u32x4 f = {0, 0, 0, (u32)~(tag >> 32)};
        await(f, lane * 128, sWS, WS_ST + i * 8192, tag);
      }
      if (lane == 0) put_flag(ws, WS_EPOCH, epoch + 1);
    }
    if (!has_tile) { STAMP_END(); return; }
    const u32x4 sH = mk_srd(ws + WS_H, 64 * 112 * 1024);
    const u32 oh0 = ((2 * rt + (gw & 1)) * 112 + 28 * gw) * 1024, oh1 = ((2 * rt + 1 - (gw & 1)) * 112 + 28 * gw) * 1024;
    u32x4 hfr[DPH][2];
    auto hload = [&](auto K) LAM {
      aload_sc1(hfr[K.value % DPH][0], vl, sH, oh0 + krot(K.value) * 1024);
      aload_sc1(hfr[K.value % DPH][1], vl, sH, oh1 + krot(K.value) * 1024);
    };
    cnt_wait(3, 4);
    poll_flags<NAPB>(ws, WS_FH + (rt * 32 + 7 * gw + (lane < 7 ? lane : 0)) * FSH, tag);
    STAMP(6);
    sfor<0, DPH>([&](auto K) LAM { hload(K); });
    __builtin_amdgcn_s_setprio(PRB);
    u32 wa[2];
    sfor<0, 2>([&](auto n) LAM { wa[n.value] = lg + trow + ((64 * (n.value ^ (gw >> 1)) + 32 * blk + 8 * p4) ^ swz); });
    f32x16 acc[2][2] = {};
    u32x4 wf[2][2];
    auto ready = [&](auto K, auto N) LAM {
      constexpr int k = K.value;
      constexpr int o = wd_loc(k), d = (o & ~65535) - (k ? (int)(wd_loc(k - 1) & ~65535) : 0);
      vm_on2<N.value>(hfr[k % DPH][0], hfr[k % DPH][1]);
      sfor<0, 2>([&](auto n) LAM {
        if constexpr (d != 0) { wa[n.value] += d; PIN(wa[n.value]); }
        wf[k & 1][n.value] = trd2(wa[n.value] + (o & 65535), wa[n.value] + (o & 65535) + 2048);
      });
    };
    ready(IC<0>{}, IC<2 * (DPH - 1)>{});
    sfor<0, 28>([&](auto K) LAM {
      constexpr int k = K.value;
      if constexpr (k == KD) cnt_wait(9, 4);
      if constexpr (k + 1 < 28 && k != 17) ready(IC<k + 1>{}, IC<gemm_wait(k + 1)>{});
      sfor<0, 2>([&](auto n) LAM {
        sfor<0, 2>([&](auto m) LAM { acc[m.value][n.value] = mfma32(wf[k & 1][n.value], hfr[k % DPH][m.value], acc[m.value][n.value]); PIN(acc[m.value][n.value]); });
      });
      SBAR();
      if constexpr (k == 17) cnt_add(1);
      if constexpr (k + DPH < 28) hload(IC<k + DPH>{});
      sfor<9, 14>([&](auto B) LAM { if constexpr (wd_at(B.value) == k) wload(B); });
      if constexpr (k == 17) ready(IC<18>{}, IC<gemm_wait(18)>{});
      SBAR();
    });
    STAMP(7);
    sfor<1, 4>([&](auto x) LAM {
      const f32x16& a = acc[x.value & 1][x.value >> 1];
      sfor<0, 4>([&](auto i) LAM {
        *(LAS f32x4*)(IC<wd_blk(x.value - 1)>::value * 16384 + lg + i.value * 1024 + vl) = (f32x4){a[4 * i.value], a[4 * i.value + 1], a[4 * i.value + 2], a[4 * i.value + 3]};
      });
    });
    cnt_add(4);
    cnt_wait(4, 4);
    sfor<0, 4>([&](auto i) LAM {
      const u32 ra = i.value * 1024 + vl;
      const f32x4 a1 = lds128f(2 * 16384 + (gw ^ 1) * 4096 + ra), a2 = lds128f(3 * 16384 + (gw ^ 2) * 4096 + ra), a3_ = lds128f(4 * 16384 + (gw ^ 3) * 4096 + ra);
      f32x4 gv;
      sfor<0, 4>([&](auto j) LAM {
        const float f = (acc[0][0][4 * i.value + j.value] + a1[j.value]) + (a2[j.value] + a3_[j.value]);
        gv[j.value] = f * rcp(1.f + ex2(f * -1.4426950408889634f));
      });
      *(LAS f32x4*)(5 * 16384 + lg + ra) = gv;
    });
    asm volatile(".Lfb_endb:");
    cnt_add(5);
    STAMP(10);
    STAMP_END();
  }
}

extern "C" void kernel_launch(void* const* d_in, const int* in_sizes, int n_in, void* d_out, int out_size, void* d_ws,
                              size_t ws_size, hipStream_t stream) {
  fused_block<<<256, 512, 0, stream>>>(d_in[0], d_in[1], d_in[2], d_in[4], d_in[5], d_in[6], d_out, (char*)d_ws);
}
```

```cpp
#include <hip/hip_runtime.h>
#include <stdint.h>
#pragma clang diagnostic ignored "-Wint-to-pointer-cast"

#define DEV __device__ __forceinline__
#define LAM __attribute__((always_inline))
#define LAS __attribute__((address_space(3)))
#define GAS __attribute__((address_space(1)))

#define STAMPS 0

typedef unsigned u32;
typedef unsigned long long u64;
typedef u32 u32x2 __attribute__((ext_vector_type(2)));
typedef u32 u32x4 __attribute__((ext_vector_type(4)));
typedef short s16x4 __attribute__((ext_vector_type(4)));
typedef float f32x2 __attribute__((ext_vector_type(2)));
typedef float f32x4 __attribute__((ext_vector_type(4)));
typedef float f32x8 __attribute__((ext_vector_type(8)));
typedef float f32x16 __attribute__((ext_vector_type(16)));
typedef __bf16 bf16x8 __attribute__((ext_vector_type(8)));
typedef __bf16 bf16x16 __attribute__((ext_vector_type(16)));

extern "C" __device__ u64 llvm_dispatch_id() __asm("llvm.amdgcn.dispatch.id");

constexpr int S_ = 2048, D_ = 448, F_ = 1792;
#define V3 1
#define V4 1
#define V5 1
#define FSQ 16
#define FSH 8
#define FSO 8
constexpr u32 WS_EPOCH = 0, WS_ST = 32768, WS_DBG = 65536;
constexpr u32 WS_Q = 1u << 20, WS_KV = 3u << 20, WS_O = 7u << 20, WS_H = 9u << 20;
constexpr u32 WS_CS = 16u << 20, WS_FO = WS_CS + (512u << 10), WS_FH = WS_FO + (256u << 10), WS_FQ = WS_FH + (256u << 10), WS_END = 18u << 20;
constexpr u32 BOX = 512;
constexpr u32 L_QK = 147456, L_CS = 159744, L_CNT = L_CS + 2560, L_DUM = L_CS + 2816, L_RV = L_CS + 3072;
constexpr u32 OOB = 0x7ffffff0;
constexpr u32 L_VT = L_QK;
constexpr u32 LDS_BYTES = 163840;

template <int V> struct IC { static constexpr int value = V; };
template <int I, int N, class Fn> DEV void sfor(Fn&& f) {
  if constexpr (I < N) { f(IC<I>{}); sfor<I + 1, N>(f); }
}

#define SBAR() __builtin_amdgcn_sched_barrier(0)
template <class T> DEV void PIN(T& x) { asm volatile("" : "+v"(x)); }
template <class T> DEV void undef(T& x) { asm volatile("" : "=v"(x)); }
template <class T> DEV void nop_on(T& x) { asm volatile("s_nop 7" : "+v"(x)); }
template <class T> DEV void lgkm0_on(T& x) { asm volatile("s_waitcnt lgkmcnt(0)" : "+v"(x)::"memory"); }
template <int T> DEV void colsum(f32x4& c0, f32x4& c1, u32x4 one, const u32x4* w, int who) {
#define CSM(k, c, r) "s_cmp_lg_u32 %8, " #k "\n\ts_cbranch_scc1 .Lcs" #r "_%=\n\tv_mfma_f32_16x16x32_bf16 %" #c ", %2, %" #r ", %" #c "\n.Lcs" #r "_%=:\n\t"
  if constexpr (T == 0)
    asm volatile(CSM(0, 0, 3) CSM(1, 0, 4) CSM(2, 0, 5) CSM(3, 0, 6) CSM(0, 1, 7)
                 : "+v"(c0), "+v"(c1) : "v"(one), "v"(w[1]), "v"(w[2]), "v"(w[3]), "v"(w[4]), "v"(w[0]), "s"(who) : "scc");
  else
    asm volatile(CSM(0, 0, 3) CSM(1, 0, 4) CSM(2, 0, 5) CSM(3, 0, 6)
                 : "+v"(c0), "+v"(c1) : "v"(one), "v"(w[1]), "v"(w[2]), "v"(w[3]), "v"(w[4]), "v"(one), "s"(who) : "scc");
#undef CSM
}
DEV void mfma4_unless(f32x4* c, const u32x4* a, u32x4 b, int skip) {
  asm volatile("s_cmp_lg_u32 %9, 0\n\ts_cbranch_scc1 .Lm4_%=\n\tv_mfma_f32_16x16x32_bf16 %0, %4, %8, %0\n\tv_mfma_f32_16x16x32_bf16 %1, %5, %8, %1\n\t"
               "v_mfma_f32_16x16x32_bf16 %2, %6, %8, %2\n\tv_mfma_f32_16x16x32_bf16 %3, %7, %8, %3\n.Lm4_%=:"
               : "+v"(c[0]), "+v"(c[1]), "+v"(c[2]), "+v"(c[3]) : "v"(a[0]), "v"(a[1]), "v"(a[2]), "v"(a[3]), "v"(b), "s"(skip) : "scc");
}
template <int OFF> DEV void aload(u32x4& d, u32 voff, u32x4 srd, u32 soff) {
  asm volatile("buffer_load_dwordx4 %0, %1, %2, %3 offen offset:%4" : "=v"(d) : "v"(voff), "s"(srd), "s"(soff), "i"(OFF) : "memory");
}
template <int N> DEV void vm_on(u32x4& a) { asm volatile("s_waitcnt vmcnt(%1)" : "+v"(a) : "i"(N) : "memory"); }
DEV void aload_sc1(u32x4& d, u32 voff, u32x4 srd, u32 soff) {
  asm volatile("buffer_load_dwordx4 %0, %1, %2, %3 offen sc1" : "=v"(d) : "v"(voff), "s"(srd), "s"(soff) : "memory");
}
template <int B> DEV void load_if(u32x4* d, u32 voff, u32x4 srd, u32 soff, u32 mask) {
  u32 s2 = soff + 4096;
#define LIF(h, a, b, s, o) "s_bitcmp0_b32 %12, %13+" #h "\n\ts_cbranch_scc1 .Lli" #h "_%=\n\tbuffer_load_dwordx4 %" #a ", %8, %9, " s " offen offset:" #o " sc1\n\t" \
  "buffer_load_dwordx4 %" #b ", %8, %9, " s " offen offset:" #o "+1024 sc1\n.Lli" #h "_%=:\n\t"
  asm volatile(LIF(0, 0, 1, "%10", 0) LIF(1, 2, 3, "%10", 2048) LIF(2, 4, 5, "%11", 0) LIF(3, 6, 7, "%11", 2048)
               : "+v"(d[0]), "+v"(d[1]), "+v"(d[2]), "+v"(d[3]), "+v"(d[4]), "+v"(d[5]), "+v"(d[6]), "+v"(d[7])
               : "v"(voff), "s"(srd), "s"(soff), "s"(s2), "s"(mask), "i"(B) : "memory", "scc");
#undef LIF
}
template <int N> DEV void vm_on2(u32x4& a, u32x4& b) { asm volatile("s_waitcnt vmcnt(%2)" : "+v"(a), "+v"(b) : "i"(N) : "memory"); }

DEV f32x16 mfma32(u32x4 a, u32x4 b, f32x16 c) {
  return __builtin_amdgcn_mfma_f32_32x32x16_bf16(__builtin_bit_cast(bf16x8, a), __builtin_bit_cast(bf16x8, b), c, 0, 0, 0);
}
template <bool FIRST> DEV void mac16(f32x4& c, u32x4 a, u32x4 b) {
  if constexpr (FIRST) asm volatile("v_mfma_f32_16x16x32_bf16 %0, %1, %2, 0" : "=&v"(c) : "v"(a), "v"(b));
  else asm volatile("v_mfma_f32_16x16x32_bf16 %0, %1, %2, %0" : "+v"(c) : "v"(a), "v"(b));
}
DEV f32x16 smf_lo(u32x4 a, f32x16 p, f32x16 c, int idx) {
  f32x8 b = __builtin_shufflevector(p, p, 0, 1, 2, 3, 4, 5, 6, 7);
  return __builtin_amdgcn_smfmac_f32_32x32x32_bf16(__builtin_bit_cast(bf16x8, a), __builtin_bit_cast(bf16x16, b), c, idx, 0, 0);
}
DEV f32x16 smf_hi(u32x4 a, f32x16 p, f32x16 c, int idx) {
  f32x8 b = __builtin_shufflevector(p, p, 8, 9, 10, 11, 12, 13, 14, 15);
  return __builtin_amdgcn_smfmac_f32_32x32x32_bf16(__builtin_bit_cast(bf16x8, a), __builtin_bit_cast(bf16x16, b), c, idx, 0, 0);
}
typedef __bf16 bf16x2 __attribute__((ext_vector_type(2)));
DEV u32 cvtpk(float lo, float hi) {
  f32x2 v = {lo, hi};
  return __builtin_bit_cast(u32, __builtin_convertvector(v, bf16x2));
}
DEV float asf(u32 u) { return __builtin_bit_cast(float, u); }
DEV u32 asu(float f) { return __builtin_bit_cast(u32, f); }
DEV float mul1(float a, float b) { float r; asm("v_mul_f32 %0, %1, %2" : "=v"(r) : "v"(a), "v"(b)); return r; }
DEV float add1(float a, float b) { float r; asm("v_add_f32 %0, %1, %2" : "=v"(r) : "v"(a), "v"(b)); return r; }
DEV float fma1(float a, float b, float c) { float r; asm("v_fma_f32 %0, %1, %2, %3" : "=v"(r) : "v"(a), "v"(b), "v"(c)); return r; }
DEV float bfround(float f) { return asf(cvtpk(f, f) << 16); }
DEV float ex2(float x) { return __builtin_amdgcn_exp2f(x); }
DEV float rcp(float x) { return __builtin_amdgcn_rcpf(x); }
DEV float other_half(float v) {
  auto r = __builtin_amdgcn_permlane32_swap(asu(v), asu(v), false, false);
  return asf(r[0] ^ r[1] ^ asu(v));
}
DEV u32x4 mk_srd(const void* p, u32 bytes) {
  u64 a = (u64)p;
  u32x4 r = {(u32)a, (u32)(a >> 32) & 0xffffu, bytes, 0x00020000u};
  return r;
}
template <int LD> DEV void dma_half(u32x4 srd, u32 voff, u32 so, u32 l0) {
#define EXH "0"
  asm volatile("s_add_u32 m0, %0, %1\n\ts_mov_b32 exec_hi, " EXH "\n\tbuffer_load_dwordx4 %2, %3, %4 offen lds\n\ts_mov_b32 exec_hi, -1"
               ::"s"(l0), "i"(LD), "v"(voff), "s"(srd), "s"(so) : "memory", "scc");
}
template <int LD, int OFF> DEV void dma1(u32x4 srd, u32 voff, u32 so, u32 l0) {
  asm volatile("s_add_u32 m0, %0, %1\n\ts_nop 0\n\tbuffer_load_dwordx4 %2, %3, %4 offen offset:%5 lds"
               ::"s"(l0), "i"(LD), "v"(voff), "s"(srd), "s"(so), "i"(OFF) : "memory", "scc");
}
template <int LD, int OFF> DEV void dma2(u32x4 s0, u32 v0, u32x4 s1, u32 v1, u32 so, u32 l0) {
  asm volatile("s_add_u32 m0, %0, %1\n\ts_nop 0\n\tbuffer_load_dwordx4 %2, %3, %6 offen offset:%7 lds\n\tbuffer_load_dwordx4 %4, %5, %6 offen offset:%8 lds"
               ::"s"(l0), "i"(LD), "v"(v0), "s"(s0), "v"(v1), "s"(s1), "s"(so), "i"(OFF), "i"(OFF + 1024) : "memory", "scc");
}
DEV void dma_sc1(u32x4 srd, u32 voff, u32 so, u32 l0) {
  asm volatile("s_mov_b32 m0, %0\n\ts_nop 0\n\tbuffer_load_dwordx4 %1, %2, %3 offen sc1 lds" ::"s"(l0), "v"(voff), "s"(srd), "s"(so) : "memory");
}
DEV u32 opaque(u32 x) { asm volatile("" : "+s"(x)); return x; }
template <int LD, int ADD> DEV void dma_i(u32x4 srd, u32 voff, u32 s0, u32 l0) {
  u32 t;
  asm volatile("s_add_u32 m0, %1, %2\n\ts_add_u32 %0, %3, %4\n\tbuffer_load_dwordx4 %5, %6, %0 offen lds"
               : "=&s"(t) : "s"(l0), "i"(LD), "s"(s0), "i"(ADD), "v"(voff), "s"(srd) : "memory", "scc");
}
DEV int lane_id() {
  u32 z = 0;
  asm volatile("" : "+s"(z));
  return __builtin_amdgcn_mbcnt_hi(~0u, __builtin_amdgcn_mbcnt_lo(~0u, z));
}
#define LANES() const int lane = lane_id(), r32 = lane & 31, hf = lane >> 5, blk = (lane >> 4) & 1, q4 = (lane & 15) >> 2, p4 = lane & 3; \
  const u32 vl = lane * 16; (void)r32; (void)hf; (void)blk; (void)q4; (void)p4; (void)vl
DEV void touch(const void* p) {
  asm volatile("s_mov_b32 m0, %0\n\ts_nop 0\n\tglobal_load_lds_dword %1, off" ::"i"(L_DUM), "v"(p) : "memory");
}
DEV void touch_b(u32x4 srd, u32 voff, u32 so) {
  asm volatile("s_mov_b32 m0, %0\n\ts_nop 0\n\tbuffer_load_dword %1, %2, %3 offen lds" ::"i"(L_DUM), "v"(voff), "s"(srd), "s"(so) : "memory");
}
template <int N> DEV void vmwait() { asm volatile("s_waitcnt vmcnt(%0)" ::"i"(N) : "memory"); }
DEV void cfence() { asm volatile("" ::: "memory"); }
DEV void cnt_add(int i) {
  asm volatile("s_mov_b64 exec, 1\n\tds_inc_u32 %0, %0\n\ts_mov_b64 exec, -1" ::"v"(L_CNT + 4 * i) : "memory");
}
DEV u32 cnt_get(int i) { return __hip_atomic_load((LAS u32*)(L_CNT + 4 * i), __ATOMIC_RELAXED, __HIP_MEMORY_SCOPE_WORKGROUP); }
DEV void cnt_wait(int i, u32 n) {
  while (__builtin_amdgcn_readfirstlane(cnt_get(i)) < n) __builtin_amdgcn_s_sleep(1);
  cfence();
}
DEV u32x2 trd(u32 a) { return __builtin_bit_cast(u32x2, __builtin_amdgcn_ds_read_tr16_b64_v4i16((LAS s16x4*)a)); }
DEV u32x4 trd2(u32 a0, u32 a1) { u32x2 x = trd(a0), y = trd(a1); u32x4 r = {x[0], x[1], y[0], y[1]}; return r; }
DEV u32x4 lds128(u32 a) { return *(LAS u32x4*)a; }
DEV f32x4 lds128f(u32 a) { return *(LAS f32x4*)a; }
template <int N> DEV void sync_stage() { asm volatile("s_waitcnt vmcnt(%0) lgkmcnt(0)\n\ts_barrier" ::"i"(N) : "memory"); }
template <int I> DEV void wlane(u32& f, u32 v) { asm volatile("v_writelane_b32 %0, %1, %2" : "+v"(f) : "s"(v), "i"(I)); }
typedef __amdgpu_buffer_rsrc_t rsrc_t;
DEV void st128(rsrc_t r, u32x4 v, u32 voff, u32 soff) {
  __builtin_amdgcn_raw_buffer_store_b128(v, r, voff, __builtin_amdgcn_readfirstlane(soff), 16);
  asm volatile("s_nop 1" ::"v"(v) : "memory");
}
DEV void st128p(rsrc_t r, u32x4 v, u32 voff) {
  __builtin_amdgcn_raw_buffer_store_b128(v, r, voff, 0, 16);
}
DEV void aflag(u64& d, const char* p) { asm volatile("global_load_dwordx2 %0, %1, off sc1" : "=v"(d) : "v"(p) : "memory"); }
template <int N> DEV void vm_on1(u64& a) { asm volatile("s_waitcnt vmcnt(%1)" : "+v"(a) : "i"(N) : "memory"); }
DEV void swap32(u32& a, u32& b) {
  auto r = __builtin_amdgcn_permlane32_swap(a, b, false, false);
  a = r[0]; b = r[1];
}
DEV void swap16(u32& a, u32& b) {
  auto r = __builtin_amdgcn_permlane16_swap(a, b, false, false);
  a = r[0]; b = r[1];
}
DEV u32 from_lane(int l, u32 v) { return __builtin_amdgcn_ds_bpermute(l * 4, v); }
DEV u32x4 ld128(rsrc_t r, u32 voff, u32 soff) { return __builtin_amdgcn_raw_buffer_load_b128(r, voff, __builtin_amdgcn_readfirstlane(soff), 16); }
DEV void put_flag(char* ws, u32 off, u64 tag) { __hip_atomic_store((GAS u64*)(ws + off), tag, __ATOMIC_RELAXED, __HIP_MEMORY_SCOPE_AGENT); }
template <int NAP> DEV void poll_flags(char* ws, u32 off, u64 tag) {
#pragma nounroll
  for (u32 spins = 0; spins < (1u << 20); ++spins) {
    u64 v = __hip_atomic_load((GAS u64*)(ws + off), __ATOMIC_RELAXED, __HIP_MEMORY_SCOPE_AGENT);
    if (__builtin_amdgcn_ballot_w64(v != tag) == 0) break;
    __builtin_amdgcn_s_sleep(NAP);
  }
  __builtin_amdgcn_fence(__ATOMIC_ACQUIRE, "wavefront");
  asm volatile("" ::: "memory");
}
DEV u32 half_max(u32 x) {
#define DPPMAX(ctrl) { u32 y = __builtin_amdgcn_update_dpp(0u, x, ctrl, 0xf, 0xf, true); x = x > y ? x : y; }
  DPPMAX(0x111) DPPMAX(0x112) DPPMAX(0x114) DPPMAX(0x118) DPPMAX(0x142)
  return x;
}
DEV float max8_nonneg(float v) {
  u32 x = asu(v);
  DPPMAX(0x111) DPPMAX(0x112) DPPMAX(0x114)
#undef DPPMAX
  return asf(__builtin_amdgcn_readlane(x, 7));
}

DEV void widen(float& lo, float& hi, u32 x) {
  asm volatile("v_lshlrev_b32 %0, 16, %2\n\tv_and_b32 %1, 0xffff0000, %2" : "=&v"(lo), "=v"(hi) : "v"(x));
}
DEV float half_sum(float v) {
#define DPPADD(ctrl) v += asf(__builtin_amdgcn_update_dpp(0u, asu(v), ctrl, 0xf, 0xf, true));
  DPPADD(0x111) DPPADD(0x112) DPPADD(0x114) DPPADD(0x118) DPPADD(0x142)
#undef DPPADD
  return v;
}
DEV u64 stale(u32x4 f, u64 tag) { return __builtin_amdgcn_ballot_w64(f[1] != (u32)tag) | __builtin_amdgcn_ballot_w64(f[3] != (u32)(tag >> 32)); }
DEV bool fresh(u32x4 f, u64 tag) { return stale(f, tag) == 0; }
DEV void await(u32x4& f, u32 voff, u32x4 srd, u32 soff, u64 tag) {
#pragma nounroll
  for (u32 spins = 0; !fresh(f, tag) && spins < (1u << 20); ++spins) {
    __builtin_amdgcn_s_sleep(1);
    aload_sc1(f, voff, srd, soff);
    vm_on<0>(f);
  }
}

#define STAMP(i) do {} while (0)
#define STAMPF(i) do {} while (0)
#define STAMP_END() do {} while (0)

#define STAMPG(i) do {} while (0)
#define ROT1 2
#define PAT 0
#define DPH 10
#define PRIO 1
#define NR 4
#define NR1 1
#define C3E 1
#define PROG 0
#define DW 0
#define PGA 0
#define GA 4
#define PI 2
#define WL 0
#define OWN 0
#define PK0 1
#define CPK 0
#define NAPB 4
#define WE 0
#define HP 4
#define PRB 0
#define HCNT 4
#define UPF 2
#define WS 1
#define WT 2
#define NAPA 1
#define NAPO 2
#define G0 1
#define WA 4
#define TJ1 1
#define TW 1
#define TJ2 5
constexpr int TWM = (TW & 1 ? 4 << TJ1 : 0) | (TW & 2 ? 20 << TJ2 : 0);
#define KD 18
#define ID 60

constexpr int piece_at(int T, int j, int i) {
  constexpr int at[2][2][7] = {{{2, 5, 8, 11, 14, 16, 18}, {6, 11, 16, 19, 5, 14, 18}}, {{1, 3, -1, 5, 7, 9, 11}, {3, 7, -1, 11, 2, 6, 10}}};
  const bool all = j == 0 || (T == 0 && j == 7);
  if (T == 0 && j == 8) return -1;
  if (j == 11) return i % 4 == 3 ? i / 4 : -1;
  for (int p = all || (j & 1) ? 0 : 4; p < (!all && (j & 1) ? 4 : 7); ++p) if (at[T][!all][p] == i) return p;
  return -1;
}
constexpr int slots(int T, int j) { return T == 1 || j > 10 ? 16 : j == 10 ? 36 : 20; }
constexpr int slot_tile(int T, int j, int i) { return T == 1 || j > 10 ? i / 4 + 1 : j < 10 || i < 12 ? i / 4 : i < 28 ? 0 : i / 4 - 4; }
constexpr int slot_step(int T, int j, int i) { return T == 1 || j != 10 || i < 12 || i >= 28 ? j : i < 20 ? 8 + i / 4 : i < 24 ? -1 : 13; }
constexpr int slot_read(int T, int j, int i) {
  const int r = i - 2 * (j & 1);
  if (T == 1) return j < 13 && r >= 0 && r < 12 ? 16 * (j + 1) + (r < 2 ? r + 6 : r < 6 ? r : r + 2) : -1;
  if (j < 10) return r >= 0 && r < 14 ? 16 * (j + 1) + r : -1;
  if (j == 10) return i < 18 ? 16 * (11 + i / 6) + i % 6 : i < 26 ? 16 * 11 + i - 12 : -1;
  return j < 13 && i < 8 ? 16 * (j + 1) + i + 6 : -1;
}
constexpr int wd_blk(int b) { return (int)(0x61087610875432ull >> (4 * b)) & 15; }
constexpr u32 wd_loc(int k) { return wd_blk(k / 2) * 16384 + (k & 1) * 1024; }

constexpr int wd_at(int b) { return 2 * b - 9; }
constexpr int gemm_wait(int k) {
  int n = 0, last = 0;
  const int end = k == 18 ? 18 : k - 1;
  for (int i = -DPH; i < end; ++i) {
    if (i + DPH < 28) { n += 2; if (i + DPH == k) last = n; }
    for (int b = 9; b < 14; ++b) if (wd_at(b) == i) { n += 4; if (k / 2 == b) last = n; }
  }
  return n - last < 63 ? n - last : 63;
}
static_assert(DPH == 10);
typedef u32 u32x16 __attribute__((ext_vector_type(16)));
DEV u32x4 mk_srd2(u32 lo, u32 hi, u32 bytes) { return (u32x4){lo, hi & 0xffffu, bytes, 0x00020000u}; }

__global__ __launch_bounds__(512, 2) void fused_block(const void*, const void*, const void*, const void*, const void*, const void*,
                                                           void*, char*) {
  __shared__ __attribute__((aligned(16))) char smem[LDS_BYTES];
  u32x16 ka;
  asm volatile("s_load_dwordx16 %0, %1, 0x0" : "=s"(ka) : "s"(__builtin_amdgcn_kernarg_segment_ptr()));
  SBAR();
  const int tid = __builtin_amdgcn_workitem_id_x();
  const int w = __builtin_amdgcn_readfirstlane(tid >> 6);
  const int bid = __builtin_amdgcn_workgroup_id_x();
  const int xcd = bid & 7, bi = bid >> 3;
  u64 pc0;
  u32 clen;
  asm volatile("s_getpc_b64 %0\n.Lfb_pc0:\n\ts_max_u32 %1, .Lfb_enda-.Lfb_pc0, 0\n\ts_max_u32 %1, .Lfb_endb-.Lfb_pc0, %1" : "=s"(pc0), "=s"(clen)::"scc");
  {
    const u32 o = (bi * 16 + w * 2 + (tid & 1)) * 128;
    touch((const char*)pc0 + (o < clen ? o : clen));
  }
  SBAR();
  const int rb = 4 * (xcd & 1) + (bi & 3), quad = xcd >> 1, cl = bi >> 2, cb = 8 * quad + cl;
  const bool t1 = cl == 7;
  const int mg = w & 3, g = w >> 2, un = 7 * quad + cl;
  const u32 rot = 2 * (bi & 3);
  const int rr = (tid >> 3) & 7, ch = tid & 7, odd = (tid >> 5) & 2;
  u32 vx0 = rr * 896 + ((ch ^ (rr >> 1)) << 4), vx1 = (vx0 ^ 64) + 6144;
  u32 ox = (rb * 8 + w) * (32 * 896), lw = w * 1024, so0 = ox + rot * 128;
  asm volatile("s_waitcnt lgkmcnt(0)" : "+s"(ka), "+v"(vx0), "+v"(vx1), "+s"(so0), "+s"(lw));
  STAMPF(3);
  const u32x4 sX = mk_srd2(ka[0], ka[1], S_ * D_ * 2);
  dma2<0, 0>(sX, vx0, sX, vx1, so0, 4 * lw);
  dma2<0, 2048>(sX, vx0, sX, vx1, so0 + 12288, 4 * lw);
  SBAR();
  const u32 wst = t1 ? 2688 : 3584, cw = ch ^ ((rr & 2) << 1);
  const u32 cv = (tid & 15) ^ ((tid >> 2) & 12) ^ odd;
  const u32 va = t1 ? (rr >> 1) * 2688 + (cv >= 14 ? cv - 2 : cv) * 16 : rr * 3584 + (cw ^ odd) * 16;
  const u32 ow = 8 * w * wst + (t1 ? 1792 + 224 * quad : un * 128);
  const u32 vq = ((tid >> 2) & 7) * 2688 + ((tid & 3) ^ odd) * 16;
  const u32 oq = w * (8 * 2688) + 64 * un;
  const u64 pb_ = (t1 ? ((u64)ka[3] << 32 | ka[2]) + 4 * 2688 : ((u64)ka[9] << 32 | ka[8])) - 1024;
  const u32x4 sA = mk_srd2(t1 ? ka[2] : ka[6], t1 ? ka[3] : ka[7], 1u << 21), sB = mk_srd2((u32)pb_, (u32)(pb_ >> 32), 1u << 21);
  const u32x4 sQ = mk_srd2(ka[2], ka[3], D_ * 3 * D_ * 2);
  u32 kst = rot, so = 0;
  auto piece = [&](auto H_, auto I_) LAM {
    constexpr int H = H_.value, i = I_.value;
    if constexpr (i == 0) {
      if constexpr (H > 0) { kst = opaque(kst) + 1; kst = kst == 7 ? 0 : kst; }
      so = ow + kst * (64 * wst);
    }
    if constexpr (i < 2) dma1<(6 + H % 3) * 16384, i * 1024>(i ? sB : sA, va, so, 2 * lw);
    if constexpr (i == 2) dma_half<L_QK + (H % 3) * 4096>(sQ, vq, oq + opaque(kst) * (64 * 2688), lw >> 1);
    if constexpr (i == 3) so = ox + opaque(kst) * 128;
    if constexpr (i == 5) so = opaque(so) + 12288;
    if constexpr (i >= 3) dma1<(H % 3) * 32768, (i - 3) * 1024>(sX, (i & 1) ? vx0 : vx1, so, 4 * lw);
  };
  sfor<0, 3>([&](auto i) LAM { piece(IC<0>{}, i); });
  asm volatile("s_barrier" ::: "memory");
  STAMPF(4);
  u32x4 xr[2];
  const u32 row = rb * 256 + cb * 8 + 2 * mg;
  {
    const u32 vr = (tid & 31) < 28 * g ? (tid & 31) * 16 + (tid & 32) * 28 : OOB;
    aload<0>(xr[0], vr, sX, row * 896);
    aload<448>(xr[1], vr, sX, row * 896);
  }
  char* const ws = (char*)((u64)ka[15] << 32 | ka[14]);
  u64 epoch;
  asm volatile("s_load_dwordx2 %0, %1, 0x0" : "=s"(epoch) : "s"(ws));
  asm volatile("" ::"v"((u32)(uintptr_t)(LAS char*)smem));
  const char* const px = (const char*)((u64)ka[1] << 32 | ka[0]);
  const char* const pwo = (const char*)((u64)ka[5] << 32 | ka[4]);
  char* const pout = (char*)((u64)ka[13] << 32 | ka[12]);
  const u32x4 sD = mk_srd2(ka[10], ka[11], F_ * D_ * 2), sWS = mk_srd2(ka[14], ka[15], WS_END);
  const rsrc_t rWS = __builtin_amdgcn_make_buffer_rsrc(ws, 0, WS_END, 0x00020000);
  if (tid < 32) *(LAS u32*)(L_CNT + tid * 4) = 0;
  const bool has_tile = !t1;
  const int rq = bi / 7, rt = 4 * xcd + rq, nt = bi - 7 * rq;
  const int rotg = (4 * nt + 8 * rq) % 28;
  const int head = 2 * xcd + (bi >> 4), qb = bi & 15;
  const u32 vw0 = rr * 896 + cw * 16, vw1 = vw0 + 6144;
  auto krot = [&](u32 k) LAM -> u32 { k += opaque(rotg); return k >= 28 ? k - 28 : k; };
  const u32 od = 448 * mg * 896 + nt * 128;

  asm volatile("s_waitcnt lgkmcnt(0)" : "+s"(epoch));
  u64 tag = (epoch + 0x9E3779B97F4A7C15ull) * 0xBF58476D1CE4E5B9ull;
  tag = (tag ^ (tag >> 29)) + llvm_dispatch_id() * 0x94D049BB133111EBull;
  tag = (tag ^ (tag >> 31)) * 0xD6E8FEB86659FD93ull & 0xBFFFBFFFBFFFBFFFull;
  asm volatile("" : "+s"(tag));
  float rs[4];
  f32x4 acc[5][4];
  auto geglu = [&](auto M) LAM -> u32x4 {
    constexpr int m = M.value;
    const float r2 = mul1(rs[m], rs[m]), K1 = (-2.f * 0.7978845608028654f * 1.4426950408889634f) * rs[m], K2 = K1 * 0.044715f * r2, IR2 = rcp(r2);
    u32x4 hp;
    f32x2 y[4], z[4];
    auto all = [&](auto f) LAM {
      sfor<0, 4>([&](auto i) LAM {
        const f32x4 t = acc[1 + (i.value & 2)][m], u = acc[2 + (i.value & 2)][m];
        constexpr int e = 2 * (i.value & 1);
        f(y[i.value], z[i.value], (f32x2){t[e], t[e + 1]}, (f32x2){u[e], u[e + 1]});
      });
      SBAR();
    };
    auto mul = [&](f32x2 a, f32x2 b) LAM -> f32x2 { if constexpr (m < PK0) return (f32x2){mul1(a[0], b[0]), mul1(a[1], b[1])}; else return a * b; };
    auto fma = [&](f32x2 a, float b, float c) LAM -> f32x2 {
      if constexpr (m < PK0) return (f32x2){fma1(a[0], b, c), fma1(a[1], b, c)}; else return __builtin_elementwise_fma(a, (f32x2){b, b}, (f32x2){c, c});
    };
    SBAR();
    all([&](f32x2& y, f32x2&, f32x2 t, f32x2) LAM { y = mul(t, t); });
    all([&](f32x2& y, f32x2& z, f32x2 t, f32x2 u) LAM { y = fma(y, K2, K1); z = mul(t, u); });
    all([&](f32x2& y, f32x2&, f32x2 t, f32x2) LAM { y = mul(y, t); });
    all([&](f32x2& y, f32x2&, f32x2, f32x2) LAM { y = (f32x2){ex2(y[0]), ex2(y[1])}; });
    all([&](f32x2& y, f32x2&, f32x2, f32x2) LAM { y = fma(y, IR2, IR2); });
    all([&](f32x2& y, f32x2&, f32x2, f32x2) LAM { y = (f32x2){rcp(y[0]), rcp(y[1])}; });
    all([&](f32x2& y, f32x2& z, f32x2, f32x2) LAM { y = mul(y, z); });
    sfor<0, 4>([&](auto j) LAM { hp[j.value] = cvtpk(y[j.value][0], y[j.value][1]); });
    PIN(hp);
    STAMPG(6 + m);
    return hp;
  };
  auto frag_off = [&]() LAM -> u32 { const u32 l = lane_id(); return (l >> 4) * 512 + (l & 15) * 16; };
  u32x4 hp[4];

  auto phase1 = [&](auto T_) LAM {
    constexpr int T = T_.value;
    u32 xa[2][2], fa[4], qa, vl_, vcs, bt, vq1, vq2, vv[4];
    const bool isq = quad < 2;
    const u32 ts = isq ? 2048 : 4096;
    {
      const int lane = lane_id(), g4 = lane >> 4, tok = lane & 15, q4 = tok >> 2, p4 = lane & 3;
      xa[0][0] = (mg * 64 + tok) * 128 + ((g4 ^ (tok >> 1)) << 4);
      xa[0][1] = xa[0][0] ^ 64;
      xa[1][0] = xa[0][0] + 65536;
      xa[1][1] = xa[0][1] + 65536;
      sfor<0, 4>([&](auto t) LAM {
        if constexpr (T == 0) fa[t.value] = 6 * 16384 + g4 * 2048 + q4 * 128 + ((64 * g + 16 * (t.value & 2)) ^ ((q4 & 2) << 5) ^ ((g4 & 1) << 5)) + 8 * p4;
        else fa[t.value] = 6 * 16384 + g4 * 2048 + q4 * 256 + ((128 * g + 32 * t.value + 8 * p4) ^ (q4 * 64) ^ ((g4 & 1) << 5));
      });
      qa = L_QK + g4 * 512 + q4 * 64 + ((32 * g + 8 * p4) ^ ((g4 & 1) << 5));
      PIN(xa[0][0]); PIN(xa[0][1]); PIN(xa[1][0]); PIN(xa[1][1]); PIN(fa[0]); PIN(fa[2]);
      if constexpr (T == 1) { PIN(fa[1]); PIN(fa[3]); }
      vl_ = lane * 16;
      bt = tok * 4;
      vcs = mg * 1024 + g4 * 4096 + tok * 16;
      vcs = lane < (T == 0 && mg == 0 ? 32 : 16) ? vcs : OOB;
      PIN(vl_); PIN(bt); PIN(vcs);
      if constexpr (T == 0) {
        const u32 G = 4 * cl + 2 * g + (lane >> 5), p = (G * 37) >> 8, r = G - 7 * p;
        vq2 = (isq ? vl_ & 0x1f0u : (vl_ & 0x30u) | ((vl_ & 0xc0u) << 1) | ((vl_ & 0x100u) >> 2)) + p * (128 * ts);
        vq1 = vq2 + (r >> 2) * (64 * ts) + (r & 3) * 512;
        vq2 = r == 3 ? vq1 + 64 * ts : OOB;
        PIN(qa); PIN(vq1); PIN(vq2);
      }
      if constexpr (T == 1) sfor<0, 4>([&](auto t) LAM {
        const u32 c = 64 * g + 16 * t.value + tok, hd = (c * 147) >> 12;
        vv[t.value] = hd * (64 * 4096 - 28 * 16) + c * 16 + g4 * 512;
        PIN(vv[t.value]);
        acc[4][t.value] = (f32x4){};
        PIN(acc[4][t.value]);
      });
    }
    u32x4 one = {0x3f803f80u, 0x3f803f80u, 0x3f803f80u, 0x3f803f80u};
    PIN(one);
    u32x4 xf[4][4], wf[4][5], sg, cf[5], nm[4];
    f32x4 c0 = {}, c1 = {};
    PIN(c0); PIN(c1);
    const u32 scs = WS_CS + (cb * 2 + g) * 5120, sst = WS_ST + (rb * 256 + mg * 64) * 16;
    f32x4 rv[4];
    float rl, sc[4];
    u32 nl;

    SBAR();
    sfor<0, 7>([&](auto i) LAM { if constexpr (T == 0 || i.value != 2) piece(IC<1>{}, i); });
    sync_stage<7 - T>();
    STAMP(1);
    auto rd = [&](auto J, auto R) LAM {
      constexpr int j = J.value, r = R.value, sl = (j / 2) % 3, s = j & 1;
      if constexpr (r >= 2 && r < 6) xf[j & 3][r - 2] = lds128(xa[sl >> 1][s] + (sl & 1) * 32768 + (r - 2) * 2048);
      else {
        constexpr int t = r < 2 ? 0 : (r - 4) / 2, h = r & 1;
        u32x2 v;
        if constexpr (t == 0) v = trd(qa + sl * 4096 + s * 2048 + h * 256);
        else if constexpr (T == 0) v = trd(fa[(t - 1) & 2] + sl * 16384 + s * 8192 + ((t - 1) & 1) * 1024 + h * 512);
        else v = trd(fa[t - 1] + sl * 16384 + s * 8192 + h * 1024);
        wf[j & 3][t][2 * h] = v[0];
        wf[j & 3][t][2 * h + 1] = v[1];
      }
    };
    sfor<2 * T, 14>([&](auto r) LAM { rd(IC<0>{}, r); });
    SBAR();
    {
      u32 d = 0, vs = OOB;
      if (g != 0) {
        float a[16], t = 0.f, q = 0.f;
        sfor<0, 8>([&](auto i) LAM {
          widen(a[2 * i.value], a[2 * i.value + 1], xr[i.value >> 2][i.value & 3]);
          t += add1(a[2 * i.value], a[2 * i.value + 1]);
        });
        t = half_sum(t) * (-1.f / 448.f);
        const float m0 = asf(__builtin_amdgcn_readlane(asu(t), 31)), m1 = asf(__builtin_amdgcn_readlane(asu(t), 63)), nmean = vl_ < 512 ? m0 : m1;
        sfor<0, 16>([&](auto i) LAM { const float e = add1(a[i.value], nmean); q = __builtin_fmaf(e, e, q); });
        const float var = half_sum((vl_ & 511) < 448 ? q : 0.f) * (1.f / 448.f);
        d = cvtpk(nmean, __builtin_amdgcn_sqrtf(bfround(bfround(var) + 1e-5f)));
        vs = (vl_ & 511) == 496 ? vl_ >> 5 & 16 : OOB;
      }
      st128(rWS, (u32x4){d, (u32)tag, d, (u32)(tag >> 32)}, vs, WS_ST + row * 16);
    }
    SBAR();
    u32 P[4], n[2][4], last = 0;
    auto qk_put = [&](auto M) LAM {
      constexpr int m = M.value;
      sfor<0, 4>([&](auto e) LAM { acc[0][m][e.value] = mul1(acc[0][m][e.value], sc[m]); });
      P[2 * (m & 1)] = cvtpk(acc[0][m][0], acc[0][m][1]);
      P[2 * (m & 1) + 1] = cvtpk(acc[0][m][2], acc[0][m][3]);
      if constexpr (m & 1) {
        swap16(P[0], P[2]);
        swap16(P[1], P[3]);
        const u32 so = (isq ? WS_Q : WS_KV) + ((quad & 1) * 512 + rb * 8 + mg * 2 + (m >> 1)) * ts;
        st128(rWS, (u32x4){P[0], P[1], vq2 == OOB ? P[2] : 0u, vq2 == OOB ? P[3] : 0u}, vq1, so);
        st128(rWS, (u32x4){P[2], P[3], 0u, 0u}, vq2, so);
      } else { PIN(P[0]); PIN(P[1]); }
    };
    auto norm = [&](auto I) LAM {
      constexpr int i = I.value;
      if constexpr (i < 4) {
        const f32x4 a = acc[0][i];
        const u32 v = asu(fma1(a[0], a[0], mul1(a[1], a[1])) + fma1(a[2], a[2], mul1(a[3], a[3])));
        n[0][i] = (int)vl_ < (7 - cl - 4 * g) * 256 ? v : 0u;
        n[1][i] = v ^ n[0][i];
        PIN(n[0][i]); PIN(n[1][i]);
      } else if constexpr (i < 6) {
        u32* const v = n[i & 1];
        swap16(v[0], v[1]);
        swap16(v[2], v[3]);
        v[0] = asu(add1(asf(v[0]), asf(v[1])));
        v[2] = asu(asf(v[2]) + asf(v[3]));
        swap32(v[0], v[2]);
        v[0] = asu(asf(v[0]) + asf(v[2]));
        PIN(v[0]);
      } else {
        swap32(n[0][0], n[1][0]);
        const u32 v = half_max(n[0][0] > n[1][0] ? n[0][0] : n[1][0]);
        if ((vl_ & 511) == 496) __hip_atomic_fetch_max((LAS u32*)(L_CNT + 64 + 8 * g + (vl_ >> 7 & 4)), v, __ATOMIC_RELAXED, __HIP_MEMORY_SCOPE_WORKGROUP);
      }
    };
    auto arrive = [&]() LAM {
      cfence();
      if (vl_ == 0) last = __hip_atomic_fetch_add((LAS u32*)L_CNT, 1u, __ATOMIC_RELAXED, __HIP_MEMORY_SCOPE_WORKGROUP);
      cfence();
    };
    auto tell = [&]() LAM {
      if (__builtin_amdgcn_readfirstlane(last) == 7) {
        const u32 lane = lane_id(), l = lane & 15, p = lane >> 4;
        if constexpr (T == 0) {
          const f32x4 k = lds128f(L_CNT + 64);
          const u32 k2 = p == 0 ? asu(k[0] + k[2]) : p == 1 ? asu(k[1] + k[3]) : 0u;
          const u32 sd = isq ? 16 : 256, ex = cl == 0 ? sd : cl == 6 ? 16 * BOX : OOB;
          const u32 o = l * BOX + (p == 0 ? 0u : p == 1 ? 16 * BOX + sd : ex);
          st128(rWS, (u32x4){k2, (u32)tag, k2, (u32)(tag >> 32)}, l < (isq ? 2 : 16) && p < 3 && o < OOB ? o : OOB,
                WS_FQ + ((quad & 1) * 8 + cl) * (16 * BOX) + (isq ? 2 * rb * BOX + 24 * 16 : rb * 16));
        } else st128(rWS, (u32x4){0u, (u32)tag, 0u, (u32)(tag >> 32)}, lane * BOX, WS_FQ + quad * (64 * BOX) + (8 + rb) * 16);
      }
      STAMP(13);
    };
    auto v_put = [&](auto I) LAM {
      constexpr int t = 1 + I.value / 2, u = I.value & 1;
      u32x4 V;
      sfor<0, 4>([&](auto e) LAM {
        constexpr int m = 2 * u + e.value / 2, c = 2 * (e.value & 1);
        V[e.value] = cvtpk(mul1(acc[t][m][c], rv[m][c]), mul1(acc[t][m][c + 1], rv[m][c + 1]));
      });
      st128(rWS, V, vv[t - 1], WS_KV + (quad * 256 + rb * 8 + mg * 2 + u) * 4096 + 2048);
    };
    sfor<0, 14>([&](auto J) LAM {
      constexpr int j = J.value, G = j / 2;
      sfor<0, slots(T, j)>([&](auto I) LAM {
        constexpr int i = I.value, t = slot_tile(T, j, i), js = slot_step(T, j, i), m = i % 4;
        if constexpr (T == 1 && t == 4) {
          if constexpr (m == 0) mfma4_unless(acc[4], xf[j & 3], wf[j & 3][4], g);
        } else if constexpr (T == 1) mac16<j == 0>(acc[t][m], xf[j & 3][m], wf[j & 3][t]);
        else if constexpr (js < 0) mac16<false>(acc[0][m], cf[0], nm[m]);
        else mac16<j == 0>(acc[t][m], wf[js & 3][t], xf[js & 3][m]);
        SBAR();
        if constexpr ((j & 1) && i == 1 && j < 13) sync_stage<(T ? (G == 5 ? 1 : 7 - (G & 1)) : G == 1 ? 7 : 8) + (TWM >> j & 1)>();
        if constexpr (T == 0 && j == 10 && i == 5) sync_stage<5>();
        if constexpr ((j & 1) && i == 1 && j < 13) STAMPF(5 + G);
        if constexpr (i == 1 && (j == TJ2 || j == TJ2 + 2) && (TW & 2)) touch_b(sD, vl_ < 25 * 16 ? vl_ * 8 : OOB, ((bi * 8 + w) * 2 + (j - TJ2) / 2) * (25 * 128));
        if constexpr (i == 1 && j == TJ1 && (TW & 1)) touch_b(sD, vl_ < 7 * 16 ? vl_ * 8 : OOB, (bid * 8 + w) * (7 * 128));
        constexpr int r = slot_read(T, j, i);
        if constexpr (r >= 0) rd(IC<(r >= 0 ? r / 16 : 0)>{}, IC<(r >= 0 ? r % 16 : 0)>{});
        if constexpr (j < 4 && i == 3) colsum<T>(c0, c1, one, wf[j & 3], (0xf0700e0fu >> (8 * j + rb)) & 1 ? mg : 4);
        if constexpr (j == 3 && i == 8) {
          PIN(c0); PIN(c1);
          const float a0 = c0[0], a1 = c1[0], v = vl_ < 256 ? a0 : a1, hi = bfround(v);
          __builtin_amdgcn_raw_buffer_store_b64((u32x2){cvtpk(hi, v - hi), (u32)(tag >> (32 * (rb & 1)))}, rWS, vcs, scs + (rb >> 1) * 256 + (rb & 1) * 8, 16);
        }
        constexpr int pc = piece_at(T, j, i), H = (j + 5) / 2;
        if constexpr (pc >= 0 && H < 7) piece(IC<H>{}, IC<(pc >= 0 ? pc : 0)>{});
        if constexpr (T == 0 && (j == 9 || j == 11) && pc >= 0) {
          if constexpr (pc == 0) so = od + krot(2 * (j - 9) + 2 * g) * 14336;
          if constexpr (pc == 2) so = opaque(so) + 12288;
          dma1<(j - 7) * 16384, pc * 1024>(sD, (pc & 1) ? vw1 : vw0, so, 4 * lw);
        }
        constexpr int jn = 9 + 2 * T;
        if constexpr (j == jn - 2 && i == 0) aload_sc1(sg, vl_, sWS, sst);
        if constexpr (T == 0 && j == 8 && i == 0) aload_sc1(cf[0], vl_, sWS, scs + 4096);
        if constexpr (j == 9 && i == 1) dma_sc1(sWS, vl_, scs + mg * 1024, 7 * 16384 + lw);
        if constexpr (T == 1 && j == 9 && i == 8) st128(rWS, one, (vl_ >> 8) * (64 * 4096) + (vl_ & 0xc0) * 8 + (vl_ & 48) + 448, WS_KV + (quad * 256 + rb * 8 + w) * 4096 + 2048);
        if constexpr (j == 11 && i == 0) { vcs = 7 * 16384 + g * 4096 + vl_; PIN(vcs); }
        if constexpr (j == 11 && i == 1) sfor<1, 5>([&](auto u) LAM { cf[u.value] = lds128(vcs + (u.value - 1) * 1024); });
        if constexpr (j == jn - 1 && i >= 10 && i < 14) { nm[i - 10] = (u32x4){}; PIN(nm[i - 10]); }
        if constexpr (j == jn && i == 3) {
          __builtin_amdgcn_s_setprio(3);
          PIN(sg);
          await(sg, vl_, sWS, sst, tag);
        }
        if constexpr (T == 0 && j == 10 && i == 7) { PIN(cf[0]); await(cf[0], vl_, sWS, scs + 4096, tag); }
        if constexpr (j == 11 && i == 6) {
          sfor<1, 5>([&](auto u) LAM { PIN(cf[u.value]); });
#pragma nounroll
          for (u32 spins = 0; spins < (1u << 20); ++spins) {
            if ((stale(cf[1], tag) | stale(cf[2], tag) | stale(cf[3], tag) | stale(cf[4], tag)) == 0) break;
            sfor<1, 5>([&](auto u) LAM { aload_sc1(cf[u.value], vl_, sWS, scs + (u.value - 1) * 1024); });
            vm_on2<0>(cf[1], cf[2]); vm_on2<0>(cf[3], cf[4]);
          }
        }
        if constexpr (j == jn && i == 8) {
          rl = rcp(asf(sg[0] & 0xffff0000u));
          nl = __builtin_amdgcn_perm(sg[0], sg[0], 0x01000100u);
          if constexpr (T == 1) *(LAS float*)(L_RV + mg * 256 + (vl_ >> 2)) = rl;
        }
        if constexpr (j == jn && i >= 12 && i < 16) {
          nm[i - 12][0] = __builtin_amdgcn_ds_bpermute(bt + 64 * (i - 12), nl);
          PIN(bt);
          nm[i - 12][2] = __builtin_amdgcn_ds_bpermute(bt + 64 * (i - 12), nl);
        }
        if constexpr (T == 0 && j == 9 && i >= 16) rs[i - 16] = asf(__builtin_amdgcn_ds_bpermute(bt + 64 * (i - 16), asu(rl)));
        if constexpr (T == 0 && j == 10 && i == 26) sfor<0, 4>([&](auto e) LAM {
          sc[e.value] = rs[e.value] * (isq ? 0.18898223650461362f * 1.4426950408889634f : 1.f);
          PIN(sc[e.value]);
        });
        if constexpr (T == 0 && j == 10 && i >= 29 && i < 33) qk_put(IC<(i >= 29 && i < 33 ? i - 29 : 0)>{});
        if constexpr (T == 0 && j == 10 && i == 33) { __builtin_amdgcn_s_setprio(2); STAMPF(11); }
        if constexpr (T == 0 && j == 11 && i >= 8 && i < 15) norm(IC<(i >= 8 && i < 15 ? i - 8 : 0)>{});
        if constexpr (j == 12) {
          constexpr int tc = (i + 8) % 16 / 4 + 1;
          SBAR();
          if constexpr (T == 1) mac16<false>(acc[tc][m], nm[m], cf[tc]);
          else mac16<false>(acc[tc][m], cf[tc], nm[m]);
          SBAR();
          if constexpr (T == 1 && i == 10) { vcs = L_RV + mg * 256 + ((vl_ >> 4) & 48); PIN(vcs); }
          if constexpr (T == 1 && i >= 10 && i < 14) rv[i - 10] = lds128f(vcs + 64 * (i - 10));
          if constexpr (T == 0 && i == DW) { vmwait<4>(); arrive(); }
          if constexpr (T == 0 && i == DW + 2) { tell(); __builtin_amdgcn_s_setprio(1); }
        }
        if constexpr (T == 0 && WL > 0 && j == 12 && i >= WL && i < WL + 12 && ((i - WL) & 3) == 0) {
          constexpr int c = (i - WL) / 4;
          if constexpr (c == 0) so = od + krot(10 + 2 * g) * 14336;
          if constexpr (c < 2) dma2<0, 2048 * c>(sD, vw0, sD, vw1, opaque(so) + 12288 * c, mg * 4096 + (g ? 0 : 8 * 16384));
          else dma2<16384, 0>(sD, vw0, sD, vw1, od + krot(14) * 14336 + g * 14336, mg * 4096 + g * 2048);
        }
        if constexpr (T == 0 && j == 13 && i == 0) cnt_add(20);
        if constexpr (T == 1 && j == 13 && i >= 5 && (i & 1)) v_put(IC<(i >= 5 ? (i - 5) / 2 : 0)>{});
        SBAR();
      });
    });
    asm volatile("s_nop 7");
    SBAR();
    STAMP(2);
    if constexpr (T == 1) {
      if (g == 0) { v_put(IC<6>{}); v_put(IC<7>{}); }
      vmwait<0>();
      arrive();
      tell();
    }
    STAMP(3);
    __builtin_amdgcn_s_setprio(0);
    if (PGA && g == 0) __builtin_amdgcn_s_setprio(PGA);
    SBAR();
    if constexpr (T == 0) sfor<0, PI>([&](auto m) LAM { hp[m.value] = geglu(m); PIN(hp[m.value]); });
  };
  if (!t1) phase1(IC<0>{}); else phase1(IC<1>{});
  LANES();
  auto wload = [&](auto B) LAM {
    const u32 so = od + krot(2 * B.value) * 14336;
    dma2<wd_loc(2 * B.value), 0>(sD, vw0, sD, vw1, so, mg * 4096);
    dma2<wd_loc(2 * B.value), 2048>(sD, vw0, sD, vw1, so + 12288, mg * 4096);
  };
  if (w < 4) {
    if (PRIO) __builtin_amdgcn_s_setprio(2);
    u32x4 fk;
    const u32 vfo = (lane < 26 ? lane : 0) * 16, sfo = WS_FQ + (head * 16 + qb) * BOX;
    u32 seen = 0;
    auto inbox = [&](u32 blocks) LAM {
      u32 need = blocks << (qb >> 1);
      need = ((need | need >> 8) & 255) * 0x10101 + 0x3000000;
#pragma nounroll
      for (u32 spins = 0; (seen & need) != need && spins < (1u << 20); ++spins) {
        if (spins) __builtin_amdgcn_s_sleep(NAPA);
        aload_sc1(fk, vfo, sWS, sfo);
        vm_on<0>(fk);
        seen = ~(u32)stale(fk, tag);
      }
    };
    auto hand = [&](auto M0) LAM {
      sfor<M0.value, 4>([&](auto m) LAM { hp[m.value] = geglu(m); PIN(hp[m.value]); });
      cnt_wait(20, 8);
      STAMPG(10);
      sfor<0, 4>([&](auto m) LAM { *(LAS u32x4*)(6 * 16384 + w * 4096 + m.value * 1024 + vl) = hp[m.value]; });
      cnt_add(12 + w);
    };
    if (has_tile) {
      aload_sc1(fk, vfo, sWS, sfo);
      STAMPG(5);
      if constexpr (GA == 4) hand(IC<PI>{});
      else sfor<PI, GA>([&](auto m) LAM { hp[m.value] = geglu(m); PIN(hp[m.value]); });
      STAMP(3);
      vm_on<0>(fk);
      seen = ~(u32)stale(fk, tag);
    }
    STAMP(4);
    inbox((1u << NR1) - 1);
    STAMP(5);
    const u32 kvb = WS_KV + (head * 64 + 2 * w) * 4096;
    u32 kvc = 0;
    auto kv_next = [&](int kt) LAM {
      if (kt & 1) kvc = opaque(kvc) ^ 4096;
      else kvc = kvb + (qb & 1) * 4096 + ((opaque(qb >> 1) + (kt >> 1)) & 7) * 32768;
    };
    u32x4 Q[4][2], KV[4][4];
    auto q_load = [&](auto t) LAM {
      const u32 so = WS_Q + (head * 64 + qb * 4 + ((w + 1 + t.value) & 3)) * 2048;
      Q[t.value][0] = ld128(rWS, vl, so);
      Q[t.value][1] = ld128(rWS, vl + 1024, so);
    };
    kv_next(0);
    KV[0][0] = ld128(rWS, vl, kvc);
    KV[0][1] = ld128(rWS, vl + 1024, kvc);
    q_load(IC<0>{});
    q_load(IC<1>{});
    KV[0][2] = ld128(rWS, vl + 2048, kvc);
    q_load(IC<2>{});
    KV[0][3] = ld128(rWS, vl + 3072, kvc);
    q_load(IC<3>{});
    SBAR();
    sfor<1, UPF>([&](auto b) LAM {
      kv_next(b.value);
      sfor<0, 4>([&](auto i) LAM { KV[b.value][i.value] = ld128(rWS, vl + i.value * 1024, kvc); });
    });
    SBAR();
    if (C3E) cnt_add(3);
    if constexpr (GA < 4) { if (has_tile) hand(IC<GA>{}); }
    inbox((1u << NR) - 1);
    if (!C3E) cnt_add(3);
    STAMP(6);
    const int idx = 0xDDDD;
    f32x16 O[4] = {};
    const f32x16 Z = {};
    auto rgn = [&](int t) LAM -> u32 { return (2 + t) * 16384u; };
    auto put_part = [&](auto t, auto i) LAM {
      const f32x16& o = O[t.value];
      *(LAS f32x4*)(rgn((w + 1 + t.value) & 3) + t.value * 4096 + i.value * 1024 + vl) =
          (f32x4){o[4 * i.value], o[4 * i.value + 1], o[4 * i.value + 2], o[4 * i.value + 3]};
    };
    f32x16 Sx[4];
    u32 cb1 = 0;
    constexpr int CK = 8 * NR - 12, CP = CK - (NR > 3 ? 10 : 8);
    auto tile = [&](auto I) LAM {
      constexpr int kt = I.value >> 2, b = kt & 3, t = I.value & 3, tn = (t + 1) & 3, tp = (t + 3) & 3;
      constexpr int bn = t == 3 ? (b + 1) & 3 : b, bp = t == 0 ? (b + 3) & 3 : b;
      constexpr int E1 = PAT ? 3 : 4, E2 = 2 * E1, E3 = 3 * E1;
      auto exps = [&](auto lo, auto hi) LAM {
        SBAR();
        sfor<lo.value, hi.value>([&](auto e) LAM { Sx[t][e.value] = ex2(Sx[t][e.value]); });
        PIN(Sx[t]);
        SBAR();
      };
      if constexpr (I.value < 63) Sx[tn] = mfma32(KV[bn][0], Q[tn][0], Z);
      exps(IC<0>{}, IC<E1>{});
      if constexpr (I.value < 63) Sx[tn] = mfma32(KV[bn][1], Q[tn][1], Sx[tn]);
      if constexpr (I.value < 63) PIN(Sx[tn]);
      exps(IC<E1>{}, IC<E2>{});
      if constexpr (I.value > 0) O[tp] = smf_hi(KV[bp][3], Sx[tp], O[tp], idx);
      PIN(O[tp]);
      exps(IC<E2>{}, IC<E3>{});
      O[t] = smf_lo(KV[b][2], Sx[t], O[t], idx);
      PIN(O[t]);
      exps(IC<E3>{}, IC<E3 + 2>{});
      if constexpr (I.value == CP) aload_sc1(fk, vfo, sWS, sfo);
      constexpr int NX = 4 * (3 - UPF);
      sfor<(I.value < NX ? 4 * UPF + 2 * I.value : I.value + 12), (I.value < NX ? 4 * UPF + 2 * I.value + 2 : I.value + 13)>([&](auto L) LAM {
        if constexpr (L.value < 64) {
          if constexpr ((L.value & 3) == 0) kv_next(L.value >> 2);
          KV[(L.value >> 2) & 3][L.value & 3] = ld128(rWS, vl + (L.value & 3) * 1024, kvc);
        }
      });
      if constexpr (I.value == ID) cnt_add(9);
      if constexpr (I.value == 58) cb1 = cnt_get(1);
      if constexpr (I.value == 60) { if (has_tile && __builtin_amdgcn_readfirstlane(cb1) < 4u) cnt_wait(1, 4); cfence(); }
      if constexpr (I.value >= 62) sfor<0, 4>([&](auto i) LAM { put_part(IC<I.value - 62>{}, i); });
      exps(IC<E3 + 2>{}, IC<16>{});
    };
    Sx[0] = mfma32(KV[0][1], Q[0][1], mfma32(KV[0][0], Q[0][0], Z));
    SBAR();
    sfor<0, CK>(tile);
    vm_on<CK - CP>(fk);
    await(fk, vfo, sWS, sfo, tag);
    float bound = asf(__builtin_amdgcn_readlane(fk[0], 24)) + asf(__builtin_amdgcn_readlane(fk[0], 25));
    {
      u32 ka_ = fk[0], kb_ = fk[2];
      swap16(ka_, kb_);
      bound *= max8_nonneg(asf(ka_) + asf(kb_));
    }
    PIN(bound);
    sfor<CK, 64>(tile);
    O[3] = smf_hi(KV[3][3], Sx[3], O[3], idx);
    if (__builtin_amdgcn_ballot_w64(!(bound <= 2200.f)) != 0) {
      sfor<0, 4>([&](auto t) LAM { O[t.value] = Z; });
      auto kvo = [&](int kt) LAM -> u32 { return kvb + (kt >> 1) * 32768 + (kt & 1) * 4096; };
      float mx[4] = {-3.0e38f, -3.0e38f, -3.0e38f, -3.0e38f};
      for (int kt = 0; kt < 16; ++kt) {
        const u32x4 k0 = ld128(rWS, vl, kvo(kt)), k1 = ld128(rWS, vl + 1024, kvo(kt));
        sfor<0, 4>([&](auto t) LAM {
          const f32x16 s = mfma32(k1, Q[t.value][1], mfma32(k0, Q[t.value][0], Z));
          sfor<0, 16>([&](auto i) LAM { mx[t.value] = __builtin_fmaxf(mx[t.value], s[i.value]); });
        });
      }
      sfor<0, 4>([&](auto t) LAM {
        mx[t.value] = __builtin_fmaxf(mx[t.value], other_half(mx[t.value]));
        *(LAS float*)(L_VT + ((((w + 1 + t.value) & 3) * 32 + r32) * 4 + w) * 4) = mx[t.value];
      });
      cnt_add(8);
      cnt_wait(8, 4);
      sfor<0, 4>([&](auto t) LAM {
        const f32x4 m4 = lds128f(L_VT + (((w + 1 + t.value) & 3) * 32 + r32) * 16);
        mx[t.value] = __builtin_fmaxf(__builtin_fmaxf(m4[0], m4[1]), __builtin_fmaxf(m4[2], m4[3]));
      });
      for (int kt = 0; kt < 16; ++kt) {
        const u32x4 k0 = ld128(rWS, vl, kvo(kt)), k1 = ld128(rWS, vl + 1024, kvo(kt));
        const u32x4 v0 = ld128(rWS, vl + 2048, kvo(kt)), v1 = ld128(rWS, vl + 3072, kvo(kt));
        sfor<0, 4>([&](auto t) LAM {
          f32x16 s = mfma32(k1, Q[t.value][1], mfma32(k0, Q[t.value][0], Z));
          sfor<0, 16>([&](auto i) LAM { s[i.value] = ex2(s[i.value] - mx[t.value]); });
          O[t.value] = smf_hi(v1, s, smf_lo(v0, s, O[t.value], idx), idx);
        });
      }
      sfor<0, 2>([&](auto t) LAM { sfor<0, 4>([&](auto i) LAM { put_part(t, i); }); });
    }
    STAMP(7);
    sfor<0, 4>([&](auto i) LAM { put_part(IC<2>{}, i); });
    cnt_add(2);
    cnt_wait(2, 4);
    const u32 red = rgn(w);
    {
      LANES();
      float o[16];
      sfor<0, 4>([&](auto i) LAM {
        const u32 ra = red + i.value * 1024 + vl;
        const f32x4 a0 = lds128f(ra), a1 = lds128f(ra + 4096), a2 = lds128f(ra + 8192);
        sfor<0, 4>([&](auto j) LAM {
          if constexpr (CPK) o[4 * i.value + j.value] = (a0[j.value] + a1[j.value]) + (a2[j.value] + O[3][4 * i.value + j.value]);
          else o[4 * i.value + j.value] = add1(add1(a0[j.value], a1[j.value]), add1(a2[j.value], O[3][4 * i.value + j.value]));
        });
      });
      const float lo_ = other_half(o[12]);
      const float il = rcp(hf ? o[12] : lo_);
      u32 P[8];
      sfor<0, 8>([&](auto i) LAM { P[i.value] = CPK ? cvtpk(o[2 * i.value] * il, o[2 * i.value + 1] * il) : cvtpk(mul1(o[2 * i.value], il), mul1(o[2 * i.value + 1], il)); });
      const int om = qb * 4 + w;
      st128(rWS, (u32x4){P[0], P[1], P[2], P[3]}, vl, WS_O + (om * 16 + head) * 2048);
      st128(rWS, (u32x4){P[4], P[5], hf ? 0u : P[6], hf ? 0u : P[7]}, vl + 1024, WS_O + (om * 16 + head) * 2048);
      if (has_tile) {
        const u32x4 sO = mk_srd(pwo, D_ * D_ * 2);
        const u32 vw = (lane >> 3) * 896 + (((lane & 7) ^ ((lane >> 2) & 4)) << 4);
        sfor<0, 14>([&](auto e) LAM { dma_i<e.value * 1024, 8 * e.value * 896>(sO, vw, 112 * w * 896 + nt * 128, red); });
        vmwait<14>();
      } else vmwait<0>();
      if (lane == 0) put_flag(ws, WS_FO + (om * 16 + head) * FSO, tag);
    }
    STAMP(8);
    if (!has_tile) { STAMP_END(); return; }
    {
    LANES();
    const u32 trow = (4 * hf + q4) * 128, swz = (q4 & 2) << 5;
    const int m = w & 1, kh = w >> 1;
    const rsrc_t rX = __builtin_amdgcn_make_buffer_rsrc((void*)px, 0, S_ * D_ * 2, 0x00020000);
    const u32 xo = ((rt * 64 + m * 32 + r32) * 448 + nt * 64 + kh * 32 + hf * 8) * 2;
    u32x4 xr[2];
    xr[0] = __builtin_amdgcn_raw_buffer_load_b128(rX, xo, 0, 0);
    xr[1] = __builtin_amdgcn_raw_buffer_load_b128(rX, xo + 32, 0, 0);
    PIN(xr[0]); PIN(xr[1]);
    vmwait<0>();
    cnt_add(6);
    u32x4 of[16];
    poll_flags<NAPO>(ws, WS_FO + ((2 * rt + m) * 16 + 8 * kh + (lane & 7)) * FSO, tag);
    STAMP(9);
    sfor<0, 16>([&](auto k) LAM { of[k.value] = ld128(rWS, vl + (k.value & 3) * 1024, WS_O + ((2 * rt + m) * 16 + 8 * kh) * 2048 + (k.value >> 2) * 4096); });
    SBAR();
    cnt_wait(6, 4);
    cnt_wait(5, 4);
    f32x4 gt[4];
    sfor<0, 4>([&](auto i) LAM { gt[i.value] = lds128f(1 * 16384 + w * 4096 + i.value * 1024 + vl); });
    u32 xs[8] = {xr[0][0], xr[0][1], xr[0][2], xr[0][3], xr[1][0], xr[1][1], xr[1][2], xr[1][3]};
    swap32(xs[0], xs[2]); swap32(xs[1], xs[3]); swap32(xs[4], xs[6]); swap32(xs[5], xs[7]);
    f32x16 acc[2] = {};
    sfor<0, 2>([&](auto r) LAM {
      const u32 rg = rgn(2 * kh + r.value);
      u32 wa[2], wp[2];
      sfor<0, 2>([&](auto n) LAM {
        wa[n.value] = rg + trow + ((64 * (n.value ^ kh) + 32 * blk + 8 * p4) ^ swz);
        wp[n.value] = wa[n.value] - hf * 512;
      });
      sfor<0, 8>([&](auto k) LAM {
        constexpr int hh = k.value >> 1, s = k.value & 1;
        constexpr u32 off = (28 * hh + 16 * s) * 128;
        sfor<0, 2>([&](auto n) LAM {
          constexpr int nn = 1 - n.value;
          const u32x4 wf = trd2(wa[nn] + off, (s ? wp[nn] : wa[nn]) + off + 1024);
          acc[nn] = mfma32(wf, of[8 * r.value + k.value], acc[nn]);
        });
      });
    });
    sfor<0, 4>([&](auto i) LAM {
      *(LAS f32x4*)(w * 4096 + i.value * 1024 + vl) =
          (f32x4){acc[1][4 * i.value], acc[1][4 * i.value + 1], acc[1][4 * i.value + 2], acc[1][4 * i.value + 3]};
    });
    STAMP(10);
    cnt_add(7);
    cnt_wait(7, 4);
    u32 R[8];
    sfor<0, 4>([&](auto i) LAM {
      const f32x4 a = lds128f((w ^ 2) * 4096 + i.value * 1024 + vl);
      float y[4];
      sfor<0, 4>([&](auto j) LAM {
        const u32 xd = xs[2 * i.value + (j.value >> 1)];
        const float xv = asf((j.value & 1) ? (xd & 0xffff0000u) : (xd << 16));
        y[j.value] = __builtin_fmaf(acc[0][4 * i.value + j.value] + a[j.value], gt[i.value][j.value], xv);
      });
      R[2 * i.value] = cvtpk(y[0], y[1]);
      R[2 * i.value + 1] = cvtpk(y[2], y[3]);
    });
    swap32(R[0], R[2]); swap32(R[1], R[3]); swap32(R[4], R[6]); swap32(R[5], R[7]);
    const rsrc_t rOut = __builtin_amdgcn_make_buffer_rsrc(pout, 0, S_ * D_ * 2, 0x00020000);
    st128p(rOut, (u32x4){R[0], R[1], R[2], R[3]}, xo);
    st128p(rOut, (u32x4){R[4], R[5], R[6], R[7]}, xo + 32);
    asm volatile(".Lfb_enda:");
    STAMP(11);
    STAMP_END();
    }
  } else {
    const u32 trow = (4 * hf + q4) * 128, swz = (q4 & 2) << 5;
    const int gw = w - 4;
    const u32 lg = gw * 4096;
    if (has_tile) {
      u32x4 ha[4];
      sfor<PI, 4>([&](auto m) LAM { hp[m.value] = geglu(m); PIN(hp[m.value]); });
      STAMP(4);
      cnt_wait(12 + gw, 1);
      sfor<0, 4>([&](auto m) LAM { ha[m.value] = lds128(6 * 16384 + lg + m.value * 1024 + vl); PIN(ha[m.value]); });
      if constexpr (WE == 2) { wload(IC<4>{}); wload(IC<8>{}); }
      const u32 fo = frag_off(), ho = WS_H + ((rb * 8 + gw * 2) * 112 + un * 4) * 1024;
      cnt_wait(3, HCNT);
      if constexpr (WE == 1) { wload(IC<4>{}); wload(IC<8>{}); }
      STAMP(9);
      sfor<0, 8>([&](auto i) LAM {
        constexpr int m = i.value >> 1;
        st128(rWS, (i.value & 1) ? hp[m] : ha[m], fo + (m & 1) * 256 + (i.value & 1) * 2048, ho + (m >> 1) * (112 * 1024));
        if constexpr (i.value < 7) __builtin_amdgcn_s_sleep(HP);
      });
      if constexpr (WE == 3) { wload(IC<4>{}); wload(IC<8>{}); vmwait<8>(); }
      else vmwait<0>();
      if (lane == 0) put_flag(ws, WS_FH + ((rb * 4 + gw) * 32 + un) * FSH, tag);
      STAMP(5);
      if constexpr (WE == 0) { wload(IC<4>{}); wload(IC<8>{}); }
      if constexpr (WL == 0) sfor<5, 8>(wload);
      touch(pwo + ((rq & 3) * 112 + gw * 28 + (lane < 28 ? lane : 0)) * 896 + nt * 128);
      STAMP(8);
    }
    if (bid == 255 && gw == 0) {
      for (u32 i = 0; i < 4; ++i) {
        u32x4 f = {0, 0, 0, (u32)~(tag >> 32)};
        await(f, lane * 128, sWS, WS_ST + i * 8192, tag);
      }
      if (lane == 0) put_flag(ws, WS_EPOCH, epoch + 1);
    }
    if (!has_tile) { STAMP_END(); return; }
    const u32x4 sH = mk_srd(ws + WS_H, 64 * 112 * 1024);
    const u32 oh0 = ((2 * rt + (gw & 1)) * 112 + 28 * gw) * 1024, oh1 = ((2 * rt + 1 - (gw & 1)) * 112 + 28 * gw) * 1024;
    u32x4 hfr[DPH][2];
    auto hload = [&](auto K) LAM {
      aload_sc1(hfr[K.value % DPH][0], vl, sH, oh0 + krot(K.value) * 1024);
      aload_sc1(hfr[K.value % DPH][1], vl, sH, oh1 + krot(K.value) * 1024);
    };
    cnt_wait(3, 4);
    poll_flags<NAPB>(ws, WS_FH + (rt * 32 + 7 * gw + (lane < 7 ? lane : 0)) * FSH, tag);
    STAMP(6);
    sfor<0, DPH>([&](auto K) LAM { hload(K); });
    __builtin_amdgcn_s_setprio(PRB);
    u32 wa[2];
    sfor<0, 2>([&](auto n) LAM { wa[n.value] = lg + trow + ((64 * (n.value ^ (gw >> 1)) + 32 * blk + 8 * p4) ^ swz); });
    f32x16 acc[2][2] = {};
    u32x4 wf[2][2];
    auto ready = [&](auto K, auto N) LAM {
      constexpr int k = K.value;
      constexpr int o = wd_loc(k), d = (o & ~65535) - (k ? (int)(wd_loc(k - 1) & ~65535) : 0);
      vm_on2<N.value>(hfr[k % DPH][0], hfr[k % DPH][1]);
      sfor<0, 2>([&](auto n) LAM {
        if constexpr (d != 0) { wa[n.value] += d; PIN(wa[n.value]); }
        wf[k & 1][n.value] = trd2(wa[n.value] + (o & 65535), wa[n.value] + (o & 65535) + 2048);
      });
    };
    ready(IC<0>{}, IC<2 * (DPH - 1)>{});
    sfor<0, 28>([&](auto K) LAM {
      constexpr int k = K.value;
      if constexpr (k == KD) cnt_wait(9, 4);
      if constexpr (k + 1 < 28 && k != 17) ready(IC<k + 1>{}, IC<gemm_wait(k + 1)>{});
      sfor<0, 2>([&](auto n) LAM {
        sfor<0, 2>([&](auto m) LAM { acc[m.value][n.value] = mfma32(wf[k & 1][n.value], hfr[k % DPH][m.value], acc[m.value][n.value]); PIN(acc[m.value][n.value]); });
      });
      SBAR();
      if constexpr (k == 7) cnt_add(1);
      if constexpr (k + DPH < 28) hload(IC<k + DPH>{});
      sfor<9, 14>([&](auto B) LAM { if constexpr (wd_at(B.value) == k) wload(B); });
      if constexpr (k == 17) ready(IC<18>{}, IC<gemm_wait(18)>{});
      SBAR();
    });
    STAMP(7);
    sfor<1, 4>([&](auto x) LAM {
      const f32x16& a = acc[x.value & 1][x.value >> 1];
      sfor<0, 4>([&](auto i) LAM {
        *(LAS f32x4*)((5 + x.value) * 16384 + lg + i.value * 1024 + vl) = (f32x4){a[4 * i.value], a[4 * i.value + 1], a[4 * i.value + 2], a[4 * i.value + 3]};
      });
    });
    cnt_add(4);
    cnt_wait(4, 4);
    sfor<0, 4>([&](auto i) LAM {
      const u32 ra = i.value * 1024 + vl;
      const f32x4 a1 = lds128f(6 * 16384 + (gw ^ 1) * 4096 + ra), a2 = lds128f(7 * 16384 + (gw ^ 2) * 4096 + ra), a3_ = lds128f(8 * 16384 + (gw ^ 3) * 4096 + ra);
      f32x4 gv;
      sfor<0, 4>([&](auto j) LAM {
        const float f = (acc[0][0][4 * i.value + j.value] + a1[j.value]) + (a2[j.value] + a3_[j.value]);
        gv[j.value] = f * rcp(1.f + ex2(f * -1.4426950408889634f));
      });
      *(LAS f32x4*)(1 * 16384 + lg + ra) = gv;
    });
    asm volatile(".Lfb_endb:");
    cnt_add(5);
    STAMP(10);
    STAMP_END();
  }
}

extern "C" void kernel_launch(void* const* d_in, const int* in_sizes, int n_in, void* d_out, int out_size, void* d_ws,
                              size_t ws_size, hipStream_t stream) {
  fused_block<<<256, 512, 0, stream>>>(d_in[0], d_in[1], d_in[2], d_in[4], d_in[5], d_in[6], d_out, (char*)d_ws);
}
```

```cpp
#include <hip/hip_runtime.h>
#include <stdint.h>
#pragma clang diagnostic ignored "-Wint-to-pointer-cast"

#define DEV __device__ __forceinline__
#define LAM __attribute__((always_inline))
#define LAS __attribute__((address_space(3)))
#define GAS __attribute__((address_space(1)))

#define STAMPS 0

typedef unsigned u32;
typedef unsigned long long u64;
typedef u32 u32x2 __attribute__((ext_vector_type(2)));
typedef u32 u32x4 __attribute__((ext_vector_type(4)));
typedef short s16x4 __attribute__((ext_vector_type(4)));
typedef float f32x2 __attribute__((ext_vector_type(2)));
typedef float f32x4 __attribute__((ext_vector_type(4)));
typedef float f32x8 __attribute__((ext_vector_type(8)));
typedef float f32x16 __attribute__((ext_vector_type(16)));
typedef __bf16 bf16x8 __attribute__((ext_vector_type(8)));
typedef __bf16 bf16x16 __attribute__((ext_vector_type(16)));

extern "C" __device__ u64 llvm_dispatch_id() __asm("llvm.amdgcn.dispatch.id");

constexpr int S_ = 2048, D_ = 448, F_ = 1792;
#define V3 1
#define V4 1
#define V5 1
#define RWS 0
#define BPK 1
#define FSQ 16
#define FSH 8
#define FSO 8
constexpr u32 WS_EPOCH = 0, WS_ST = 32768, WS_DBG = 65536;
constexpr u32 WS_Q = 1u << 20, WS_KV = 3u << 20, WS_O = 7u << 20, WS_H = 9u << 20;
constexpr u32 WS_CS = 16u << 20, WS_FO = WS_CS + (512u << 10), WS_FH = WS_FO + (256u << 10), WS_FQ = WS_FH + (256u << 10), WS_END = 18u << 20;
constexpr u32 BOX = 512;
constexpr u32 L_QK = 147456, L_CS = 159744, L_CNT = L_CS + 2560, L_DUM = L_CS + 2816, L_RV = L_CS + 3072;
constexpr u32 OOB = 0x7ffffff0;
constexpr u32 L_VT = L_QK;
constexpr u32 LDS_BYTES = 163840;

template <int V> struct IC { static constexpr int value = V; };
template <int I, int N, class Fn> DEV void sfor(Fn&& f) {
  if constexpr (I < N) { f(IC<I>{}); sfor<I + 1, N>(f); }
}

#define SBAR() __builtin_amdgcn_sched_barrier(0)
template <class T> DEV void PIN(T& x) { asm volatile("" : "+v"(x)); }
template <class T> DEV void undef(T& x) { asm volatile("" : "=v"(x)); }
template <class T> DEV void nop_on(T& x) { asm volatile("s_nop 7" : "+v"(x)); }
template <class T> DEV void lgkm0_on(T& x) { asm volatile("s_waitcnt lgkmcnt(0)" : "+v"(x)::"memory"); }
template <int T> DEV void colsum(f32x4& c0, f32x4& c1, u32x4 one, const u32x4* w, int who) {
#define CSM(k, c, r) "s_cmp_lg_u32 %8, " #k "\n\ts_cbranch_scc1 .Lcs" #r "_%=\n\tv_mfma_f32_16x16x32_bf16 %" #c ", %2, %" #r ", %" #c "\n.Lcs" #r "_%=:\n\t"
  if constexpr (T == 0)
    asm volatile(CSM(0, 0, 3) CSM(1, 0, 4) CSM(2, 0, 5) CSM(3, 0, 6) CSM(0, 1, 7)
                 : "+v"(c0), "+v"(c1) : "v"(one), "v"(w[1]), "v"(w[2]), "v"(w[3]), "v"(w[4]), "v"(w[0]), "s"(who) : "scc");
  else
    asm volatile(CSM(0, 0, 3) CSM(1, 0, 4) CSM(2, 0, 5) CSM(3, 0, 6)
                 : "+v"(c0), "+v"(c1) : "v"(one), "v"(w[1]), "v"(w[2]), "v"(w[3]), "v"(w[4]), "v"(one), "s"(who) : "scc");
#undef CSM
}
DEV void mfma4_unless(f32x4* c, const u32x4* a, u32x4 b, int skip) {
  asm volatile("s_cmp_lg_u32 %9, 0\n\ts_cbranch_scc1 .Lm4_%=\n\tv_mfma_f32_16x16x32_bf16 %0, %4, %8, %0\n\tv_mfma_f32_16x16x32_bf16 %1, %5, %8, %1\n\t"
               "v_mfma_f32_16x16x32_bf16 %2, %6, %8, %2\n\tv_mfma_f32_16x16x32_bf16 %3, %7, %8, %3\n.Lm4_%=:"
               : "+v"(c[0]), "+v"(c[1]), "+v"(c[2]), "+v"(c[3]) : "v"(a[0]), "v"(a[1]), "v"(a[2]), "v"(a[3]), "v"(b), "s"(skip) : "scc");
}
template <int OFF> DEV void aload(u32x4& d, u32 voff, u32x4 srd, u32 soff) {
  asm volatile("buffer_load_dwordx4 %0, %1, %2, %3 offen offset:%4" : "=v"(d) : "v"(voff), "s"(srd), "s"(soff), "i"(OFF) : "memory");
}
template <int N> DEV void vm_on(u32x4& a) { asm volatile("s_waitcnt vmcnt(%1)" : "+v"(a) : "i"(N) : "memory"); }
DEV void aload_sc1(u32x4& d, u32 voff, u32x4 srd, u32 soff) {
  asm volatile("buffer_load_dwordx4 %0, %1, %2, %3 offen sc1" : "=v"(d) : "v"(voff), "s"(srd), "s"(soff) : "memory");
}
template <int B> DEV void load_if(u32x4* d, u32 voff, u32x4 srd, u32 soff, u32 mask) {
  u32 s2 = soff + 4096;
#define LIF(h, a, b, s, o) "s_bitcmp0_b32 %12, %13+" #h "\n\ts_cbranch_scc1 .Lli" #h "_%=\n\tbuffer_load_dwordx4 %" #a ", %8, %9, " s " offen offset:" #o " sc1\n\t" \
  "buffer_load_dwordx4 %" #b ", %8, %9, " s " offen offset:" #o "+1024 sc1\n.Lli" #h "_%=:\n\t"
  asm volatile(LIF(0, 0, 1, "%10", 0) LIF(1, 2, 3, "%10", 2048) LIF(2, 4, 5, "%11", 0) LIF(3, 6, 7, "%11", 2048)
               : "+v"(d[0]), "+v"(d[1]), "+v"(d[2]), "+v"(d[3]), "+v"(d[4]), "+v"(d[5]), "+v"(d[6]), "+v"(d[7])
               : "v"(voff), "s"(srd), "s"(soff), "s"(s2), "s"(mask), "i"(B) : "memory", "scc");
#undef LIF
}
template <int N> DEV void vm_on2(u32x4& a, u32x4& b) { asm volatile("s_waitcnt vmcnt(%2)" : "+v"(a), "+v"(b) : "i"(N) : "memory"); }

DEV f32x16 mfma32(u32x4 a, u32x4 b, f32x16 c) {
  return __builtin_amdgcn_mfma_f32_32x32x16_bf16(__builtin_bit_cast(bf16x8, a), __builtin_bit_cast(bf16x8, b), c, 0, 0, 0);
}
template <bool FIRST> DEV void mac16(f32x4& c, u32x4 a, u32x4 b) {
  if constexpr (FIRST) asm volatile("v_mfma_f32_16x16x32_bf16 %0, %1, %2, 0" : "=&v"(c) : "v"(a), "v"(b));
  else asm volatile("v_mfma_f32_16x16x32_bf16 %0, %1, %2, %0" : "+v"(c) : "v"(a), "v"(b));
}
DEV f32x16 smf_lo(u32x4 a, f32x16 p, f32x16 c, int idx) {
  f32x8 b = __builtin_shufflevector(p, p, 0, 1, 2, 3, 4, 5, 6, 7);
  return __builtin_amdgcn_smfmac_f32_32x32x32_bf16(__builtin_bit_cast(bf16x8, a), __builtin_bit_cast(bf16x16, b), c, idx, 0, 0);
}
DEV f32x16 smf_hi(u32x4 a, f32x16 p, f32x16 c, int idx) {
  f32x8 b = __builtin_shufflevector(p, p, 8, 9, 10, 11, 12, 13, 14, 15);
  return __builtin_amdgcn_smfmac_f32_32x32x32_bf16(__builtin_bit_cast(bf16x8, a), __builtin_bit_cast(bf16x16, b), c, idx, 0, 0);
}
typedef __bf16 bf16x2 __attribute__((ext_vector_type(2)));
DEV u32 cvtpk(float lo, float hi) {
  f32x2 v = {lo, hi};
  return __builtin_bit_cast(u32, __builtin_convertvector(v, bf16x2));
}
DEV float asf(u32 u) { return __builtin_bit_cast(float, u); }
DEV u32 asu(float f) { return __builtin_bit_cast(u32, f); }
DEV float mul1(float a, float b) { float r; asm("v_mul_f32 %0, %1, %2" : "=v"(r) : "v"(a), "v"(b)); return r; }
DEV float add1(float a, float b) { float r; asm("v_add_f32 %0, %1, %2" : "=v"(r) : "v"(a), "v"(b)); return r; }
DEV float fma1(float a, float b, float c) { float r; asm("v_fma_f32 %0, %1, %2, %3" : "=v"(r) : "v"(a), "v"(b), "v"(c)); return r; }
DEV void scale4(f32x4& a, float s) {
  float x = a[0], y = a[1], z = a[2], w = a[3];
  asm("v_mul_f32 %0, %0, %4\n\tv_mul_f32 %1, %1, %4\n\tv_mul_f32 %2, %2, %4\n\tv_mul_f32 %3, %3, %4" : "+v"(x), "+v"(y), "+v"(z), "+v"(w) : "v"(s));
  a = (f32x4){x, y, z, w};
}
DEV float sumsq4(f32x4 a) {
  float r, t;
  asm("v_mul_f32 %0, %2, %2\n\tv_mul_f32 %1, %3, %3\n\tv_fmac_f32 %0, %4, %4\n\tv_fmac_f32 %1, %5, %5\n\tv_add_f32 %0, %0, %1"
      : "=&v"(r), "=&v"(t) : "v"(a[0]), "v"(a[1]), "v"(a[2]), "v"(a[3]));
  return r;
}
DEV float bfround(float f) { return asf(cvtpk(f, f) << 16); }
DEV float ex2(float x) { return __builtin_amdgcn_exp2f(x); }
DEV float rcp(float x) { return __builtin_amdgcn_rcpf(x); }
DEV float other_half(float v) {
  auto r = __builtin_amdgcn_permlane32_swap(asu(v), asu(v), false, false);
  return asf(r[0] ^ r[1] ^ asu(v));
}
DEV u32x4 mk_srd(const void* p, u32 bytes) {
  u64 a = (u64)p;
  u32x4 r = {(u32)a, (u32)(a >> 32) & 0xffffu, bytes, 0x00020000u};
  return r;
}
template <int LD> DEV void dma_half(u32x4 srd, u32 voff, u32 so, u32 l0) {
#define EXH "0"
  asm volatile("s_add_u32 m0, %0, %1\n\ts_mov_b32 exec_hi, " EXH "\n\tbuffer_load_dwordx4 %2, %3, %4 offen lds\n\ts_mov_b32 exec_hi, -1"
               ::"s"(l0), "i"(LD), "v"(voff), "s"(srd), "s"(so) : "memory", "scc");
}
template <int LD, int OFF> DEV void dma1(u32x4 srd, u32 voff, u32 so, u32 l0) {
  asm volatile("s_add_u32 m0, %0, %1\n\ts_nop 0\n\tbuffer_load_dwordx4 %2, %3, %4 offen offset:%5 lds"
               ::"s"(l0), "i"(LD), "v"(voff), "s"(srd), "s"(so), "i"(OFF) : "memory", "scc");
}
template <int LD, int OFF> DEV void dma2(u32x4 s0, u32 v0, u32x4 s1, u32 v1, u32 so, u32 l0) {
  asm volatile("s_add_u32 m0, %0, %1\n\ts_nop 0\n\tbuffer_load_dwordx4 %2, %3, %6 offen offset:%7 lds\n\tbuffer_load_dwordx4 %4, %5, %6 offen offset:%8 lds"
               ::"s"(l0), "i"(LD), "v"(v0), "s"(s0), "v"(v1), "s"(s1), "s"(so), "i"(OFF), "i"(OFF + 1024) : "memory", "scc");
}
DEV void dma_sc1(u32x4 srd, u32 voff, u32 so, u32 l0) {
  asm volatile("s_mov_b32 m0, %0\n\ts_nop 0\n\tbuffer_load_dwordx4 %1, %2, %3 offen sc1 lds" ::"s"(l0), "v"(voff), "s"(srd), "s"(so) : "memory");
}
DEV u32 opaque(u32 x) { asm volatile("" : "+s"(x)); return x; }
template <int LD, int ADD> DEV void dma_i(u32x4 srd, u32 voff, u32 s0, u32 l0) {
  u32 t;
  asm volatile("s_add_u32 m0, %1, %2\n\ts_add_u32 %0, %3, %4\n\tbuffer_load_dwordx4 %5, %6, %0 offen lds"
               : "=&s"(t) : "s"(l0), "i"(LD), "s"(s0), "i"(ADD), "v"(voff), "s"(srd) : "memory", "scc");
}
DEV int lane_id() {
  u32 z = 0;
  asm volatile("" : "+s"(z));
  return __builtin_amdgcn_mbcnt_hi(~0u, __builtin_amdgcn_mbcnt_lo(~0u, z));
}
#define LANES() const int lane = lane_id(), r32 = lane & 31, hf = lane >> 5, blk = (lane >> 4) & 1, q4 = (lane & 15) >> 2, p4 = lane & 3; \
  const u32 vl = lane * 16; (void)r32; (void)hf; (void)blk; (void)q4; (void)p4; (void)vl
DEV void touch(const void* p) {
  asm volatile("s_mov_b32 m0, %0\n\ts_nop 0\n\tglobal_load_lds_dword %1, off" ::"i"(L_DUM), "v"(p) : "memory");
}
DEV void touch_b(u32x4 srd, u32 voff, u32 so) {
  asm volatile("s_mov_b32 m0, %0\n\ts_nop 0\n\tbuffer_load_dword %1, %2, %3 offen lds" ::"i"(L_DUM), "v"(voff), "s"(srd), "s"(so) : "memory");
}
template <int N> DEV void vmwait() { asm volatile("s_waitcnt vmcnt(%0)" ::"i"(N) : "memory"); }
DEV void cfence() { asm volatile("" ::: "memory"); }
DEV void cnt_add(int i) {
  asm volatile("s_mov_b64 exec, 1\n\tds_inc_u32 %0, %0\n\ts_mov_b64 exec, -1" ::"v"(L_CNT + 4 * i) : "memory");
}
DEV u32 cnt_take(int i) {
  u32 r;
  asm volatile("s_mov_b64 exec, 1\n\tds_inc_rtn_u32 %0, %1, %1\n\ts_mov_b64 exec, -1" : "=v"(r) : "v"(L_CNT + 4 * i) : "memory");
  return r;
}
DEV u32 cnt_get(int i) { return __hip_atomic_load((LAS u32*)(L_CNT + 4 * i), __ATOMIC_RELAXED, __HIP_MEMORY_SCOPE_WORKGROUP); }
DEV void cnt_wait(int i, u32 n) {
  while (__builtin_amdgcn_readfirstlane(cnt_get(i)) < n) __builtin_amdgcn_s_sleep(1);
  cfence();
}
DEV u32x2 trd(u32 a) { return __builtin_bit_cast(u32x2, __builtin_amdgcn_ds_read_tr16_b64_v4i16((LAS s16x4*)a)); }
DEV u32x4 trd2(u32 a0, u32 a1) { u32x2 x = trd(a0), y = trd(a1); u32x4 r = {x[0], x[1], y[0], y[1]}; return r; }
DEV u32x4 lds128(u32 a) { return *(LAS u32x4*)a; }
DEV f32x4 lds128f(u32 a) { return *(LAS f32x4*)a; }
template <int N, class Fn> DEV void read_when(f32x4* d, int i, u32 n, int i2, u32 n2, Fn&& at) {
  for (;;) {
    const u32 c = cnt_get(i), c2 = i2 != i ? cnt_get(i2) : n2;
    cfence();
    sfor<0, N>([&](auto k) LAM { d[k.value] = lds128f(at(k.value)); });
    sfor<0, N>([&](auto k) LAM { PIN(d[k.value]); });
    if (__builtin_amdgcn_readfirstlane(c) >= n && __builtin_amdgcn_readfirstlane(c2) >= n2) break;
    if (RWS) __builtin_amdgcn_s_sleep(RWS);
  }
}
template <int N, int L = 0> DEV void sync_stage() { asm volatile("s_waitcnt vmcnt(%0) lgkmcnt(%1)\n\ts_barrier" ::"i"(N), "i"(L) : "memory"); }
template <int I> DEV void wlane(u32& f, u32 v) { asm volatile("v_writelane_b32 %0, %1, %2" : "+v"(f) : "s"(v), "i"(I)); }
typedef __amdgpu_buffer_rsrc_t rsrc_t;
DEV void st128(rsrc_t r, u32x4 v, u32 voff, u32 soff) {
  __builtin_amdgcn_raw_buffer_store_b128(v, r, voff, __builtin_amdgcn_readfirstlane(soff), 16);
  asm volatile("s_nop 1" ::"v"(v) : "memory");
}
DEV void st128p(rsrc_t r, u32x4 v, u32 voff) {
  __builtin_amdgcn_raw_buffer_store_b128(v, r, voff, 0, 16);
}
DEV void aflag(u64& d, const char* p) { asm volatile("global_load_dwordx2 %0, %1, off sc1" : "=v"(d) : "v"(p) : "memory"); }
template <int N> DEV void vm_on1(u64& a) { asm volatile("s_waitcnt vmcnt(%1)" : "+v"(a) : "i"(N) : "memory"); }
DEV void swap32(u32& a, u32& b) {
  auto r = __builtin_amdgcn_permlane32_swap(a, b, false, false);
  a = r[0]; b = r[1];
}
DEV void swap16(u32& a, u32& b) {
  auto r = __builtin_amdgcn_permlane16_swap(a, b, false, false);
  a = r[0]; b = r[1];
}
DEV u32 from_lane(int l, u32 v) { return __builtin_amdgcn_ds_bpermute(l * 4, v); }
DEV u32x4 ld128(rsrc_t r, u32 voff, u32 soff) { return __builtin_amdgcn_raw_buffer_load_b128(r, voff, __builtin_amdgcn_readfirstlane(soff), 16); }
DEV void put_flag(char* ws, u32 off, u64 tag) { __hip_atomic_store((GAS u64*)(ws + off), tag, __ATOMIC_RELAXED, __HIP_MEMORY_SCOPE_AGENT); }
template <int NAP> DEV void poll_flags(char* ws, u32 off, u64 tag) {
#pragma nounroll
  for (u32 spins = 0; spins < (1u << 20); ++spins) {
    u64 v = __hip_atomic_load((GAS u64*)(ws + off), __ATOMIC_RELAXED, __HIP_MEMORY_SCOPE_AGENT);
    if (__builtin_amdgcn_ballot_w64(v != tag) == 0) break;
    __builtin_amdgcn_s_sleep(NAP);
  }
  __builtin_amdgcn_fence(__ATOMIC_ACQUIRE, "wavefront");
  asm volatile("" ::: "memory");
}
DEV void row_max_to(u32 a, u32 x) {
#define DPPMAX(ctrl) { u32 y = __builtin_amdgcn_update_dpp(0u, x, ctrl, 0xf, 0xf, true); x = x > y ? x : y; }
  DPPMAX(0x111) DPPMAX(0x112) DPPMAX(0x114) DPPMAX(0x118)
  asm volatile("s_mov_b32 exec_lo, 0x80008000\n\ts_mov_b32 exec_hi, 0x80008000\n\tds_max_u32 %0, %1\n\ts_mov_b64 exec, -1" ::"v"(a), "v"(x) : "memory");
}
DEV float max8_nonneg(float v) {
  u32 x = asu(v);
  DPPMAX(0x111) DPPMAX(0x112) DPPMAX(0x114)
#undef DPPMAX
  return asf(__builtin_amdgcn_readlane(x, 7));
}

DEV void widen(float& lo, float& hi, u32 x) {
  asm volatile("v_lshlrev_b32 %0, 16, %2\n\tv_and_b32 %1, 0xffff0000, %2" : "=&v"(lo), "=v"(hi) : "v"(x));
}
DEV float half_sum(float v) {
#define DPPADD(ctrl) v += asf(__builtin_amdgcn_update_dpp(0u, asu(v), ctrl, 0xf, 0xf, true));
  DPPADD(0x111) DPPADD(0x112) DPPADD(0x114) DPPADD(0x118) DPPADD(0x142)
  return v;
}
DEV float row_sum(float v) {
  DPPADD(0x128) DPPADD(0x124) DPPADD(0x122) DPPADD(0x121)
#undef DPPADD
  return v;
}
DEV u64 stale(u32x4 f, u64 tag) { return __builtin_amdgcn_ballot_w64(f[1] != (u32)tag) | __builtin_amdgcn_ballot_w64(f[3] != (u32)(tag >> 32)); }
DEV bool fresh(u32x4 f, u64 tag) { return stale(f, tag) == 0; }
template <int N = 1> DEV void await(u32x4* f, u32 voff, u32x4 srd, u32 soff, u64 tag) {
  auto old = [&]() LAM -> bool { u64 o = 0; sfor<0, N>([&](auto i) LAM { o |= stale(f[i.value], tag); }); return o != 0; };
  if (__builtin_expect(old(), 0)) {
    u32 spins = 0;
#pragma nounroll
    do {
      __builtin_amdgcn_s_sleep(1);
      sfor<0, N>([&](auto i) LAM { aload_sc1(f[i.value], voff, srd, soff + i.value * 1024); });
      sfor<0, N>([&](auto i) LAM { vm_on<0>(f[i.value]); });
    } while (old() && ++spins < (1u << 20));
  }
}

#define STAMP(i) do {} while (0)
#define STAMPF(i) do {} while (0)
#define STAMP_END() do {} while (0)

#define STAMPG(i) do {} while (0)
#define ROT1 2
#define PAT 0
#define DPH 10
#define PRIO 1
#define NR 4
#define NR1 1
#define C3E 0
#define PROG 0
#define DW 14
#define PGA 0
#define GA 4
#define PI 1
#define WL 0
#define OWN 0
#define PK0 1
#define CPK 0
#define P9 1
#define C3T -1
#define NAPB 4
#define WE 0
#define HP 4
#define PRB 0
#define HCNT 4
#define UPF 2
#define WS 1
#define WT 2
#define NAPA 1
#define NAPO 2
#define G0 1
#define WA 4
#define TJ1 1
#define TW 1
#define TJ2 5
constexpr int TWM = (TW & 1 ? 4 << TJ1 : 0) | (TW & 2 ? 20 << TJ2 : 0);
#define KD 18
#define ID 60

constexpr int piece_at(int T, int j, int i) {
  constexpr int at[2][2][7] = {{{2, 5, 8, 11, 14, 16, 18}, {6, 11, 16, 19, 5, 14, 18}}, {{1, 3, -1, 5, 7, 9, 11}, {3, 7, -1, 11, 2, 6, 10}}};
  const bool all = j == 0 || (T == 0 && j == 7);
  if (T == 0 && j == 8) return -1;
  if (j == 12) return i >= 6 && i < 14 && i % 2 == 0 ? i / 2 - 3 : -1;
  if (j == 11) return -1;
  for (int p = all || (j & 1) ? 0 : 4; p < (!all && (j & 1) ? 4 : 7); ++p) if (at[T][!all][p] == i) return p;
  return -1;
}
constexpr int slots(int T, int j) { return T == 1 || j > 10 ? 16 : j == 10 ? 36 : 20; }
constexpr int slot_tile(int T, int j, int i) { return T == 1 || j > 10 ? i / 4 + 1 : j < 10 || i < 12 ? i / 4 : i < 28 ? 0 : i / 4 - 4; }
constexpr int slot_step(int T, int j, int i) { return T == 1 || j != 10 || i < 12 || i >= 28 ? j : i < 16 ? 11 : i < 20 ? -1 : 7 + i / 4; }
constexpr int slot_read(int T, int j, int i) {
  const int r = i - 2 * (j & 1);
  if (T == 1) return j < 13 && r >= 0 && r < 12 ? 16 * (j + 1) + (r < 2 ? r + 6 : r < 6 ? r : r + 2) : -1;
  if (j < 10) return r >= 0 && r < 14 ? 16 * (j + 1) + r : -1;
  if (j == 10) return i < 8 ? 16 * 11 + i : i < 20 ? 16 * (12 + (i - 8) / 6) + (i - 8) % 6 : i < 26 ? 16 * 11 + i - 12 : -1;
  return j < 13 && i < 8 ? 16 * (j + 1) + i + 6 : -1;
}
constexpr int wd_blk(int b) { return (int)(0x61087610875432ull >> (4 * b)) & 15; }
constexpr u32 wd_loc(int k) { return wd_blk(k / 2) * 16384 + (k & 1) * 1024; }

constexpr int wd_at(int b) { return 2 * b - 9; }
constexpr int gemm_wait(int k) {
  int n = 0, last = 0;
  const int end = k == KD ? KD : k - 1;
  for (int i = -DPH; i < end; ++i) {
    if (i + DPH < 28) { n += 2; if (i + DPH == k) last = n; }
    for (int b = 9; b < 14; ++b) if (wd_at(b) == i) { n += 4; if (k / 2 == b) last = n; }
  }
  return n - last < 63 ? n - last : 63;
}
typedef u32 u32x16 __attribute__((ext_vector_type(16)));
DEV u32x4 mk_srd2(u32 lo, u32 hi, u32 bytes) { return (u32x4){lo, hi & 0xffffu, bytes, 0x00020000u}; }

__global__ __launch_bounds__(512, 2) void fused_block(const void*, const void*, const void*, const void*, const void*, const void*,
                                                           void*, char*) {
  __shared__ __attribute__((aligned(16))) char smem[LDS_BYTES];
  u32x16 ka;
  asm volatile("s_load_dwordx16 %0, %1, 0x0" : "=s"(ka) : "s"(__builtin_amdgcn_kernarg_segment_ptr()));
  SBAR();
  const int tid = __builtin_amdgcn_workitem_id_x();
  const int w = __builtin_amdgcn_readfirstlane(tid >> 6);
  const int bid = __builtin_amdgcn_workgroup_id_x();
  const int xcd = bid & 7, bi = bid >> 3;
  u64 pc0;
  u32 clen;
  asm volatile("s_getpc_b64 %0\n.Lfb_pc0:\n\ts_max_u32 %1, .Lfb_enda-.Lfb_pc0, 0\n\ts_max_u32 %1, .Lfb_endb-.Lfb_pc0, %1" : "=s"(pc0), "=s"(clen)::"scc");
  {
    const u32 o = (bi * 16 + w * 2 + (tid & 1)) * 128;
    touch((const char*)pc0 + (o < clen ? o : clen));
  }
  SBAR();
  const int rb = 4 * (xcd & 1) + (bi & 3), quad = xcd >> 1, cl = bi >> 2, cb = 8 * quad + cl;
  const bool t1 = cl == 7;
  const int mg = w & 3, g = w >> 2, un = 7 * quad + cl;
  const u32 rot = 2 * (bi & 3);
  const int rr = (tid >> 3) & 7, ch = tid & 7, odd = (tid >> 5) & 2;
  u32 vx0 = rr * 896 + ((ch ^ (rr >> 1)) << 4), vx1 = (vx0 ^ 64) + 6144;
  u32 ox = (rb * 8 + w) * (32 * 896), lw = w * 1024, so0 = ox + rot * 128;
  asm volatile("s_waitcnt lgkmcnt(0)" : "+s"(ka), "+v"(vx0), "+v"(vx1), "+s"(so0), "+s"(lw));
  STAMPF(3);
  const u32x4 sX = mk_srd2(ka[0], ka[1], S_ * D_ * 2);
  dma2<0, 0>(sX, vx0, sX, vx1, so0, 4 * lw);
  dma2<0, 2048>(sX, vx0, sX, vx1, so0 + 12288, 4 * lw);
  SBAR();
  const u32 wst = t1 ? 2688 : 3584, cw = ch ^ ((rr & 2) << 1);
  const u32 cv = (tid & 15) ^ ((tid >> 2) & 12) ^ odd;
  const u32 va = t1 ? (rr >> 1) * 2688 + (cv >= 14 ? cv - 2 : cv) * 16 : rr * 3584 + (cw ^ odd) * 16;
  const u32 ow = 8 * w * wst + (t1 ? 1792 + 224 * quad : un * 128);
  const u32 vq = ((tid >> 2) & 7) * 2688 + ((tid & 3) ^ odd) * 16;
  const u32 oq = w * (8 * 2688) + 64 * un;
  const u64 pb_ = (t1 ? ((u64)ka[3] << 32 | ka[2]) + 4 * 2688 : ((u64)ka[9] << 32 | ka[8])) - 1024;
  const u32x4 sA = mk_srd2(t1 ? ka[2] : ka[6], t1 ? ka[3] : ka[7], 1u << 21), sB = mk_srd2((u32)pb_, (u32)(pb_ >> 32), 1u << 21);
  const u32x4 sQ = mk_srd2(ka[2], ka[3], D_ * 3 * D_ * 2);
  u32 kst = rot, so = 0;
  auto piece = [&](auto H_, auto I_) LAM {
    constexpr int H = H_.value, i = I_.value;
    if constexpr (i == 0) {
      if constexpr (H > 0) { kst = opaque(kst) + 1; kst = kst == 7 ? 0 : kst; }
      so = ow + kst * (64 * wst);
    }
    if constexpr (i < 2) dma1<(6 + H % 3) * 16384, i * 1024>(i ? sB : sA, va, so, 2 * lw);
    if constexpr (i == 2) dma_half<L_QK + (H % 3) * 4096>(sQ, vq, oq + opaque(kst) * (64 * 2688), lw >> 1);
    if constexpr (i == 3) so = ox + opaque(kst) * 128;
    if constexpr (i == 5) so = opaque(so) + 12288;
    if constexpr (i >= 3) dma1<(H % 3) * 32768, (i - 3) * 1024>(sX, (i & 1) ? vx0 : vx1, so, 4 * lw);
  };
  sfor<0, 3>([&](auto i) LAM { piece(IC<0>{}, i); });
  asm volatile("s_barrier" ::: "memory");
  STAMPF(4);
  u32x4 xr[2];
  const u32 row = rb * 256 + cb * 8 + 2 * mg;
  {
    const u32 vr = (tid & 31) < 28 * g ? (tid & 31) * 16 + (tid & 32) * 28 : OOB;
    aload<0>(xr[0], vr, sX, row * 896);
    aload<448>(xr[1], vr, sX, row * 896);
  }
  char* const ws = (char*)((u64)ka[15] << 32 | ka[14]);
  u64 epoch;
  asm volatile("s_load_dwordx2 %0, %1, 0x0" : "=s"(epoch) : "s"(ws));
  asm volatile("" ::"v"((u32)(uintptr_t)(LAS char*)smem));
  const char* const px = (const char*)((u64)ka[1] << 32 | ka[0]);
  const char* const pwo = (const char*)((u64)ka[5] << 32 | ka[4]);
  char* const pout = (char*)((u64)ka[13] << 32 | ka[12]);
  const u32x4 sD = mk_srd2(ka[10], ka[11], F_ * D_ * 2), sWS = mk_srd2(ka[14], ka[15], WS_END);
  const rsrc_t rWS = __builtin_amdgcn_make_buffer_rsrc(ws, 0, WS_END, 0x00020000);
  if (tid < 32) *(LAS u32*)(L_CNT + tid * 4) = 0;
  const bool has_tile = !t1;
  const int rq = bi / 7, rt = 4 * xcd + rq, nt = bi - 7 * rq;
  const int rotg = (4 * nt + 8 * rq) % 28;
  const int head = 2 * xcd + (bi >> 4), qb = bi & 15;
  const u32 vw0 = rr * 896 + cw * 16, vw1 = vw0 + 6144;
  auto krot = [&](u32 k) LAM -> u32 { k += opaque(rotg); return k >= 28 ? k - 28 : k; };
  const u32 od = 448 * mg * 896 + nt * 128;

  asm volatile("s_waitcnt lgkmcnt(0)" : "+s"(epoch));
  u64 tag = (epoch + 0x9E3779B97F4A7C15ull) * 0xBF58476D1CE4E5B9ull;
  tag = (tag ^ (tag >> 29)) + llvm_dispatch_id() * 0x94D049BB133111EBull;
  tag = (tag ^ (tag >> 31)) * 0xD6E8FEB86659FD93ull & 0xBFFFBFFFBFFFBFFFull;
  asm volatile("" : "+s"(tag));
  float rs[4];
  f32x4 acc[5][4];
  auto geglu = [&](auto M) LAM -> u32x4 {
    constexpr int m = M.value;
    const float r2 = mul1(rs[m], rs[m]), K1 = (-2.f * 0.7978845608028654f * 1.4426950408889634f) * rs[m], K2 = K1 * 0.044715f * r2, IR2 = rcp(r2);
    u32x4 hp;
    f32x2 y[4], z[4];
    auto all = [&](auto f) LAM {
      sfor<0, 4>([&](auto i) LAM {
        const f32x4 t = acc[1 + (i.value & 2)][m], u = acc[2 + (i.value & 2)][m];
        constexpr int e = 2 * (i.value & 1);
        f(y[i.value], z[i.value], (f32x2){t[e], t[e + 1]}, (f32x2){u[e], u[e + 1]});
      });
      SBAR();
    };
    auto mul = [&](f32x2 a, f32x2 b) LAM -> f32x2 { if constexpr (m < PK0) return (f32x2){mul1(a[0], b[0]), mul1(a[1], b[1])}; else return a * b; };
    auto fma = [&](f32x2 a, float b, float c) LAM -> f32x2 {
      if constexpr (m < PK0) return (f32x2){fma1(a[0], b, c), fma1(a[1], b, c)}; else return __builtin_elementwise_fma(a, (f32x2){b, b}, (f32x2){c, c});
    };
    SBAR();
    all([&](f32x2& y, f32x2&, f32x2 t, f32x2) LAM { y = mul(t, t); });
    all([&](f32x2& y, f32x2& z, f32x2 t, f32x2 u) LAM { y = fma(y, K2, K1); z = mul(t, u); });
    all([&](f32x2& y, f32x2&, f32x2 t, f32x2) LAM { y = mul(y, t); });
    all([&](f32x2& y, f32x2&, f32x2, f32x2) LAM { y = (f32x2){ex2(y[0]), ex2(y[1])}; });
    all([&](f32x2& y, f32x2&, f32x2, f32x2) LAM { y = fma(y, IR2, IR2); });
    all([&](f32x2& y, f32x2&, f32x2, f32x2) LAM { y = (f32x2){rcp(y[0]), rcp(y[1])}; });
    all([&](f32x2& y, f32x2& z, f32x2, f32x2) LAM { y = mul(y, z); });
    sfor<0, 4>([&](auto j) LAM { hp[j.value] = cvtpk(y[j.value][0], y[j.value][1]); });
    PIN(hp);
    return hp;
  };
  auto frag_off = [&]() LAM -> u32 { const u32 l = lane_id(); return (l >> 4) * 512 + (l & 15) * 16; };
  u32x4 hp[4];

  auto phase1 = [&](auto T_) LAM {
    constexpr int T = T_.value;
    u32 xa[2][2], fa[4], qa, vl_, vcs, bt, vq1, vq2, vv[4];
    const bool isq = quad < 2;
    const u32 ts = isq ? 2048 : 4096;
    {
      const int lane = lane_id(), g4 = lane >> 4, tok = lane & 15, q4 = tok >> 2, p4 = lane & 3;
      xa[0][0] = (mg * 64 + tok) * 128 + ((g4 ^ (tok >> 1)) << 4);
      xa[0][1] = xa[0][0] ^ 64;
      xa[1][0] = xa[0][0] + 65536;
      xa[1][1] = xa[0][1] + 65536;
      sfor<0, 4>([&](auto t) LAM {
        if constexpr (T == 0) fa[t.value] = 6 * 16384 + g4 * 2048 + q4 * 128 + ((64 * g + 16 * (t.value & 2)) ^ ((q4 & 2) << 5) ^ ((g4 & 1) << 5)) + 8 * p4;
        else fa[t.value] = 6 * 16384 + g4 * 2048 + q4 * 256 + ((128 * g + 32 * t.value + 8 * p4) ^ (q4 * 64) ^ ((g4 & 1) << 5));
      });
      qa = L_QK + g4 * 512 + q4 * 64 + ((32 * g + 8 * p4) ^ ((g4 & 1) << 5));
      PIN(xa[0][0]); PIN(xa[0][1]); PIN(xa[1][0]); PIN(xa[1][1]); PIN(fa[0]); PIN(fa[2]);
      if constexpr (T == 1) { PIN(fa[1]); PIN(fa[3]); }
      vl_ = lane * 16;
      bt = tok * 4;
      vcs = mg * 1024 + g4 * 4096 + tok * 16;
      vcs = lane < (T == 0 && mg == 0 ? 32 : 16) ? vcs : OOB;
      PIN(vl_); PIN(bt); PIN(vcs);
      if constexpr (T == 0) {
        const u32 G = 4 * cl + 2 * g + (lane >> 5), p = (G * 37) >> 8, r = G - 7 * p;
        vq2 = (isq ? vl_ & 0x1f0u : (vl_ & 0x30u) | ((vl_ & 0xc0u) << 1) | ((vl_ & 0x100u) >> 2)) + p * (128 * ts);
        vq1 = vq2 + (r >> 2) * (64 * ts) + (r & 3) * 512;
        vq2 = r == 3 ? vq1 + 64 * ts : OOB;
        PIN(qa); PIN(vq1); PIN(vq2);
      }
      if constexpr (T == 1) sfor<0, 4>([&](auto t) LAM {
        const u32 c = 64 * g + 16 * t.value + tok, hd = (c * 147) >> 12;
        vv[t.value] = hd * (64 * 4096 - 28 * 16) + c * 16 + g4 * 512;
        PIN(vv[t.value]);
        acc[4][t.value] = (f32x4){};
        PIN(acc[4][t.value]);
      });
    }
    u32x4 one = {0x3f803f80u, 0x3f803f80u, 0x3f803f80u, 0x3f803f80u};
    PIN(one);
    u32x4 xf[4][4], wf[4][5], sg, cf[5], nm[4];
    f32x4 c0 = {}, c1 = {};
    PIN(c0); PIN(c1);
    const u32 scs = WS_CS + (cb * 2 + g) * 5120, sst = WS_ST + (rb * 256 + mg * 64) * 16;
    f32x4 rv[4];
    float rl, sc[4];
    u32 nl;

    SBAR();
    sfor<0, 7>([&](auto i) LAM { if constexpr (T == 0 || i.value != 2) piece(IC<1>{}, i); });
    sync_stage<7 - T>();
    STAMP(1);
    auto rd = [&](auto J, auto R) LAM {
      constexpr int j = J.value, r = R.value, sl = (j / 2) % 3, s = j & 1;
      if constexpr (r >= 2 && r < 6) xf[j & 3][r - 2] = lds128(xa[sl >> 1][s] + (sl & 1) * 32768 + (r - 2) * 2048);
      else {
        constexpr int t = r < 2 ? 0 : (r - 4) / 2, h = r & 1;
        u32x2 v;
        if constexpr (t == 0) v = trd(qa + sl * 4096 + s * 2048 + h * 256);
        else if constexpr (T == 0) v = trd(fa[(t - 1) & 2] + sl * 16384 + s * 8192 + ((t - 1) & 1) * 1024 + h * 512);
        else v = trd(fa[t - 1] + sl * 16384 + s * 8192 + h * 1024);
        wf[j & 3][t][2 * h] = v[0];
        wf[j & 3][t][2 * h + 1] = v[1];
      }
    };
    sfor<2 * T, 14>([&](auto r) LAM { rd(IC<0>{}, r); });
    SBAR();
    {
      u32 d = 0, vs = OOB;
      if (g != 0) {
        float a[16], t = 0.f, q = 0.f;
        sfor<0, 8>([&](auto i) LAM {
          widen(a[2 * i.value], a[2 * i.value + 1], xr[i.value >> 2][i.value & 3]);
          t += add1(a[2 * i.value], a[2 * i.value + 1]);
        });
        t = half_sum(t) * (-1.f / 448.f);
        const float m0 = asf(__builtin_amdgcn_readlane(asu(t), 31)), m1 = asf(__builtin_amdgcn_readlane(asu(t), 63)), nmean = vl_ < 512 ? m0 : m1;
        sfor<0, 16>([&](auto i) LAM { const float e = add1(a[i.value], nmean); q = __builtin_fmaf(e, e, q); });
        const float var = half_sum((vl_ & 511) < 448 ? q : 0.f) * (1.f / 448.f);
        d = cvtpk(nmean, __builtin_amdgcn_sqrtf(bfround(bfround(var) + 1e-5f)));
        vs = (vl_ & 511) == 496 ? vl_ >> 5 & 16 : OOB;
      }
      st128(rWS, (u32x4){d, (u32)tag, d, (u32)(tag >> 32)}, vs, WS_ST + row * 16);
    }
    SBAR();
    u32 P[4], nx, sq, last, tsrc, tdst;
    auto qk_put = [&](auto M) LAM {
      constexpr int m = M.value;
      f32x4& a = acc[0][m];
      scale4(a, sc[m]);
      P[2 * (m & 1)] = cvtpk(a[0], a[1]);
      P[2 * (m & 1) + 1] = cvtpk(a[2], a[3]);
      if constexpr (m & 1) {
        swap16(P[0], P[2]);
        swap16(P[1], P[3]);
        st128(rWS, (u32x4){P[0], P[1], vq2 == OOB ? P[2] : 0u, vq2 == OOB ? P[3] : 0u}, vq1, sq + (m >> 1) * ts);
        st128(rWS, (u32x4){P[2], P[3], 0u, 0u}, vq2, sq + (m >> 1) * ts);
      } else { PIN(P[0]); PIN(P[1]); }
    };
    auto norm = [&](auto I) LAM {
      constexpr int i = I.value;
      if constexpr (i < 4) {
        const f32x4 a = acc[0][i];
        const u32 v = asu(sumsq4(a));
        nx = i == 0 || v > nx ? v : nx;
        PIN(nx);
      } else row_max_to(L_CNT + 64 + 16 * g + (vl_ >> 6 & 12), nx);
    };
    auto tell = [&]() LAM {
      lgkm0_on(last);
      last = __builtin_amdgcn_readfirstlane(last);
      if (last == 7) {
        if constexpr (T == 0) {
          const u32 k2 = asu(row_sum(*(LAS float*)tsrc));
          st128(rWS, (u32x4){k2, (u32)tag, k2, (u32)(tag >> 32)}, tdst, WS_FQ + ((quad & 1) * 8 + cl) * (16 * BOX) + (isq ? 2 * rb * BOX + 24 * 16 : rb * 16));
        } else st128(rWS, (u32x4){0u, (u32)tag, 0u, (u32)(tag >> 32)}, vl_ * (BOX / 16), WS_FQ + quad * (64 * BOX) + (8 + rb) * 16);
      }
      STAMP(13);
    };
    auto v_put = [&](auto I) LAM {
      constexpr int t = 1 + I.value / 2, u = I.value & 1;
      u32x4 V;
      sfor<0, 4>([&](auto e) LAM {
        constexpr int m = 2 * u + e.value / 2, c = 2 * (e.value & 1);
        V[e.value] = cvtpk(mul1(acc[t][m][c], rv[m][c]), mul1(acc[t][m][c + 1], rv[m][c + 1]));
      });
      st128(rWS, V, vv[t - 1], WS_KV + (quad * 256 + rb * 8 + mg * 2 + u) * 4096 + 2048);
    };
    sfor<0, 14>([&](auto J) LAM {
      constexpr int j = J.value, G = j / 2;
      sfor<0, slots(T, j)>([&](auto I) LAM {
        constexpr int i = I.value, t = slot_tile(T, j, i), js = slot_step(T, j, i), m = i % 4;
        if constexpr (T == 1 && t == 4) {
          if constexpr (m == 0) mfma4_unless(acc[4], xf[j & 3], wf[j & 3][4], g);
        } else if constexpr (T == 1) mac16<j == 0>(acc[t][m], xf[j & 3][m], wf[j & 3][t]);
        else if constexpr (js < 0) mac16<false>(acc[0][m], cf[0], nm[m]);
        else mac16<j == 0>(acc[t][m], wf[js & 3][t], xf[js & 3][m]);
        SBAR();
        if constexpr ((j & 1) && i == 1 && j < 11 + 2 * T) sync_stage<(T ? (G == 5 ? 1 : 7 - (G & 1)) : G == 1 ? 7 : 8) + (TWM >> j & 1)>();
        if constexpr (T == 0 && j == 9 && i == 2) STAMPG(5);
        if constexpr (T == 0 && j == 10 && i == 8) { STAMPG(6); sync_stage<4, 2>(); __builtin_amdgcn_s_setprio(3); STAMPG(7); }
        if constexpr (T == 0 && j == 10 && i == 28) STAMPG(8);
        if constexpr (T == 0 && j == 11 && i == 0) STAMPG(9);
        if constexpr (T == 0 && j == 13 && i == 0) STAMPG(13);
        if constexpr ((j & 1) && i == 1 && j < 13) STAMPF(5 + G);
        if constexpr (i == 1 && (j == TJ2 || j == TJ2 + 2) && (TW & 2)) touch_b(sD, vl_ < 25 * 16 ? vl_ * 8 : OOB, ((bi * 8 + w) * 2 + (j - TJ2) / 2) * (25 * 128));
        if constexpr (i == 1 && j == TJ1 && (TW & 1)) touch_b(sD, vl_ < 7 * 16 ? vl_ * 8 : OOB, (bid * 8 + w) * (7 * 128));
        constexpr int r = slot_read(T, j, i);
        if constexpr (r >= 0) rd(IC<(r >= 0 ? r / 16 : 0)>{}, IC<(r >= 0 ? r % 16 : 0)>{});
        if constexpr (j < 4 && i == 3) colsum<T>(c0, c1, one, wf[j & 3], (0xf0700e0fu >> (8 * j + rb)) & 1 ? mg : 4);
        if constexpr (j == 3 && i == 8) {
          PIN(c0); PIN(c1);
          const float a0 = c0[0], a1 = c1[0], v = vl_ < 256 ? a0 : a1, hi = bfround(v);
          __builtin_amdgcn_raw_buffer_store_b64((u32x2){cvtpk(hi, v - hi), (u32)(tag >> (32 * (rb & 1)))}, rWS, vcs, scs + (rb >> 1) * 256 + (rb & 1) * 8, 16);
        }
        constexpr int pc = piece_at(T, j, i), H = (j + 5) / 2;
        if constexpr (pc >= 0 && H < 7) piece(IC<H>{}, IC<(pc >= 0 ? pc : 0)>{});
        if constexpr (T == 0 && (j == 9 || j == 12) && pc >= 0) {
          if constexpr (pc == 0) so = od + krot((j & 4) + 2 * g) * 14336;
          if constexpr (pc == 2) so = opaque(so) + 12288;
          dma1<(j == 9 ? 2 : 4) * 16384, pc * 1024>(sD, (pc & 1) ? vw1 : vw0, so, 4 * lw);
        }
        if constexpr (T == 0 && j == 8 && i == 4) {
          const u32 l = bt >> 2, p = vl_ >> 8;
          tsrc = l < 8 && p == (l + cl >= 7) ? L_CNT + 64 + bt : L_CNT + 124;
          PIN(tsrc);
        }
        if constexpr (T == 0 && j == 8 && i == 6) {
          const u32 p = vl_ >> 8, sd = isq ? 16 : 256;
          tdst = bt * (BOX / 4) + (p == 0 ? 0u : p == 1 ? 16 * BOX + sd : cl == 0 ? sd : 16 * BOX);
          tdst = bt < (isq ? 8 : 64) && p < (cl == 0 || cl == 6 ? 3 : 2) ? tdst : OOB;
          PIN(tdst);
        }
        constexpr int jn = 9 + 2 * T;
        if constexpr (j == jn - 2 && i == 0) aload_sc1(sg, vl_, sWS, sst);
        if constexpr (T == 0 && j == 8 && i == 0) aload_sc1(cf[0], vl_, sWS, scs + 4096);
        if constexpr (j == 9 && i == 1) dma_sc1(sWS, vl_, scs + mg * 1024, 7 * 16384 + lw);
        if constexpr (T == 1 && j == 9 && i == 8) st128(rWS, one, (vl_ >> 8) * (64 * 4096) + (vl_ & 0xc0) * 8 + (vl_ & 48) + 448, WS_KV + (quad * 256 + rb * 8 + w) * 4096 + 2048);
        if constexpr (j == 11 && i == 8 - 8 * T) { vcs = 7 * 16384 + g * 4096 + vl_; PIN(vcs); }
        if constexpr (T == 0 && j == 11 && i >= 9 && i < 13) cf[i - 8] = lds128(vcs + (i - 9) * 1024);
        if constexpr (T == 1 && j == 11 && i == 1) sfor<1, 5>([&](auto u) LAM { cf[u.value] = lds128(vcs + (u.value - 1) * 1024); });
        if constexpr (j == jn - 1 && i >= 10 && i < 14) { nm[i - 10] = (u32x4){}; PIN(nm[i - 10]); }
        if constexpr (j == jn && i == 3) {
          if constexpr (T == 1 || P9) __builtin_amdgcn_s_setprio(3);
          PIN(sg);
          await(&sg, vl_, sWS, sst, tag);
        }
        if constexpr (T == 0 && j == 10 && i == 10) { PIN(cf[0]); await(cf, vl_, sWS, scs + 4096, tag); }
        if constexpr (T == 0 ? j == 12 && i == 14 : j == 11 && i == 6) {
          sfor<1, 5>([&](auto u) LAM { PIN(cf[u.value]); });
          await<4>(cf + 1, vl_, sWS, scs, tag);
        }
        if constexpr (j == jn && i == 8) {
          rl = rcp(asf(sg[0] & 0xffff0000u));
          nl = __builtin_amdgcn_perm(sg[0], sg[0], 0x01000100u);
          if constexpr (T == 1) *(LAS float*)(L_RV + mg * 256 + (vl_ >> 2)) = rl;
        }
        if constexpr (j == jn && i >= 12 && i < 16) {
          nm[i - 12][0] = __builtin_amdgcn_ds_bpermute(bt + 64 * (i - 12), nl);
          PIN(bt);
          nm[i - 12][2] = __builtin_amdgcn_ds_bpermute(bt + 64 * (i - 12), nl);
        }
        if constexpr (T == 0 && j == 10 && i < 4) PIN(nm[i]);
        if constexpr (T == 0 && j == 9 && i >= 16) rs[i - 16] = asf(__builtin_amdgcn_ds_bpermute(bt + 64 * (i - 16), asu(rl)));
        if constexpr (T == 0 && j == 10 && i == 24) sq = opaque((isq ? WS_Q : WS_KV) + ((quad & 1) * 512 + rb * 8 + mg * 2) * ts);
        if constexpr (T == 0 && j == 10 && i == 26) sfor<0, 4>([&](auto e) LAM {
          sc[e.value] = rs[e.value] * (isq ? 0.18898223650461362f * 1.4426950408889634f : 1.f);
          PIN(sc[e.value]);
        });
        if constexpr (T == 0 && j == 10 && i >= 29 && i < 33) qk_put(IC<(i >= 29 && i < 33 ? i - 29 : 0)>{});
        if constexpr (T == 0 && j == 10 && i == 33) { __builtin_amdgcn_s_setprio(2); STAMPF(11); }
        if constexpr (T == 0 && j == 11 && i >= 2 && i < 7) norm(IC<(i >= 2 && i < 7 ? i - 2 : 0)>{});
        if constexpr (j == 13 - T) {
          constexpr int tc = (i + 8) % 16 / 4 + 1;
          SBAR();
          if constexpr (T == 1) mac16<false>(acc[tc][m], nm[m], cf[tc]);
          else mac16<false>(acc[tc][m], cf[tc], nm[m]);
          SBAR();
          if constexpr (T == 1 && i == 10) { vcs = L_RV + mg * 256 + ((vl_ >> 4) & 48); PIN(vcs); }
          if constexpr (T == 1 && i >= 10 && i < 14) rv[i - 10] = lds128f(vcs + 64 * (i - 10));
        }
        if constexpr (T == 0 && j > 10 && 16 * (j - 11) + i == DW) { STAMPG(10); vmwait<(DW < 22 ? 0 : DW < 29 ? (DW - 20) / 2 : 4)>(); STAMPG(11); last = cnt_take(0); }
        if constexpr (T == 0 && j > 10 && 16 * (j - 11) + i == DW + 2) { tell(); __builtin_amdgcn_s_setprio(1); }
        if constexpr (T == 0 && j == 13 && i == 0) cnt_add(24);
        if constexpr (T == 1 && j == 13 && i >= 5 && (i & 1)) v_put(IC<(i >= 5 ? (i - 5) / 2 : 0)>{});
        SBAR();
      });
    });
    asm volatile("s_nop 7");
    SBAR();
    STAMP(2);
    if constexpr (T == 1) {
      if (g == 0) { v_put(IC<6>{}); v_put(IC<7>{}); }
      vmwait<0>();
      last = cnt_take(0);
      tell();
    }
    STAMP(3);
    __builtin_amdgcn_s_setprio(0);
    if (PGA && g == 0) __builtin_amdgcn_s_setprio(PGA);
    SBAR();
    if constexpr (T == 0) sfor<0, PI>([&](auto m) LAM { hp[m.value] = geglu(m); PIN(hp[m.value]); });
  };
  if (!t1) phase1(IC<0>{}); else phase1(IC<1>{});
  LANES();
  auto wload = [&](auto B) LAM {
    const u32 so = od + krot(2 * B.value) * 14336;
    dma2<wd_loc(2 * B.value), 0>(sD, vw0, sD, vw1, so, mg * 4096);
    dma2<wd_loc(2 * B.value), 2048>(sD, vw0, sD, vw1, so + 12288, mg * 4096);
  };
  if (w < 4) {
    if (PRIO) __builtin_amdgcn_s_setprio(2);
    u32x4 fk;
    const u32 vfo = (lane < 26 ? lane : 0) * 16, sfo = WS_FQ + (head * 16 + qb) * BOX;
    u32 seen = 0;
    auto inbox = [&](u32 blocks) LAM {
      u32 need = blocks << (qb >> 1);
      need = ((need | need >> 8) & 255) * 0x10101 + 0x3000000;
#pragma nounroll
      for (u32 spins = 0; (seen & need) != need && spins < (1u << 20); ++spins) {
        if (spins) __builtin_amdgcn_s_sleep(NAPA);
        aload_sc1(fk, vfo, sWS, sfo);
        vm_on<0>(fk);
        seen = ~(u32)stale(fk, tag);
      }
    };
    auto hand = [&](auto M0) LAM {
      sfor<M0.value, 4>([&](auto m) LAM { hp[m.value] = geglu(m); PIN(hp[m.value]); });
      cnt_wait(24, 8);
      sfor<0, 4>([&](auto m) LAM { *(LAS u32x4*)(6 * 16384 + w * 4096 + m.value * 1024 + vl) = hp[m.value]; });
      cnt_add(12 + w);
    };
    if (has_tile) {
      aload_sc1(fk, vfo, sWS, sfo);
      if constexpr (GA == 4) hand(IC<PI>{});
      else sfor<PI, GA>([&](auto m) LAM { hp[m.value] = geglu(m); PIN(hp[m.value]); });
      STAMP(3);
      vm_on<0>(fk);
      seen = ~(u32)stale(fk, tag);
    }
    STAMP(4);
    inbox((1u << NR1) - 1);
    STAMP(5);
    const u32 kvb = WS_KV + (head * 64 + 2 * w) * 4096;
    u32 kvc = 0;
    auto kv_next = [&](int kt) LAM {
      if (kt & 1) kvc = opaque(kvc) ^ 4096;
      else kvc = kvb + (qb & 1) * 4096 + ((opaque(qb >> 1) + (kt >> 1)) & 7) * 32768;
    };
    u32x4 Q[4][2], KV[4][4];
    auto q_load = [&](auto t) LAM {
      const u32 so = WS_Q + (head * 64 + qb * 4 + ((w + 1 + t.value) & 3)) * 2048;
      Q[t.value][0] = ld128(rWS, vl, so);
      Q[t.value][1] = ld128(rWS, vl + 1024, so);
    };
    kv_next(0);
    KV[0][0] = ld128(rWS, vl, kvc);
    KV[0][1] = ld128(rWS, vl + 1024, kvc);
    q_load(IC<0>{});
    q_load(IC<1>{});
    KV[0][2] = ld128(rWS, vl + 2048, kvc);
    q_load(IC<2>{});
    KV[0][3] = ld128(rWS, vl + 3072, kvc);
    q_load(IC<3>{});
    SBAR();
    sfor<1, UPF>([&](auto b) LAM {
      kv_next(b.value);
      sfor<0, 4>([&](auto i) LAM { KV[b.value][i.value] = ld128(rWS, vl + i.value * 1024, kvc); });
    });
    SBAR();
    if (C3E) cnt_add(3);
    if constexpr (GA < 4) { if (has_tile) hand(IC<GA>{}); }
    inbox((1u << NR) - 1);
    if (!C3E && C3T < 0) cnt_add(3);
    STAMP(6);
    const int idx = 0xDDDD;
    f32x16 O[4] = {};
    const f32x16 Z = {};
    auto rgn = [&](int t) LAM -> u32 { return (2 + t) * 16384u; };
    auto put_part = [&](auto t, auto i) LAM {
      const f32x16& o = O[t.value];
      *(LAS f32x4*)(rgn((w + 1 + t.value) & 3) + t.value * 4096 + i.value * 1024 + vl) =
          (f32x4){o[4 * i.value], o[4 * i.value + 1], o[4 * i.value + 2], o[4 * i.value + 3]};
    };
    f32x16 Sx[4];
    u32 cb1 = 0;
    constexpr int CK = 8 * NR - 12, CP = CK - (NR > 3 ? 10 : 8);
    auto tile = [&](auto I) LAM {
      constexpr int kt = I.value >> 2, b = kt & 3, t = I.value & 3, tn = (t + 1) & 3, tp = (t + 3) & 3;
      constexpr int bn = t == 3 ? (b + 1) & 3 : b, bp = t == 0 ? (b + 3) & 3 : b;
      constexpr int E1 = PAT ? 3 : 4, E2 = 2 * E1, E3 = 3 * E1;
      auto exps = [&](auto lo, auto hi) LAM {
        SBAR();
        sfor<lo.value, hi.value>([&](auto e) LAM { Sx[t][e.value] = ex2(Sx[t][e.value]); });
        PIN(Sx[t]);
        SBAR();
      };
      if constexpr (I.value < 63) Sx[tn] = mfma32(KV[bn][0], Q[tn][0], Z);
      exps(IC<0>{}, IC<E1>{});
      if constexpr (I.value < 63) Sx[tn] = mfma32(KV[bn][1], Q[tn][1], Sx[tn]);
      if constexpr (I.value < 63) PIN(Sx[tn]);
      exps(IC<E1>{}, IC<E2>{});
      if constexpr (I.value > 0) O[tp] = smf_hi(KV[bp][3], Sx[tp], O[tp], idx);
      PIN(O[tp]);
      exps(IC<E2>{}, IC<E3>{});
      O[t] = smf_lo(KV[b][2], Sx[t], O[t], idx);
      PIN(O[t]);
      exps(IC<E3>{}, IC<E3 + 2>{});
      if constexpr (I.value == CP) aload_sc1(fk, vfo, sWS, sfo);
      constexpr int NX = 4 * (3 - UPF);
      sfor<(I.value < NX ? 4 * UPF + 2 * I.value : I.value + 12), (I.value < NX ? 4 * UPF + 2 * I.value + 2 : I.value + 13)>([&](auto L) LAM {
        if constexpr (L.value < 64) {
          if constexpr ((L.value & 3) == 0) kv_next(L.value >> 2);
          KV[(L.value >> 2) & 3][L.value & 3] = ld128(rWS, vl + (L.value & 3) * 1024, kvc);
        }
      });
      if constexpr (I.value == C3T) cnt_add(3);
      if constexpr (I.value == ID) cnt_add(9);
      if constexpr (I.value == 58) cb1 = cnt_get(1);
      if constexpr (I.value == 60) { if (has_tile && __builtin_amdgcn_readfirstlane(cb1) < 4u) cnt_wait(1, 4); cfence(); }
      if constexpr (I.value >= 62) sfor<0, 4>([&](auto i) LAM { put_part(IC<I.value - 62>{}, i); });
      exps(IC<E3 + 2>{}, IC<16>{});
    };
    Sx[0] = mfma32(KV[0][1], Q[0][1], mfma32(KV[0][0], Q[0][0], Z));
    SBAR();
    sfor<0, CK>(tile);
    vm_on<CK - CP>(fk);
    await(&fk, vfo, sWS, sfo, tag);
    float bound = asf(__builtin_amdgcn_readlane(fk[0], 24)) + asf(__builtin_amdgcn_readlane(fk[0], 25));
    {
      u32 ka_ = fk[0], kb_ = fk[2];
      swap16(ka_, kb_);
      bound *= max8_nonneg(asf(ka_) + asf(kb_));
    }
    PIN(bound);
    sfor<CK, 64>(tile);
    O[3] = smf_hi(KV[3][3], Sx[3], O[3], idx);
    if (__builtin_amdgcn_ballot_w64(!(bound <= 4000.f)) != 0) {
      sfor<0, 4>([&](auto t) LAM { O[t.value] = Z; });
      auto kvo = [&](int kt) LAM -> u32 { return kvb + (kt >> 1) * 32768 + (kt & 1) * 4096; };
      float mx[4] = {-3.0e38f, -3.0e38f, -3.0e38f, -3.0e38f};
      for (int kt = 0; kt < 16; ++kt) {
        const u32x4 k0 = ld128(rWS, vl, kvo(kt)), k1 = ld128(rWS, vl + 1024, kvo(kt));
        sfor<0, 4>([&](auto t) LAM {
          const f32x16 s = mfma32(k1, Q[t.value][1], mfma32(k0, Q[t.value][0], Z));
          sfor<0, 16>([&](auto i) LAM { mx[t.value] = __builtin_fmaxf(mx[t.value], s[i.value]); });
        });
      }
      sfor<0, 4>([&](auto t) LAM {
        mx[t.value] = __builtin_fmaxf(mx[t.value], other_half(mx[t.value]));
        *(LAS float*)(L_VT + ((((w + 1 + t.value) & 3) * 32 + r32) * 4 + w) * 4) = mx[t.value];
      });
      cnt_add(8);
      cnt_wait(8, 4);
      sfor<0, 4>([&](auto t) LAM {
        const f32x4 m4 = lds128f(L_VT + (((w + 1 + t.value) & 3) * 32 + r32) * 16);
        mx[t.value] = __builtin_fmaxf(__builtin_fmaxf(m4[0], m4[1]), __builtin_fmaxf(m4[2], m4[3]));
      });
      for (int kt = 0; kt < 16; ++kt) {
        const u32x4 k0 = ld128(rWS, vl, kvo(kt)), k1 = ld128(rWS, vl + 1024, kvo(kt));
        const u32x4 v0 = ld128(rWS, vl + 2048, kvo(kt)), v1 = ld128(rWS, vl + 3072, kvo(kt));
        sfor<0, 4>([&](auto t) LAM {
          f32x16 s = mfma32(k1, Q[t.value][1], mfma32(k0, Q[t.value][0], Z));
          sfor<0, 16>([&](auto i) LAM { s[i.value] = ex2(s[i.value] - mx[t.value]); });
          O[t.value] = smf_hi(v1, s, smf_lo(v0, s, O[t.value], idx), idx);
        });
      }
      sfor<0, 2>([&](auto t) LAM { sfor<0, 4>([&](auto i) LAM { put_part(t, i); }); });
    }
    STAMP(7);
    sfor<0, 4>([&](auto i) LAM { put_part(IC<2>{}, i); });
    cnt_add(2);
    const u32 red = rgn(w);
    {
      LANES();
      float o[16];
      f32x4 pt[12];
      read_when<12>(pt, 2, 4, 2, 4, [&](int i) LAM -> u32 { return red + i * 1024 + vl; });
      sfor<0, 4>([&](auto i) LAM {
        const f32x4 a0 = pt[i.value], a1 = pt[4 + i.value], a2 = pt[8 + i.value];
        sfor<0, 4>([&](auto j) LAM {
          if constexpr (CPK) o[4 * i.value + j.value] = (a0[j.value] + a1[j.value]) + (a2[j.value] + O[3][4 * i.value + j.value]);
          else o[4 * i.value + j.value] = add1(add1(a0[j.value], a1[j.value]), add1(a2[j.value], O[3][4 * i.value + j.value]));
        });
      });
      const float lo_ = other_half(o[12]);
      const float il = rcp(hf ? o[12] : lo_);
      u32 P[8];
      sfor<0, 8>([&](auto i) LAM { P[i.value] = CPK ? cvtpk(o[2 * i.value] * il, o[2 * i.value + 1] * il) : cvtpk(mul1(o[2 * i.value], il), mul1(o[2 * i.value + 1], il)); });
      const int om = qb * 4 + w;
      st128(rWS, (u32x4){P[0], P[1], P[2], P[3]}, vl, WS_O + (om * 16 + head) * 2048);
      st128(rWS, (u32x4){P[4], P[5], hf ? 0u : P[6], hf ? 0u : P[7]}, vl + 1024, WS_O + (om * 16 + head) * 2048);
      if (has_tile) {
        const u32x4 sO = mk_srd(pwo, D_ * D_ * 2);
        const u32 vw = (lane >> 3) * 896 + (((lane & 7) ^ ((lane >> 2) & 4)) << 4);
        sfor<0, 14>([&](auto e) LAM { dma_i<e.value * 1024, 8 * e.value * 896>(sO, vw, 112 * w * 896 + nt * 128, red); });
        vmwait<14>();
      } else vmwait<0>();
      if (lane == 0) put_flag(ws, WS_FO + (om * 16 + head) * FSO, tag);
    }
    STAMP(8);
    if (!has_tile) { STAMP_END(); return; }
    {
    LANES();
    const u32 trow = (4 * hf + q4) * 128, swz = (q4 & 2) << 5;
    const int m = w & 1, kh = w >> 1;
    const rsrc_t rX = __builtin_amdgcn_make_buffer_rsrc((void*)px, 0, S_ * D_ * 2, 0x00020000);
    const u32 xo = ((rt * 64 + m * 32 + r32) * 448 + nt * 64 + kh * 32 + hf * 8) * 2;
    u32x4 xr[2];
    xr[0] = __builtin_amdgcn_raw_buffer_load_b128(rX, xo, 0, 0);
    xr[1] = __builtin_amdgcn_raw_buffer_load_b128(rX, xo + 32, 0, 0);
    PIN(xr[0]); PIN(xr[1]);
    vmwait<0>();
    cnt_add(6);
    u32x4 of[16];
    poll_flags<NAPO>(ws, WS_FO + ((2 * rt + m) * 16 + 8 * kh + (lane & 7)) * FSO, tag);
    STAMP(9);
    sfor<0, 16>([&](auto k) LAM { of[k.value] = ld128(rWS, vl + (k.value & 3) * 1024, WS_O + ((2 * rt + m) * 16 + 8 * kh) * 2048 + (k.value >> 2) * 4096); });
    SBAR();
    cnt_wait(6, 4);
    u32 xs[8] = {xr[0][0], xr[0][1], xr[0][2], xr[0][3], xr[1][0], xr[1][1], xr[1][2], xr[1][3]};
    swap32(xs[0], xs[2]); swap32(xs[1], xs[3]); swap32(xs[4], xs[6]); swap32(xs[5], xs[7]);
    auto xch = [&](int v, int i) LAM -> u32 { return i < 3 ? L_QK + v * 3072 + i * 1024 : rgn(v) + 14336; };
    f32x16 acc[2] = {};
    sfor<0, 2>([&](auto r) LAM {
      const u32 rg = rgn(2 * kh + r.value);
      u32 wa[2], wp[2];
      sfor<0, 2>([&](auto n) LAM {
        wa[n.value] = rg + trow + ((64 * (n.value ^ kh) + 32 * blk + 8 * p4) ^ swz);
        wp[n.value] = wa[n.value] - hf * 512;
      });
      sfor<0, 8>([&](auto k) LAM {
        constexpr int hh = k.value >> 1, s = k.value & 1;
        constexpr u32 off = (28 * hh + 16 * s) * 128;
        sfor<0, 2>([&](auto n) LAM {
          constexpr int nn = 1 - n.value;
          const u32x4 wf = trd2(wa[nn] + off, (s ? wp[nn] : wa[nn]) + off + 1024);
          acc[nn] = mfma32(wf, of[8 * r.value + k.value], acc[nn]);
        });
      });
    });
    sfor<0, 4>([&](auto i) LAM {
      *(LAS f32x4*)(xch(w, i.value) + vl) =
          (f32x4){acc[1][4 * i.value], acc[1][4 * i.value + 1], acc[1][4 * i.value + 2], acc[1][4 * i.value + 3]};
    });
    STAMP(10);
    cnt_add(6 + m);
    f32x4 gt[8];
    read_when<8>(gt, 5, 4, 6 + m, 6 - 4 * m, [&](int i) LAM -> u32 { return (i < 4 ? 16384 + w * 4096 + i * 1024 : xch(w ^ 2, i - 4)) + vl; });
    u32 R[8];
    sfor<0, 4>([&](auto i) LAM {
      const f32x4 a = gt[4 + i.value];
      float y[4];
      sfor<0, 4>([&](auto j) LAM {
        const u32 xd = xs[2 * i.value + (j.value >> 1)];
        const float xv = asf((j.value & 1) ? (xd & 0xffff0000u) : (xd << 16));
        y[j.value] = __builtin_fmaf(acc[0][4 * i.value + j.value] + a[j.value], gt[i.value][j.value], xv);
      });
      R[2 * i.value] = cvtpk(y[0], y[1]);
      R[2 * i.value + 1] = cvtpk(y[2], y[3]);
    });
    swap32(R[0], R[2]); swap32(R[1], R[3]); swap32(R[4], R[6]); swap32(R[5], R[7]);
    const rsrc_t rOut = __builtin_amdgcn_make_buffer_rsrc(pout, 0, S_ * D_ * 2, 0x00020000);
    st128p(rOut, (u32x4){R[0], R[1], R[2], R[3]}, xo);
    st128p(rOut, (u32x4){R[4], R[5], R[6], R[7]}, xo + 32);
    asm volatile(".Lfb_enda:");
    STAMP(11);
    STAMP_END();
    }
  } else {
    const u32 trow = (4 * hf + q4) * 128, swz = (q4 & 2) << 5;
    const int gw = w - 4;
    const u32 lg = gw * 4096;
    if (has_tile) {
      u32x4 ha[4];
      sfor<PI, 4>([&](auto m) LAM { hp[m.value] = geglu(m); PIN(hp[m.value]); });
      STAMP(4);
      cnt_wait(12 + gw, 1);
      sfor<0, 4>([&](auto m) LAM { ha[m.value] = lds128(6 * 16384 + lg + m.value * 1024 + vl); PIN(ha[m.value]); });
      if constexpr (WE == 2) { wload(IC<4>{}); wload(IC<8>{}); }
      const u32 fo = frag_off(), ho = WS_H + ((rb * 8 + gw * 2) * 112 + un * 4) * 1024;
      cnt_wait(3, HCNT);
      if constexpr (WE == 1) { wload(IC<4>{}); wload(IC<8>{}); }
      STAMP(9);
      sfor<0, 8>([&](auto i) LAM {
        constexpr int m = i.value >> 1;
        st128(rWS, (i.value & 1) ? hp[m] : ha[m], fo + (m & 1) * 256 + (i.value & 1) * 2048, ho + (m >> 1) * (112 * 1024));
        if constexpr (i.value < 7) __builtin_amdgcn_s_sleep(HP);
      });
      if constexpr (WE == 3) { wload(IC<4>{}); wload(IC<8>{}); vmwait<8>(); }
      else vmwait<0>();
      if (lane == 0) put_flag(ws, WS_FH + ((rb * 4 + gw) * 32 + un) * FSH, tag);
      STAMP(5);
      if constexpr (WE == 0) { wload(IC<4>{}); wload(IC<8>{}); }
      if constexpr (WL == 0) sfor<5, 8>(wload);
      touch(pwo + ((rq & 3) * 112 + gw * 28 + (lane < 28 ? lane : 0)) * 896 + nt * 128);
      STAMP(8);
    }
    if (bid == 255 && gw == 0) {
      for (u32 i = 0; i < 4; ++i) {
        u32x4 f = {0, 0, 0, (u32)~(tag >> 32)};
        await(&f, lane * 128, sWS, WS_ST + i * 8192, tag);
      }
      if (lane == 0) put_flag(ws, WS_EPOCH, epoch + 1);
    }
    if (!has_tile) { STAMP_END(); return; }
    const u32x4 sH = mk_srd(ws + WS_H, 64 * 112 * 1024);
    const u32 oh0 = ((2 * rt + (gw & 1)) * 112 + 28 * gw) * 1024, oh1 = ((2 * rt + 1 - (gw & 1)) * 112 + 28 * gw) * 1024;
    u32x4 hfr[DPH][2];
    auto hload = [&](auto K) LAM {
      aload_sc1(hfr[K.value % DPH][0], vl, sH, oh0 + krot(K.value) * 1024);
      aload_sc1(hfr[K.value % DPH][1], vl, sH, oh1 + krot(K.value) * 1024);
    };
    cnt_wait(3, 4);
    poll_flags<NAPB>(ws, WS_FH + (rt * 32 + 7 * gw + (lane < 7 ? lane : 0)) * FSH, tag);
    STAMP(6);
    sfor<0, DPH>([&](auto K) LAM { hload(K); });
    __builtin_amdgcn_s_setprio(PRB);
    u32 wa[2];
    sfor<0, 2>([&](auto n) LAM { wa[n.value] = lg + trow + ((64 * (n.value ^ (gw >> 1)) + 32 * blk + 8 * p4) ^ swz); });
    f32x16 acc[2][2] = {};
    u32x4 wf[2][2];
    auto ready = [&](auto K, auto N) LAM {
      constexpr int k = K.value;
      constexpr int o = wd_loc(k), d = (o & ~65535) - (k ? (int)(wd_loc(k - 1) & ~65535) : 0);
      vm_on2<N.value>(hfr[k % DPH][0], hfr[k % DPH][1]);
      sfor<0, 2>([&](auto n) LAM {
        if constexpr (d != 0) { wa[n.value] += d; PIN(wa[n.value]); }
        wf[k & 1][n.value] = trd2(wa[n.value] + (o & 65535), wa[n.value] + (o & 65535) + 2048);
      });
    };
    ready(IC<0>{}, IC<2 * (DPH - 1)>{});
    sfor<0, 28>([&](auto K) LAM {
      constexpr int k = K.value;
      if constexpr (k == KD) cnt_wait(9, 4);
      if constexpr (k + 1 < 28 && k != KD - 1) ready(IC<k + 1>{}, IC<gemm_wait(k + 1)>{});
      sfor<0, 2>([&](auto n) LAM {
        sfor<0, 2>([&](auto m) LAM { acc[m.value][n.value] = mfma32(wf[k & 1][n.value], hfr[k % DPH][m.value], acc[m.value][n.value]); PIN(acc[m.value][n.value]); });
      });
      SBAR();
      if constexpr (k == 7) cnt_add(1);
      if constexpr (k + DPH < 28) hload(IC<k + DPH>{});
      sfor<9, 14>([&](auto B) LAM { if constexpr (wd_at(B.value) == k) wload(B); });
      if constexpr (k == KD - 1) ready(IC<KD>{}, IC<gemm_wait(KD)>{});
      SBAR();
    });
    STAMP(7);
    sfor<1, 4>([&](auto x) LAM {
      const f32x16& a = acc[x.value & 1][x.value >> 1];
      sfor<0, 4>([&](auto i) LAM {
        *(LAS f32x4*)((5 + x.value) * 16384 + lg + i.value * 1024 + vl) = (f32x4){a[4 * i.value], a[4 * i.value + 1], a[4 * i.value + 2], a[4 * i.value + 3]};
      });
    });
    cnt_add(4);
    f32x4 pt[12];
    read_when<12>(pt, 4, 4, 4, 4, [&](int i) LAM -> u32 { return (6 + i / 4) * 16384 + (gw ^ (1 + i / 4)) * 4096 + (i & 3) * 1024 + vl; });
    sfor<0, 4>([&](auto i) LAM {
      const u32 ra = i.value * 1024 + vl;
      const f32x4 a1 = pt[i.value], a2 = pt[4 + i.value], a3_ = pt[8 + i.value];
      f32x4 gv;
      sfor<0, 4>([&](auto j) LAM {
        if constexpr (BPK) {
          const float f = (acc[0][0][4 * i.value + j.value] + a1[j.value]) + (a2[j.value] + a3_[j.value]);
          gv[j.value] = f * rcp(1.f + ex2(f * -1.4426950408889634f));
        } else {
          const float f = add1(add1(acc[0][0][4 * i.value + j.value], a1[j.value]), add1(a2[j.value], a3_[j.value]));
          gv[j.value] = mul1(f, rcp(add1(1.f, ex2(mul1(f, -1.4426950408889634f)))));
        }
      });
      *(LAS f32x4*)(1 * 16384 + lg + ra) = gv;
    });
    asm volatile(".Lfb_endb:");
    cnt_add(5);
    STAMP(10);
    STAMP_END();
  }
}

extern "C" void kernel_launch(void* const* d_in, const int* in_sizes, int n_in, void* d_out, int out_size, void* d_ws,
                              size_t ws_size, hipStream_t stream) {
  fused_block<<<256, 512, 0, stream>>>(d_in[0], d_in[1], d_in[2], d_in[4], d_in[5], d_in[6], d_out, (char*)d_ws);
}
```

```cpp
#include <hip/hip_runtime.h>
#include <stdint.h>
#pragma clang diagnostic ignored "-Wint-to-pointer-cast"

#define DEV __device__ __forceinline__
#define LAM __attribute__((always_inline))
#define LAS __attribute__((address_space(3)))
#define GAS __attribute__((address_space(1)))

#define STAMPS 0

typedef unsigned u32;
typedef unsigned long long u64;
typedef u32 u32x2 __attribute__((ext_vector_type(2)));
typedef u32 u32x4 __attribute__((ext_vector_type(4)));
typedef short s16x4 __attribute__((ext_vector_type(4)));
typedef float f32x2 __attribute__((ext_vector_type(2)));
typedef float f32x4 __attribute__((ext_vector_type(4)));
typedef float f32x8 __attribute__((ext_vector_type(8)));
typedef float f32x16 __attribute__((ext_vector_type(16)));
typedef __bf16 bf16x8 __attribute__((ext_vector_type(8)));
typedef __bf16 bf16x16 __attribute__((ext_vector_type(16)));

extern "C" __device__ u64 llvm_dispatch_id() __asm("llvm.amdgcn.dispatch.id");

constexpr int S_ = 2048, D_ = 448, F_ = 1792;
#define V3 1
#define V4 1
#define V5 1
#define RWS 0
#define BPK 1
#define FSQ 16
#define FSH 8
#define FSO 8
constexpr u32 WS_EPOCH = 0, WS_ST = 32768, WS_DBG = 65536;
constexpr u32 WS_Q = 1u << 20, WS_KV = 3u << 20, WS_O = 7u << 20, WS_H = 9u << 20;
constexpr u32 WS_CS = 16u << 20, WS_FO = WS_CS + (512u << 10), WS_FH = WS_FO + (256u << 10), WS_FQ = WS_FH + (256u << 10), WS_END = 18u << 20;
constexpr u32 BOX = 512;
constexpr u32 L_QK = 147456, L_CS = 159744, L_CNT = L_CS + 2560, L_DUM = L_CS + 2816, L_RV = L_CS + 3072;
constexpr u32 OOB = 0x7ffffff0;
constexpr u32 L_VT = 5 * 16384 + 14336;
constexpr u32 LDS_BYTES = 163840;

template <int V> struct IC { static constexpr int value = V; };
template <int I, int N, class Fn> DEV void sfor(Fn&& f) {
  if constexpr (I < N) { f(IC<I>{}); sfor<I + 1, N>(f); }
}

#define SBAR() __builtin_amdgcn_sched_barrier(0)
template <class T> DEV void PIN(T& x) { asm volatile("" : "+v"(x)); }
template <class T> DEV void USE(const T& x) { asm volatile("" ::"v"(x)); }
template <class T> DEV void undef(T& x) { asm volatile("" : "=v"(x)); }
template <class T> DEV void nop_on(T& x) { asm volatile("s_nop 7" : "+v"(x)); }
template <class T> DEV void lgkm0_on(T& x) { asm volatile("s_waitcnt lgkmcnt(0)" : "+v"(x)::"memory"); }
template <int T> DEV void colsum(f32x4& c0, f32x4& c1, u32x4 one, const u32x4* w, int who) {
#define CSM(k, c, r) "s_cmp_lg_u32 %8, " #k "\n\ts_cbranch_scc1 .Lcs" #r "_%=\n\tv_mfma_f32_16x16x32_bf16 %" #c ", %2, %" #r ", %" #c "\n.Lcs" #r "_%=:\n\t"
  if constexpr (T == 0)
    asm volatile(CSM(0, 0, 3) CSM(1, 0, 4) CSM(2, 0, 5) CSM(3, 0, 6) CSM(0, 1, 7)
                 : "+v"(c0), "+v"(c1) : "v"(one), "v"(w[1]), "v"(w[2]), "v"(w[3]), "v"(w[4]), "v"(w[0]), "s"(who) : "scc");
  else
    asm volatile(CSM(0, 0, 3) CSM(1, 0, 4) CSM(2, 0, 5) CSM(3, 0, 6)
                 : "+v"(c0), "+v"(c1) : "v"(one), "v"(w[1]), "v"(w[2]), "v"(w[3]), "v"(w[4]), "v"(one), "s"(who) : "scc");
#undef CSM
}
DEV void mfma4_unless(f32x4* c, const u32x4* a, u32x4 b, int skip) {
  asm volatile("s_cmp_lg_u32 %9, 0\n\ts_cbranch_scc1 .Lm4_%=\n\tv_mfma_f32_16x16x32_bf16 %0, %4, %8, %0\n\tv_mfma_f32_16x16x32_bf16 %1, %5, %8, %1\n\t"
               "v_mfma_f32_16x16x32_bf16 %2, %6, %8, %2\n\tv_mfma_f32_16x16x32_bf16 %3, %7, %8, %3\n.Lm4_%=:"
               : "+v"(c[0]), "+v"(c[1]), "+v"(c[2]), "+v"(c[3]) : "v"(a[0]), "v"(a[1]), "v"(a[2]), "v"(a[3]), "v"(b), "s"(skip) : "scc");
}
template <int OFF> DEV void aload(u32x4& d, u32 voff, u32x4 srd, u32 soff) {
  asm volatile("buffer_load_dwordx4 %0, %1, %2, %3 offen offset:%4" : "=v"(d) : "v"(voff), "s"(srd), "s"(soff), "i"(OFF) : "memory");
}
template <int N> DEV void vm_on(u32x4& a) { asm volatile("s_waitcnt vmcnt(%1)" : "+v"(a) : "i"(N) : "memory"); }
template <int OFF = 0> DEV void aload_sc1(u32x4& d, u32 voff, u32x4 srd, u32 soff) {
  asm volatile("buffer_load_dwordx4 %0, %1, %2, %3 offen offset:%4 sc1" : "=v"(d) : "v"(voff), "s"(srd), "s"(soff), "i"(OFF) : "memory");
}
template <int B> DEV void load_if(u32x4* d, u32 voff, u32x4 srd, u32 soff, u32 mask) {
  u32 s2 = soff + 4096;
#define LIF(h, a, b, s, o) "s_bitcmp0_b32 %12, %13+" #h "\n\ts_cbranch_scc1 .Lli" #h "_%=\n\tbuffer_load_dwordx4 %" #a ", %8, %9, " s " offen offset:" #o " sc1\n\t" \
  "buffer_load_dwordx4 %" #b ", %8, %9, " s " offen offset:" #o "+1024 sc1\n.Lli" #h "_%=:\n\t"
  asm volatile(LIF(0, 0, 1, "%10", 0) LIF(1, 2, 3, "%10", 2048) LIF(2, 4, 5, "%11", 0) LIF(3, 6, 7, "%11", 2048)
               : "+v"(d[0]), "+v"(d[1]), "+v"(d[2]), "+v"(d[3]), "+v"(d[4]), "+v"(d[5]), "+v"(d[6]), "+v"(d[7])
               : "v"(voff), "s"(srd), "s"(soff), "s"(s2), "s"(mask), "i"(B) : "memory", "scc");
#undef LIF
}
template <int N> DEV void vm_on2(u32x4& a, u32x4& b) { asm volatile("s_waitcnt vmcnt(%2)" : "+v"(a), "+v"(b) : "i"(N) : "memory"); }

DEV f32x16 mfma32(u32x4 a, u32x4 b, f32x16 c) {
  return __builtin_amdgcn_mfma_f32_32x32x16_bf16(__builtin_bit_cast(bf16x8, a), __builtin_bit_cast(bf16x8, b), c, 0, 0, 0);
}
template <bool FIRST> DEV void mac16(f32x4& c, u32x4 a, u32x4 b) {
  if constexpr (FIRST) asm volatile("v_mfma_f32_16x16x32_bf16 %0, %1, %2, 0" : "=&v"(c) : "v"(a), "v"(b));
  else asm volatile("v_mfma_f32_16x16x32_bf16 %0, %1, %2, %0" : "+v"(c) : "v"(a), "v"(b));
}
DEV f32x16 smf_lo(u32x4 a, f32x16 p, f32x16 c, int idx) {
  f32x8 b = __builtin_shufflevector(p, p, 0, 1, 2, 3, 4, 5, 6, 7);
  return __builtin_amdgcn_smfmac_f32_32x32x32_bf16(__builtin_bit_cast(bf16x8, a), __builtin_bit_cast(bf16x16, b), c, idx, 0, 0);
}
DEV f32x16 smf_hi(u32x4 a, f32x16 p, f32x16 c, int idx) {
  f32x8 b = __builtin_shufflevector(p, p, 8, 9, 10, 11, 12, 13, 14, 15);
  return __builtin_amdgcn_smfmac_f32_32x32x32_bf16(__builtin_bit_cast(bf16x8, a), __builtin_bit_cast(bf16x16, b), c, idx, 0, 0);
}
typedef __bf16 bf16x2 __attribute__((ext_vector_type(2)));
DEV u32 cvtpk(float lo, float hi) {
  f32x2 v = {lo, hi};
  return __builtin_bit_cast(u32, __builtin_convertvector(v, bf16x2));
}
DEV float asf(u32 u) { return __builtin_bit_cast(float, u); }
DEV u32 asu(float f) { return __builtin_bit_cast(u32, f); }
DEV float mul1(float a, float b) { float r; asm("v_mul_f32 %0, %1, %2" : "=v"(r) : "v"(a), "v"(b)); return r; }
DEV float add1(float a, float b) { float r; asm("v_add_f32 %0, %1, %2" : "=v"(r) : "v"(a), "v"(b)); return r; }
DEV float fma1(float a, float b, float c) { float r; asm("v_fma_f32 %0, %1, %2, %3" : "=v"(r) : "v"(a), "v"(b), "v"(c)); return r; }
DEV void scale4(f32x4& a, float s) {
  float x = a[0], y = a[1], z = a[2], w = a[3];
  asm("v_mul_f32 %0, %0, %4\n\tv_mul_f32 %1, %1, %4\n\tv_mul_f32 %2, %2, %4\n\tv_mul_f32 %3, %3, %4" : "+v"(x), "+v"(y), "+v"(z), "+v"(w) : "v"(s));
  a = (f32x4){x, y, z, w};
}
DEV float sumsq4(f32x4 a) {
  float r, t;
  asm("v_mul_f32 %0, %2, %2\n\tv_mul_f32 %1, %3, %3\n\tv_fmac_f32 %0, %4, %4\n\tv_fmac_f32 %1, %5, %5\n\tv_add_f32 %0, %0, %1"
      : "=&v"(r), "=&v"(t) : "v"(a[0]), "v"(a[1]), "v"(a[2]), "v"(a[3]));
  return r;
}
DEV float bfround(float f) { return asf(cvtpk(f, f) << 16); }
DEV float ex2(float x) { return __builtin_amdgcn_exp2f(x); }
DEV float rcp(float x) { return __builtin_amdgcn_rcpf(x); }
DEV float other_half(float v) {
  auto r = __builtin_amdgcn_permlane32_swap(asu(v), asu(v), false, false);
  return asf(r[0] ^ r[1] ^ asu(v));
}
DEV u32x4 mk_srd(const void* p, u32 bytes) {
  u64 a = (u64)p;
  u32x4 r = {(u32)a, (u32)(a >> 32) & 0xffffu, bytes, 0x00020000u};
  return r;
}
template <int LD> DEV void dma_half(u32x4 srd, u32 voff, u32 so, u32 l0) {
#define EXH "0"
  asm volatile("s_add_u32 m0, %0, %1\n\ts_mov_b32 exec_hi, " EXH "\n\tbuffer_load_dwordx4 %2, %3, %4 offen lds\n\ts_mov_b32 exec_hi, -1"
               ::"s"(l0), "i"(LD), "v"(voff), "s"(srd), "s"(so) : "memory", "scc");
}
template <int LD, int OFF> DEV void dma1(u32x4 srd, u32 voff, u32 so, u32 l0) {
  asm volatile("s_add_u32 m0, %0, %1\n\ts_nop 0\n\tbuffer_load_dwordx4 %2, %3, %4 offen offset:%5 lds"
               ::"s"(l0), "i"(LD), "v"(voff), "s"(srd), "s"(so), "i"(OFF) : "memory", "scc");
}
template <int LD, int OFF> DEV void dma2(u32x4 s0, u32 v0, u32x4 s1, u32 v1, u32 so, u32 l0) {
  asm volatile("s_add_u32 m0, %0, %1\n\ts_nop 0\n\tbuffer_load_dwordx4 %2, %3, %6 offen offset:%7 lds\n\tbuffer_load_dwordx4 %4, %5, %6 offen offset:%8 lds"
               ::"s"(l0), "i"(LD), "v"(v0), "s"(s0), "v"(v1), "s"(s1), "s"(so), "i"(OFF), "i"(OFF + 1024) : "memory", "scc");
}
DEV void dma_sc1(u32x4 srd, u32 voff, u32 so, u32 l0) {
  asm volatile("s_mov_b32 m0, %0\n\ts_nop 0\n\tbuffer_load_dwordx4 %1, %2, %3 offen sc1 lds" ::"s"(l0), "v"(voff), "s"(srd), "s"(so) : "memory");
}
DEV u32 opaque(u32 x) { asm volatile("" : "+s"(x)); return x; }
template <int LD, int ADD> DEV void dma_i(u32x4 srd, u32 voff, u32 s0, u32 l0) {
  u32 t;
  asm volatile("s_add_u32 m0, %1, %2\n\ts_add_u32 %0, %3, %4\n\tbuffer_load_dwordx4 %5, %6, %0 offen lds"
               : "=&s"(t) : "s"(l0), "i"(LD), "s"(s0), "i"(ADD), "v"(voff), "s"(srd) : "memory", "scc");
}
DEV int lane_id() {
  u32 z = 0;
  asm volatile("" : "+s"(z));
  return __builtin_amdgcn_mbcnt_hi(~0u, __builtin_amdgcn_mbcnt_lo(~0u, z));
}
#define LANES() const int lane = lane_id(), r32 = lane & 31, hf = lane >> 5, blk = (lane >> 4) & 1, q4 = (lane & 15) >> 2, p4 = lane & 3; \
  const u32 vl = lane * 16; (void)r32; (void)hf; (void)blk; (void)q4; (void)p4; (void)vl
DEV void touch(const void* p) {
  asm volatile("s_mov_b32 m0, %0\n\ts_nop 0\n\tglobal_load_lds_dword %1, off" ::"i"(L_DUM), "v"(p) : "memory");
}
DEV void touch_b(u32x4 srd, u32 voff, u32 so) {
  asm volatile("s_mov_b32 m0, %0\n\ts_nop 0\n\tbuffer_load_dword %1, %2, %3 offen lds" ::"i"(L_DUM), "v"(voff), "s"(srd), "s"(so) : "memory");
}
template <int N> DEV void vmwait() { asm volatile("s_waitcnt vmcnt(%0)" ::"i"(N) : "memory"); }
DEV void cfence() { asm volatile("" ::: "memory"); }
DEV void cnt_add(int i) {
  asm volatile("s_mov_b64 exec, 1\n\tds_inc_u32 %0, %0\n\ts_mov_b64 exec, -1" ::"v"(L_CNT + 4 * i) : "memory");
}
DEV u32 cnt_take(int i) {
  u32 r;
  asm volatile("s_mov_b64 exec, 1\n\tds_inc_rtn_u32 %0, %1, %1\n\ts_mov_b64 exec, -1" : "=v"(r) : "v"(L_CNT + 4 * i) : "memory");
  return r;
}
DEV u32 cnt_get(int i) { return __hip_atomic_load((LAS u32*)(L_CNT + 4 * i), __ATOMIC_RELAXED, __HIP_MEMORY_SCOPE_WORKGROUP); }
DEV void cnt_wait(int i, u32 n) {
  while (__builtin_amdgcn_readfirstlane(cnt_get(i)) < n) __builtin_amdgcn_s_sleep(1);
  cfence();
}
DEV u32x2 trd(u32 a) { return __builtin_bit_cast(u32x2, __builtin_amdgcn_ds_read_tr16_b64_v4i16((LAS s16x4*)a)); }
DEV u32x4 trd2(u32 a0, u32 a1) { u32x2 x = trd(a0), y = trd(a1); u32x4 r = {x[0], x[1], y[0], y[1]}; return r; }
DEV u32x4 lds128(u32 a) { return *(LAS u32x4*)a; }
DEV f32x4 lds128f(u32 a) { return *(LAS f32x4*)a; }
template <int N, class Fn> DEV void read_when(f32x4* d, int i, u32 n, int i2, u32 n2, Fn&& at) {
  for (;;) {
    const u32 c = cnt_get(i), c2 = i2 != i ? cnt_get(i2) : n2;
    cfence();
    sfor<0, N>([&](auto k) LAM { d[k.value] = lds128f(at(k.value)); });
    sfor<0, N>([&](auto k) LAM { PIN(d[k.value]); });
    if (__builtin_amdgcn_readfirstlane(c) >= n && __builtin_amdgcn_readfirstlane(c2) >= n2) break;
    if (RWS) __builtin_amdgcn_s_sleep(RWS);
  }
}
template <int N, int L = 0> DEV void sync_stage() { asm volatile("s_waitcnt vmcnt(%0) lgkmcnt(%1)\n\ts_barrier" ::"i"(N), "i"(L) : "memory"); }
template <int I> DEV void wlane(u32& f, u32 v) { asm volatile("v_writelane_b32 %0, %1, %2" : "+v"(f) : "s"(v), "i"(I)); }
typedef __amdgpu_buffer_rsrc_t rsrc_t;
DEV void st128(rsrc_t r, u32x4 v, u32 voff, u32 soff) {
  __builtin_amdgcn_raw_buffer_store_b128(v, r, voff, __builtin_amdgcn_readfirstlane(soff), 16);
  asm volatile("s_nop 1" ::"v"(v) : "memory");
}
DEV void st128p(rsrc_t r, u32x4 v, u32 voff) {
  __builtin_amdgcn_raw_buffer_store_b128(v, r, voff, 0, 16);
}
DEV void aflag(u64& d, const char* p) { asm volatile("global_load_dwordx2 %0, %1, off sc1" : "=v"(d) : "v"(p) : "memory"); }
template <int N> DEV void vm_on1(u64& a) { asm volatile("s_waitcnt vmcnt(%1)" : "+v"(a) : "i"(N) : "memory"); }
DEV void swap32(u32& a, u32& b) {
  auto r = __builtin_amdgcn_permlane32_swap(a, b, false, false);
  a = r[0]; b = r[1];
}
DEV void swap16(u32& a, u32& b) {
  auto r = __builtin_amdgcn_permlane16_swap(a, b, false, false);
  a = r[0]; b = r[1];
}
DEV u32 from_lane(int l, u32 v) { return __builtin_amdgcn_ds_bpermute(l * 4, v); }
DEV u32x4 ld128(rsrc_t r, u32 voff, u32 soff) { return __builtin_amdgcn_raw_buffer_load_b128(r, voff, __builtin_amdgcn_readfirstlane(soff), 16); }
DEV void put_flag(char* ws, u32 off, u64 tag) { __hip_atomic_store((GAS u64*)(ws + off), tag, __ATOMIC_RELAXED, __HIP_MEMORY_SCOPE_AGENT); }
template <int NAP> DEV void poll_flags(char* ws, u32 off, u64 tag) {
#pragma nounroll
  for (u32 spins = 0; spins < (1u << 20); ++spins) {
    u64 v = __hip_atomic_load((GAS u64*)(ws + off), __ATOMIC_RELAXED, __HIP_MEMORY_SCOPE_AGENT);
    if (__builtin_amdgcn_ballot_w64(v != tag) == 0) break;
    __builtin_amdgcn_s_sleep(NAP);
  }
  __builtin_amdgcn_fence(__ATOMIC_ACQUIRE, "wavefront");
  asm volatile("" ::: "memory");
}
DEV void row_max_to(u32 a, u32 x) {
#define DPPMAX(ctrl) { u32 y = __builtin_amdgcn_update_dpp(0u, x, ctrl, 0xf, 0xf, true); x = x > y ? x : y; }
  DPPMAX(0x111) DPPMAX(0x112) DPPMAX(0x114) DPPMAX(0x118)
  asm volatile("s_mov_b32 exec_lo, 0x80008000\n\ts_mov_b32 exec_hi, 0x80008000\n\tds_max_u32 %0, %1\n\ts_mov_b64 exec, -1" ::"v"(a), "v"(x) : "memory");
}
DEV float max8_nonneg(float v) {
  u32 x = asu(v);
  DPPMAX(0x111) DPPMAX(0x112) DPPMAX(0x114)
#undef DPPMAX
  return asf(__builtin_amdgcn_readlane(x, 7));
}

DEV void widen(float& lo, float& hi, u32 x) {
  asm volatile("v_lshlrev_b32 %0, 16, %2\n\tv_and_b32 %1, 0xffff0000, %2" : "=&v"(lo), "=v"(hi) : "v"(x));
}
DEV float wave_sum(float v) {
#define DPPADD(ctrl) v += asf(__builtin_amdgcn_update_dpp(0u, asu(v), ctrl, 0xf, 0xf, true));
  DPPADD(0x111) DPPADD(0x112) DPPADD(0x114) DPPADD(0x118) DPPADD(0x142) DPPADD(0x143)
  return v;
}
DEV float row_sum(float v) {
  DPPADD(0x128) DPPADD(0x124) DPPADD(0x122) DPPADD(0x121)
#undef DPPADD
  return v;
}
DEV u64 stale(u32x4 f, u64 tag) { return __builtin_amdgcn_ballot_w64(f[1] != (u32)tag) | __builtin_amdgcn_ballot_w64(f[3] != (u32)(tag >> 32)); }
DEV bool fresh(u32x4 f, u64 tag) { return stale(f, tag) == 0; }
template <int N = 1> DEV void await(u32x4* f, u32 voff, u32x4 srd, u32 soff, u64 tag) {
  auto old = [&]() LAM -> bool { u64 o = 0; sfor<0, N>([&](auto i) LAM { o |= stale(f[i.value], tag); }); return o != 0; };
  if (__builtin_expect(old(), 0)) {
    u32 spins = 0;
#pragma nounroll
    do {
      __builtin_amdgcn_s_sleep(1);
      sfor<0, N>([&](auto i) LAM { aload_sc1(f[i.value], voff, srd, soff + i.value * 1024); });
      sfor<0, N>([&](auto i) LAM { vm_on<0>(f[i.value]); });
    } while (old() && ++spins < (1u << 20));
  }
}

#define STAMP(i) do {} while (0)
#define STAMPF(i) do {} while (0)
#define STAMP_END() do {} while (0)

#define STAMPG(i) do {} while (0)
#define ROT1 2
#define PAT 0
#define DPH 10
#define PRIO 1
#define NR 4
#define NR1 1
#define C3E 0
#define PROG 0
#define DW 14
#define PGA 0
#define GA 4
#define PI 1
#define WL 0
#define OWN 0
#define PK0 1
#define CPK 0
#define P9 1
#define C3T 4
#define WOL 14
#define C6E 1
#define WPF 3
#define WPE 1
#define LAG 8
#define FS 0
#define SE 1
#define XP 0
#define B0 1
#define MW 8
#define PBT 0
#define WD 1
#define NAPB 4
#define WE 0
#define HP 4
#define PRB 0
#define HCNT 4
#define UPF 1
#define WS 1
#define WT 2
#define NAPA 1
#define NAPO 1
#define G0 1
#define WA 4
#define TJ1 1
#define TW 1
#define TJ2 5
constexpr int TWM = (TW & 1 ? 4 << TJ1 : 0) | (TW & 2 ? 20 << TJ2 : 0);
#define KD 18
#define ID 60

constexpr int piece_at(int T, int j, int i) {
  constexpr int at[2][2][7] = {{{2, 5, 8, 11, 14, 16, 18}, {6, 11, 16, 19, 5, 14, 18}}, {{1, 3, -1, 5, 7, 9, 11}, {3, 7, -1, 11, 2, 6, 10}}};
  const bool all = j == 0 || (T == 0 && j == 7);
  if (T == 0 && j == 8) return -1;
  if (j == 12) return i >= 6 && i < 14 && i % 2 == 0 ? i / 2 - 3 : -1;
  if (j == 11) return -1;
  for (int p = all || (j & 1) ? 0 : 4; p < (!all && (j & 1) ? 4 : 7); ++p) if (at[T][!all][p] == i) return p;
  return -1;
}
constexpr int slots(int T, int j) { return T == 1 || j > 10 ? 16 : j == 10 ? 36 : 20; }
constexpr int slot_tile(int T, int j, int i) { return T == 1 || j > 10 ? i / 4 + 1 : j < 10 || i < 12 ? i / 4 : i < 28 ? 0 : i / 4 - 4; }
constexpr int slot_step(int T, int j, int i) { return T == 1 || j != 10 || i < 12 || i >= 28 ? j : i < 16 ? 11 : i < 20 ? -1 : 7 + i / 4; }
constexpr int slot_read(int T, int j, int i) {
  const int r = i - 2 * (j & 1);
  if (T == 1) return j < 13 && r >= 0 && r < 12 ? 16 * (j + 1) + (r < 2 ? r + 6 : r < 6 ? r : r + 2) : -1;
  if (j < 10) return r >= 0 && r < 14 ? 16 * (j + 1) + r : -1;
  if (j == 10) return i < 8 ? 16 * 11 + i : i < 20 ? 16 * (12 + (i - 8) / 6) + (i - 8) % 6 : i < 26 ? 16 * 11 + i - 12 : -1;
  return j < 13 && i < 8 ? 16 * (j + 1) + i + 6 : -1;
}
constexpr int wd_blk(int b) { return (int)(0x61087610875432ull >> (4 * b)) & 15; }
constexpr u32 wd_loc(int k) { return wd_blk(k / 2) * 16384 + (k & 1) * 1024; }

constexpr int wd_at(int b) { return b > 8 ? 2 * b - 9 : WD && b > 4 && b < 8 ? WD * (b - 5) : -99; }
constexpr int gemm_wait(int k) {
  int n = 0, last = 0;
  const int end = k == KD ? KD : k - 1;
  for (int i = -DPH; i < end; ++i) {
    if (i + DPH < 28) { n += 2; if (i + DPH == k) last = n; }
    if (i == 7) n += 8;
    for (int b = 5; b < 14; ++b) if (wd_at(b) == i) { n += 4; if (k / 2 == b) last = n; }
  }
  return n - last < 63 ? n - last : 63;
}
static_assert(KD - DPH > 7);
typedef u32 u32x16 __attribute__((ext_vector_type(16)));
DEV u32x4 mk_srd2(u32 lo, u32 hi, u32 bytes) { return (u32x4){lo, hi & 0xffffu, bytes, 0x00020000u}; }

__global__ __launch_bounds__(512, 2) void fused_block(const void*, const void*, const void*, const void*, const void*, const void*,
                                                           void*, char*) {
  __shared__ __attribute__((aligned(16))) char smem[LDS_BYTES];
  u32x16 ka;
  asm volatile("s_load_dwordx16 %0, %1, 0x0" : "=s"(ka) : "s"(__builtin_amdgcn_kernarg_segment_ptr()));
  SBAR();
  const int tid = __builtin_amdgcn_workitem_id_x();
  const int w = __builtin_amdgcn_readfirstlane(tid >> 6);
  const int bid = __builtin_amdgcn_workgroup_id_x();
  const int xcd = bid & 7, bi = bid >> 3;
  u64 pc0;
  u32 clen;
  asm volatile("s_getpc_b64 %0\n.Lfb_pc0:\n\ts_max_u32 %1, .Lfb_enda-.Lfb_pc0, 0\n\ts_max_u32 %1, .Lfb_endb-.Lfb_pc0, %1" : "=s"(pc0), "=s"(clen)::"scc");
  {
    const u32 o = (bi * 16 + w * 2 + (tid & 1)) * 128;
    touch((const char*)pc0 + (o < clen ? o : clen));
  }
  SBAR();
  const int rb = 4 * (xcd & 1) + (bi & 3), quad = xcd >> 1, cl = bi >> 2, cb = 8 * quad + cl;
  const bool t1 = cl == 7;
  const int mg = w & 3, g = w >> 2, un = 7 * quad + cl;
  const u32 rot = 2 * (bi & 3);
  const int rr = (tid >> 3) & 7, ch = tid & 7, odd = (tid >> 5) & 2;
  u32 vx0 = rr * 896 + ((ch ^ (rr >> 1)) << 4), vx1 = (vx0 ^ 64) + 6144;
  u32 ox = (rb * 8 + w) * (32 * 896), lw = w * 1024, so0 = ox + rot * 128;
  asm volatile("s_waitcnt lgkmcnt(0)" : "+s"(ka), "+v"(vx0), "+v"(vx1), "+s"(so0), "+s"(lw));
  STAMPF(3);
  const u32x4 sX = mk_srd2(ka[0], ka[1], S_ * D_ * 2);
  u32x4 xr;
  const u32 row = rb * 256 + cb * 8 + w;
  const u32 vr = (tid & 63) < 56 ? (tid & 63) * 16 : OOB;
  if constexpr (XP == 0) aload<0>(xr, vr, sX, row * 896);
  dma2<0, 0>(sX, vx0, sX, vx1, so0, 4 * lw);
  if constexpr (XP == 1) aload<0>(xr, vr, sX, row * 896);
  dma2<0, 2048>(sX, vx0, sX, vx1, so0 + 12288, 4 * lw);
  if constexpr (XP == 2) aload<0>(xr, vr, sX, row * 896);
  SBAR();
  const u32 wst = t1 ? 2688 : 3584, cw = ch ^ ((rr & 2) << 1);
  const u32 cv = (tid & 15) ^ ((tid >> 2) & 12) ^ odd;
  const u32 va = t1 ? (rr >> 1) * 2688 + (cv >= 14 ? cv - 2 : cv) * 16 : rr * 3584 + (cw ^ odd) * 16;
  const u32 ow = 8 * w * wst + (t1 ? 1792 + 224 * quad : un * 128);
  const u32 vq = ((tid >> 2) & 7) * 2688 + ((tid & 3) ^ odd) * 16;
  const u32 oq = w * (8 * 2688) + 64 * un;
  const u64 pb_ = (t1 ? ((u64)ka[3] << 32 | ka[2]) + 4 * 2688 : ((u64)ka[9] << 32 | ka[8])) - 1024;
  const u32x4 sA = mk_srd2(t1 ? ka[2] : ka[6], t1 ? ka[3] : ka[7], 1u << 21), sB = mk_srd2((u32)pb_, (u32)(pb_ >> 32), 1u << 21);
  const u32x4 sQ = mk_srd2(ka[2], ka[3], D_ * 3 * D_ * 2);
  u32 kst = rot, so = 0;
  auto piece = [&](auto H_, auto I_) LAM {
    constexpr int H = H_.value, i = I_.value;
    if constexpr (i == 0) {
      if constexpr (H > 0) { kst = opaque(kst) + 1; kst = kst == 7 ? 0 : kst; }
      so = ow + kst * (64 * wst);
    }
    if constexpr (i < 2) dma1<(6 + H % 3) * 16384, i * 1024>(i ? sB : sA, va, so, 2 * lw);
    if constexpr (i == 2) dma_half<L_QK + (H % 3) * 4096>(sQ, vq, oq + opaque(kst) * (64 * 2688), lw >> 1);
    if constexpr (i == 3) so = ox + opaque(kst) * 128;
    if constexpr (i == 5) so = opaque(so) + 12288;
    if constexpr (i >= 3) dma1<(H % 3) * 32768, (i - 3) * 1024>(sX, (i & 1) ? vx0 : vx1, so, 4 * lw);
  };
  sfor<0, 3>([&](auto i) LAM { piece(IC<0>{}, i); });
  if (B0) asm volatile("s_barrier" ::: "memory");
  STAMPF(4);
  char* const ws = (char*)((u64)ka[15] << 32 | ka[14]);
  u64 epoch;
  asm volatile("s_load_dwordx2 %0, %1, 0x0" : "=s"(epoch) : "s"(ws));
  asm volatile("" ::"v"((u32)(uintptr_t)(LAS char*)smem));
  const char* const px = (const char*)((u64)ka[1] << 32 | ka[0]);
  const char* const pwo = (const char*)((u64)ka[5] << 32 | ka[4]);
  char* const pout = (char*)((u64)ka[13] << 32 | ka[12]);
  const u32x4 sD = mk_srd2(ka[10], ka[11], F_ * D_ * 2), sWS = mk_srd2(ka[14], ka[15], WS_END);
  const rsrc_t rWS = __builtin_amdgcn_make_buffer_rsrc(ws, 0, WS_END, 0x00020000);
  if (tid < 32) *(LAS u32*)(L_CNT + tid * 4) = 0;
  const bool has_tile = !t1;
  const int rq = bi / 7, rt = 4 * xcd + rq, nt = bi - 7 * rq;
  const int rotg = (4 * nt + 8 * rq) % 28;
  const int head = 2 * xcd + (bi >> 4), qb = bi & 15;
  const u32 vw0 = rr * 896 + cw * 16, vw1 = vw0 + 6144;
  auto krot = [&](u32 k) LAM -> u32 { k += opaque(rotg); return k >= 28 ? k - 28 : k; };
  const u32 od = 448 * mg * 896 + nt * 128;

  asm volatile("s_waitcnt lgkmcnt(0)" : "+s"(epoch));
  u64 tag = (epoch + 0x9E3779B97F4A7C15ull) * 0xBF58476D1CE4E5B9ull;
  tag = (tag ^ (tag >> 29)) + llvm_dispatch_id() * 0x94D049BB133111EBull;
  tag = (tag ^ (tag >> 31)) * 0xD6E8FEB86659FD93ull & 0xBFFFBFFFBFFFBFFFull;
  asm volatile("" : "+s"(tag));
  float rs[4];
  f32x4 acc[5][4];
  auto geglu = [&](auto M) LAM -> u32x4 {
    constexpr int m = M.value;
    const float r2 = mul1(rs[m], rs[m]), K1 = (-2.f * 0.7978845608028654f * 1.4426950408889634f) * rs[m], K2 = K1 * 0.044715f * r2, IR2 = rcp(r2);
    u32x4 hp;
    f32x2 y[4], z[4];
    auto all = [&](auto f) LAM {
      sfor<0, 4>([&](auto i) LAM {
        const f32x4 t = acc[1 + (i.value & 2)][m], u = acc[2 + (i.value & 2)][m];
        constexpr int e = 2 * (i.value & 1);
        f(y[i.value], z[i.value], (f32x2){t[e], t[e + 1]}, (f32x2){u[e], u[e + 1]});
      });
      SBAR();
    };
    auto mul = [&](f32x2 a, f32x2 b) LAM -> f32x2 { if constexpr (m < PK0) return (f32x2){mul1(a[0], b[0]), mul1(a[1], b[1])}; else return a * b; };
    auto fma = [&](f32x2 a, float b, float c) LAM -> f32x2 {
      if constexpr (m < PK0) return (f32x2){fma1(a[0], b, c), fma1(a[1], b, c)}; else return __builtin_elementwise_fma(a, (f32x2){b, b}, (f32x2){c, c});
    };
    SBAR();
    all([&](f32x2& y, f32x2&, f32x2 t, f32x2) LAM { y = mul(t, t); });
    all([&](f32x2& y, f32x2& z, f32x2 t, f32x2 u) LAM { y = fma(y, K2, K1); z = mul(t, u); });
    all([&](f32x2& y, f32x2&, f32x2 t, f32x2) LAM { y = mul(y, t); });
    all([&](f32x2& y, f32x2&, f32x2, f32x2) LAM { y = (f32x2){ex2(y[0]), ex2(y[1])}; });
    all([&](f32x2& y, f32x2&, f32x2, f32x2) LAM { y = fma(y, IR2, IR2); });
    all([&](f32x2& y, f32x2&, f32x2, f32x2) LAM { y = (f32x2){rcp(y[0]), rcp(y[1])}; });
    all([&](f32x2& y, f32x2& z, f32x2, f32x2) LAM { y = mul(y, z); });
    sfor<0, 4>([&](auto j) LAM { hp[j.value] = cvtpk(y[j.value][0], y[j.value][1]); });
    PIN(hp);
    return hp;
  };
  auto frag_off = [&]() LAM -> u32 { const u32 l = lane_id(); return (l >> 4) * 512 + (l & 15) * 16; };
  u32x4 hp[4];

  auto phase1 = [&](auto T_) LAM {
    constexpr int T = T_.value;
    u32 xa[2][2], fa[4], qa, vl_, vcs, bt, vq1, vq2, vv[4];
    const bool isq = quad < 2;
    const u32 ts = isq ? 2048 : 4096;
    {
      const int lane = lane_id(), g4 = lane >> 4, tok = lane & 15, q4 = tok >> 2, p4 = lane & 3;
      xa[0][0] = (mg * 64 + tok) * 128 + ((g4 ^ (tok >> 1)) << 4);
      xa[0][1] = xa[0][0] ^ 64;
      xa[1][0] = xa[0][0] + 65536;
      xa[1][1] = xa[0][1] + 65536;
      sfor<0, 4>([&](auto t) LAM {
        if constexpr (T == 0) fa[t.value] = 6 * 16384 + g4 * 2048 + q4 * 128 + ((64 * g + 16 * (t.value & 2)) ^ ((q4 & 2) << 5) ^ ((g4 & 1) << 5)) + 8 * p4;
        else fa[t.value] = 6 * 16384 + g4 * 2048 + q4 * 256 + ((128 * g + 32 * t.value + 8 * p4) ^ (q4 * 64) ^ ((g4 & 1) << 5));
      });
      qa = L_QK + g4 * 512 + q4 * 64 + ((32 * g + 8 * p4) ^ ((g4 & 1) << 5));
      PIN(xa[0][0]); PIN(xa[0][1]); PIN(xa[1][0]); PIN(xa[1][1]); PIN(fa[0]); PIN(fa[2]);
      if constexpr (T == 1) { PIN(fa[1]); PIN(fa[3]); }
      vl_ = lane * 16;
      bt = tok * 4;
      vcs = mg * 1024 + g4 * 4096 + tok * 16;
      vcs = lane < (T == 0 && mg == 0 ? 32 : 16) ? vcs : OOB;
      PIN(vl_); PIN(bt); PIN(vcs);
      if constexpr (T == 0) {
        const u32 G = 4 * cl + 2 * g + (lane >> 5), p = (G * 37) >> 8, r = G - 7 * p;
        vq2 = (isq ? vl_ & 0x1f0u : (vl_ & 0x30u) | ((vl_ & 0xc0u) << 1) | ((vl_ & 0x100u) >> 2)) + p * (128 * ts);
        vq1 = vq2 + (r >> 2) * (64 * ts) + (r & 3) * 512;
        vq2 = r == 3 ? vq1 + 64 * ts : OOB;
        PIN(qa); PIN(vq1); PIN(vq2);
      }
      if constexpr (T == 1) sfor<0, 4>([&](auto t) LAM {
        const u32 c = 64 * g + 16 * t.value + tok, hd = (c * 147) >> 12;
        vv[t.value] = hd * (64 * 4096 - 28 * 16) + c * 16 + g4 * 512;
        PIN(vv[t.value]);
        acc[4][t.value] = (f32x4){};
        PIN(acc[4][t.value]);
      });
    }
    u32x4 one = {0x3f803f80u, 0x3f803f80u, 0x3f803f80u, 0x3f803f80u};
    PIN(one);
    u32x4 xf[4][4], wf[4][5], sg, cf[5], nm[4];
    f32x4 c0 = {}, c1 = {};
    PIN(c0); PIN(c1);
    const u32 scs = WS_CS + (cb * 2 + g) * 5120, sst = WS_ST + (rb * 256 + mg * 64) * 16;
    f32x4 rv[4];
    float rl, sc[4];
    u32 nl;

    SBAR();
    sfor<0, 7>([&](auto i) LAM { if constexpr (T == 0 || i.value != 2) piece(IC<1>{}, i); });
    {
      vmwait<14 - T - 2 * XP>();
      float a[8], t = 0.f, q = 0.f;
      sfor<0, 4>([&](auto i) LAM {
        widen(a[2 * i.value], a[2 * i.value + 1], xr[i.value]);
        t += a[2 * i.value] + a[2 * i.value + 1];
      });
      const float nmean = asf(__builtin_amdgcn_readlane(asu(wave_sum(t)), 63)) * (-1.f / 448.f);
      sfor<0, 8>([&](auto i) LAM { const float e = a[i.value] + nmean; q = __builtin_fmaf(e, e, q); });
      const float var = wave_sum(vl_ < 896 ? q : 0.f) * (1.f / 448.f);
      const u32 d = cvtpk(nmean, __builtin_amdgcn_sqrtf(bfround(bfround(var) + 1e-5f)));
      st128(rWS, (u32x4){d, (u32)tag, d, (u32)(tag >> 32)}, vl_ == 1008 ? 0 : OOB, WS_ST + row * 16);
      SBAR();
    }
    sync_stage<8 - T>();
    STAMP(1);
    auto rd = [&](auto J, auto R) LAM {
      constexpr int j = J.value, r = R.value, sl = (j / 2) % 3, s = j & 1;
      if constexpr (r >= 2 && r < 6) xf[j & 3][r - 2] = lds128(xa[sl >> 1][s] + (sl & 1) * 32768 + (r - 2) * 2048);
      else {
        constexpr int t = r < 2 ? 0 : (r - 4) / 2, h = r & 1;
        u32x2 v;
        if constexpr (t == 0) v = trd(qa + sl * 4096 + s * 2048 + h * 256);
        else if constexpr (T == 0) v = trd(fa[(t - 1) & 2] + sl * 16384 + s * 8192 + ((t - 1) & 1) * 1024 + h * 512);
        else v = trd(fa[t - 1] + sl * 16384 + s * 8192 + h * 1024);
        wf[j & 3][t][2 * h] = v[0];
        wf[j & 3][t][2 * h + 1] = v[1];
      }
    };
    sfor<2 * T, 14>([&](auto r) LAM { rd(IC<0>{}, r); });
    SBAR();
    SBAR();
    u32 P[4], nx, sq, last, tsrc, tdst;
    auto qk_put = [&](auto M) LAM {
      constexpr int m = M.value;
      f32x4& a = acc[0][m];
      scale4(a, sc[m]);
      P[2 * (m & 1)] = cvtpk(a[0], a[1]);
      P[2 * (m & 1) + 1] = cvtpk(a[2], a[3]);
      if constexpr (m & 1) {
        swap16(P[0], P[2]);
        swap16(P[1], P[3]);
        st128(rWS, (u32x4){P[0], P[1], vq2 == OOB ? P[2] : 0u, vq2 == OOB ? P[3] : 0u}, vq1, sq + (m >> 1) * ts);
        st128(rWS, (u32x4){P[2], P[3], 0u, 0u}, vq2, sq + (m >> 1) * ts);
      } else { PIN(P[0]); PIN(P[1]); }
    };
    auto norm = [&](auto I) LAM {
      constexpr int i = I.value;
      if constexpr (i < 4) {
        const f32x4 a = acc[0][i];
        const u32 v = asu(sumsq4(a));
        nx = i == 0 || v > nx ? v : nx;
        PIN(nx);
      } else row_max_to(L_CNT + 64 + 16 * g + (vl_ >> 6 & 12), nx);
    };
    auto tell = [&]() LAM {
      lgkm0_on(last);
      last = __builtin_amdgcn_readfirstlane(last);
      if (last == 7) {
        if constexpr (T == 0) {
          const u32 k2 = asu(row_sum(*(LAS float*)tsrc));
          st128(rWS, (u32x4){k2, (u32)tag, k2, (u32)(tag >> 32)}, tdst, WS_FQ + ((quad & 1) * 8 + cl) * (16 * BOX) + (isq ? 2 * rb * BOX + 24 * 16 : rb * 16));
        } else st128(rWS, (u32x4){0u, (u32)tag, 0u, (u32)(tag >> 32)}, vl_ * (BOX / 16), WS_FQ + quad * (64 * BOX) + (8 + rb) * 16);
      }
      STAMP(13);
    };
    auto v_put = [&](auto I) LAM {
      constexpr int t = 1 + I.value / 2, u = I.value & 1;
      u32x4 V;
      sfor<0, 4>([&](auto e) LAM {
        constexpr int m = 2 * u + e.value / 2, c = 2 * (e.value & 1);
        V[e.value] = cvtpk(mul1(acc[t][m][c], rv[m][c]), mul1(acc[t][m][c + 1], rv[m][c + 1]));
      });
      st128(rWS, V, vv[t - 1], WS_KV + (quad * 256 + rb * 8 + mg * 2 + u) * 4096 + 2048);
    };
    sfor<0, 14>([&](auto J) LAM {
      constexpr int j = J.value, G = j / 2;
      sfor<0, slots(T, j)>([&](auto I) LAM {
        constexpr int i = I.value, t = slot_tile(T, j, i), js = slot_step(T, j, i), m = i % 4;
        if constexpr (T == 1 && t == 4) {
          if constexpr (m == 0) mfma4_unless(acc[4], xf[j & 3], wf[j & 3][4], g);
        } else if constexpr (T == 1) mac16<j == 0>(acc[t][m], xf[j & 3][m], wf[j & 3][t]);
        else if constexpr (js < 0) mac16<false>(acc[0][m], cf[0], nm[m]);
        else mac16<j == 0>(acc[t][m], wf[js & 3][t], xf[js & 3][m]);
        SBAR();
        if constexpr ((j & 1) && i == 1 && j < 11 + 2 * T) sync_stage<(T ? (G == 5 ? 1 : 7 - (G & 1)) : G == 1 ? 7 : 8) + (TWM >> j & 1)>();
        if constexpr (T == 0 && j == 9 && i == 2) STAMPG(5);
        if constexpr (T == 0 && j == 10 && i == 8) { STAMPG(6); sync_stage<4, 2>(); __builtin_amdgcn_s_setprio(3); STAMPG(7); }
        if constexpr (T == 0 && j == 10 && i == 28) STAMPG(8);
        if constexpr (T == 0 && j == 11 && i == 0) STAMPG(9);
        if constexpr (T == 0 && j == 13 && i == 0) STAMPG(13);
        if constexpr ((j & 1) && i == 1 && j < 13) STAMPF(5 + G);
        if constexpr (i == 1 && (j == TJ2 || j == TJ2 + 2) && (TW & 2)) touch_b(sD, vl_ < 25 * 16 ? vl_ * 8 : OOB, ((bi * 8 + w) * 2 + (j - TJ2) / 2) * (25 * 128));
        if constexpr (i == 1 && j == TJ1 && (TW & 1)) touch_b(sD, vl_ < 7 * 16 ? vl_ * 8 : OOB, (bid * 8 + w) * (7 * 128));
        constexpr int r = slot_read(T, j, i);
        if constexpr (r >= 0) rd(IC<(r >= 0 ? r / 16 : 0)>{}, IC<(r >= 0 ? r % 16 : 0)>{});
        if constexpr (j < 4 && i == 3) colsum<T>(c0, c1, one, wf[j & 3], (0xf0700e0fu >> (8 * j + rb)) & 1 ? mg : 4);
        if constexpr (j == 3 && i == 8) {
          PIN(c0); PIN(c1);
          const float a0 = c0[0], a1 = c1[0], v = vl_ < 256 ? a0 : a1, hi = bfround(v);
          __builtin_amdgcn_raw_buffer_store_b64((u32x2){cvtpk(hi, v - hi), (u32)(tag >> (32 * (rb & 1)))}, rWS, vcs, scs + (rb >> 1) * 256 + (rb & 1) * 8, 16);
        }
        constexpr int pc = piece_at(T, j, i), H = (j + 5) / 2;
        if constexpr (pc >= 0 && H < 7) piece(IC<H>{}, IC<(pc >= 0 ? pc : 0)>{});
        if constexpr (T == 0 && (j == 9 || j == 12) && pc >= 0) {
          if constexpr (pc == 0) so = od + krot((j & 4) + 2 * g) * 14336;
          if constexpr (pc == 2) so = opaque(so) + 12288;
          dma1<(j == 9 ? 2 : 4) * 16384, pc * 1024>(sD, (pc & 1) ? vw1 : vw0, so, 4 * lw);
        }
        if constexpr (T == 0 && j == 8 && i == 4) {
          const u32 l = bt >> 2, p = vl_ >> 8;
          tsrc = l < 8 && p == (l + cl >= 7) ? L_CNT + 64 + bt : L_CNT + 124;
          PIN(tsrc);
        }
        if constexpr (T == 0 && j == 8 && i == 6) {
          const u32 p = vl_ >> 8, sd = isq ? 16 : 256;
          tdst = bt * (BOX / 4) + (p == 0 ? 0u : p == 1 ? 16 * BOX + sd : cl == 0 ? sd : 16 * BOX);
          tdst = bt < (isq ? 8 : 64) && p < (cl == 0 || cl == 6 ? 3 : 2) ? tdst : OOB;
          PIN(tdst);
        }
        constexpr int jn = 9 + 2 * T;
        if constexpr (j == jn - 2 && i == 0) aload_sc1(sg, vl_, sWS, sst);
        if constexpr (T == 0 && j == 8 && i == 0) aload_sc1(cf[0], vl_, sWS, scs + 4096);
        if constexpr (j == 9 && i == 1) dma_sc1(sWS, vl_, scs + mg * 1024, 7 * 16384 + lw);
        if constexpr (T == 1 && j == 9 && i == 8) st128(rWS, one, (vl_ >> 8) * (64 * 4096) + (vl_ & 0xc0) * 8 + (vl_ & 48) + 448, WS_KV + (quad * 256 + rb * 8 + w) * 4096 + 2048);
        if constexpr (j == 11 && i == 8 - 8 * T) { vcs = 7 * 16384 + g * 4096 + vl_; PIN(vcs); }
        if constexpr (T == 0 && j == 11 && i >= 9 && i < 13) cf[i - 8] = lds128(vcs + (i - 9) * 1024);
        if constexpr (T == 1 && j == 11 && i == 1) sfor<1, 5>([&](auto u) LAM { cf[u.value] = lds128(vcs + (u.value - 1) * 1024); });
        if constexpr (j == jn - 1 && i >= 10 && i < 14) { nm[i - 10] = (u32x4){}; PIN(nm[i - 10]); }
        if constexpr (j == jn && i == 3) {
          if constexpr (T == 1 || P9) __builtin_amdgcn_s_setprio(3);
          PIN(sg);
          await(&sg, vl_, sWS, sst, tag);
        }
        if constexpr (T == 0 && j == 10 && i == 10) { PIN(cf[0]); await(cf, vl_, sWS, scs + 4096, tag); }
        if constexpr (T == 0 ? j == 12 && i == 14 : j == 11 && i == 6) {
          sfor<1, 5>([&](auto u) LAM { PIN(cf[u.value]); });
          await<4>(cf + 1, vl_, sWS, scs, tag);
        }
        if constexpr (j == jn && i == 8) {
          rl = rcp(asf(sg[0] & 0xffff0000u));
          nl = __builtin_amdgcn_perm(sg[0], sg[0], 0x01000100u);
          if constexpr (T == 1) *(LAS float*)(L_RV + mg * 256 + (vl_ >> 2)) = rl;
        }
        if constexpr (j == jn && i >= 12 && i < 16) {
          nm[i - 12][0] = __builtin_amdgcn_ds_bpermute(bt + 64 * (i - 12), nl);
          PIN(bt);
          nm[i - 12][2] = __builtin_amdgcn_ds_bpermute(bt + 64 * (i - 12), nl);
        }
        if constexpr (T == 0 && j == 10 && i < 4) PIN(nm[i]);
        if constexpr (T == 0 && j == 9 && i >= 16) rs[i - 16] = asf(__builtin_amdgcn_ds_bpermute(bt + 64 * (i - 16), asu(rl)));
        if constexpr (T == 0 && j == 10 && i == 24) sq = opaque((isq ? WS_Q : WS_KV) + ((quad & 1) * 512 + rb * 8 + mg * 2) * ts);
        if constexpr (T == 0 && j == 10 && i == 26) sfor<0, 4>([&](auto e) LAM {
          sc[e.value] = rs[e.value] * (isq ? 0.18898223650461362f * 1.4426950408889634f : 1.f);
          PIN(sc[e.value]);
        });
        if constexpr (T == 0 && j == 10 && i >= 29 && i < 33) qk_put(IC<(i >= 29 && i < 33 ? i - 29 : 0)>{});
        if constexpr (T == 0 && j == 10 && i == 33) { __builtin_amdgcn_s_setprio(2); STAMPF(11); }
        if constexpr (T == 0 && j == 11 && i >= 2 && i < 7) norm(IC<(i >= 2 && i < 7 ? i - 2 : 0)>{});
        if constexpr (j == 13 - T) {
          constexpr int tc = (i + 8) % 16 / 4 + 1;
          SBAR();
          if constexpr (T == 1) mac16<false>(acc[tc][m], nm[m], cf[tc]);
          else mac16<false>(acc[tc][m], cf[tc], nm[m]);
          SBAR();
          if constexpr (T == 1 && i == 10) { vcs = L_RV + mg * 256 + ((vl_ >> 4) & 48); PIN(vcs); }
          if constexpr (T == 1 && i >= 10 && i < 14) rv[i - 10] = lds128f(vcs + 64 * (i - 10));
        }
        if constexpr (T == 0 && j > 10 && 16 * (j - 11) + i == DW) { STAMPG(10); vmwait<(DW < 22 ? 0 : DW < 29 ? (DW - 20) / 2 : 4)>(); STAMPG(11); last = cnt_take(0); }
        if constexpr (T == 0 && j > 10 && 16 * (j - 11) + i == DW + 2) { tell(); __builtin_amdgcn_s_setprio(1); }
        if constexpr (T == 0 && j == 13 && i == 0) cnt_add(24);
        if constexpr (T == 1 && j == 13 && i >= 5 && (i & 1)) v_put(IC<(i >= 5 ? (i - 5) / 2 : 0)>{});
        SBAR();
      });
    });
    asm volatile("s_nop 7");
    SBAR();
    STAMP(2);
    if constexpr (T == 1) {
      if (g == 0) { v_put(IC<6>{}); v_put(IC<7>{}); }
      vmwait<0>();
      last = cnt_take(0);
      tell();
    }
    STAMP(3);
    __builtin_amdgcn_s_setprio(0);
    if (PGA && g == 0) __builtin_amdgcn_s_setprio(PGA);
    SBAR();
    if constexpr (T == 0) sfor<0, PI>([&](auto m) LAM { hp[m.value] = geglu(m); PIN(hp[m.value]); });
  };
  if (!t1) phase1(IC<0>{}); else phase1(IC<1>{});
  LANES();
  auto wload = [&](auto B) LAM {
    const u32 so = od + krot(2 * B.value) * 14336;
    dma2<wd_loc(2 * B.value), 0>(sD, vw0, sD, vw1, so, mg * 4096);
    dma2<wd_loc(2 * B.value), 2048>(sD, vw0, sD, vw1, so + 12288, mg * 4096);
  };
  const u32x4 sO = mk_srd(pwo, D_ * D_ * 2);
  if (w < 4) {
    if (PRIO) __builtin_amdgcn_s_setprio(2);
    u32x4 fk;
    const u32 vfo = (lane < 26 ? lane : 0) * 16, sfo = WS_FQ + (head * 16 + qb) * BOX;
    u32 seen = 0;
    auto inbox = [&](u32 blocks) LAM {
      u32 need = blocks << (qb >> 1);
      need = ((need | need >> 8) & 255) * 0x10101 + 0x3000000;
#pragma nounroll
      for (u32 spins = 0; (seen & need) != need && spins < (1u << 20); ++spins) {
        if (spins) __builtin_amdgcn_s_sleep(NAPA);
        aload_sc1(fk, vfo, sWS, sfo);
        vm_on<0>(fk);
        seen = ~(u32)stale(fk, tag);
      }
    };
    auto hand = [&](auto M0) LAM {
      sfor<M0.value, 4>([&](auto m) LAM { hp[m.value] = geglu(m); PIN(hp[m.value]); });
      cnt_wait(24, 8);
      sfor<0, 4>([&](auto m) LAM { *(LAS u32x4*)(6 * 16384 + w * 4096 + m.value * 1024 + vl) = hp[m.value]; });
      cnt_add(12 + w);
    };
    if (has_tile) {
      aload_sc1(fk, vfo, sWS, sfo);
      if constexpr (GA == 4) hand(IC<PI>{});
      else sfor<PI, GA>([&](auto m) LAM { hp[m.value] = geglu(m); PIN(hp[m.value]); });
      STAMP(3);
      vm_on<0>(fk);
      seen = ~(u32)stale(fk, tag);
    }
    STAMP(4);
    inbox((1u << NR1) - 1);
    STAMP(5);
    const u32 kvb = WS_KV + (head * 64 + 2 * w) * 4096;
    u32 kvc = 0;
    auto kv_next = [&](int kt) LAM {
      if (kt & 1) kvc = opaque(kvc) ^ 4096;
      else kvc = kvb + (qb & 1) * 4096 + ((opaque(qb >> 1) + (kt >> 1)) & 7) * 32768;
    };
    u32x4 Q[4][2], KV[4][4];
    auto q_load = [&](auto t) LAM {
      const u32 so = WS_Q + (head * 64 + qb * 4 + ((w + 1 + t.value) & 3)) * 2048;
      Q[t.value][0] = ld128(rWS, vl, so);
      Q[t.value][1] = ld128(rWS, vl + 1024, so);
    };
    kv_next(0);
    KV[0][0] = ld128(rWS, vl, kvc);
    KV[0][1] = ld128(rWS, vl + 1024, kvc);
    q_load(IC<0>{});
    q_load(IC<1>{});
    KV[0][2] = ld128(rWS, vl + 2048, kvc);
    q_load(IC<2>{});
    KV[0][3] = ld128(rWS, vl + 3072, kvc);
    q_load(IC<3>{});
    SBAR();
    sfor<1, UPF>([&](auto b) LAM {
      kv_next(b.value);
      sfor<0, 4>([&](auto i) LAM { KV[b.value][i.value] = ld128(rWS, vl + i.value * 1024, kvc); });
    });
    SBAR();
    if (C3E) cnt_add(3);
    if constexpr (GA < 4) { if (has_tile) hand(IC<GA>{}); }
    inbox((1u << NR) - 1);
    if (!C3E && C3T < 0) cnt_add(3);
    STAMP(6);
    const int idx = 0xDDDD;
    f32x16 O[4] = {};
    const f32x16 Z = {};
    auto rgn = [&](int t) LAM -> u32 { return 2 * 16384u + t * 12288u; };
    auto put_part = [&](auto t, auto i) LAM {
      const f32x16& o = O[t.value];
      *(LAS f32x4*)(rgn((w + 1 + t.value) & 3) + t.value * 4096 + i.value * 1024 + vl) =
          (f32x4){o[4 * i.value], o[4 * i.value + 1], o[4 * i.value + 2], o[4 * i.value + 3]};
    };
    f32x16 Sx[4];
    u32 cb1 = 0;
    constexpr int CK = 8 * NR - 12, CP = CK - (NR > 3 ? 10 : 8);
    auto tile = [&](auto I) LAM {
      constexpr int kt = I.value >> 2, b = kt & 3, t = I.value & 3, tn = (t + 1) & 3, tp = (t + 3) & 3;
      constexpr int bn = t == 3 ? (b + 1) & 3 : b, bp = t == 0 ? (b + 3) & 3 : b;
      constexpr int E1 = PAT ? 3 : 4, E2 = 2 * E1, E3 = 3 * E1;
      auto exps = [&](auto lo, auto hi) LAM {
        SBAR();
        sfor<lo.value, hi.value>([&](auto e) LAM { Sx[t][e.value] = ex2(Sx[t][e.value]); });
        if constexpr (hi.value == E3) USE(Sx[t]); else PIN(Sx[t]);
        SBAR();
      };
      if constexpr (t == 3 && I.value > MW && I.value < 63) USE(KV[bn][3]);
      if constexpr (I.value < 63) Sx[tn] = mfma32(KV[bn][0], Q[tn][0], Z);
      exps(IC<0>{}, IC<E1>{});
      if constexpr (I.value < 63) Sx[tn] = mfma32(KV[bn][1], Q[tn][1], Sx[tn]);
      if constexpr (I.value < 63) PIN(Sx[tn]);
      exps(IC<E1>{}, IC<E2>{});
      if constexpr (I.value > 0) O[tp] = smf_hi(KV[bp][3], Sx[tp], O[tp], idx);
      PIN(O[tp]);
      exps(IC<E2>{}, IC<E3>{});
      O[t] = smf_lo(KV[b][2], Sx[t], O[t], idx);
      PIN(O[t]);
      exps(IC<E3>{}, IC<E3 + 2>{});
      if constexpr (I.value == CP) aload_sc1(fk, vfo, sWS, sfo);
      constexpr int NX = 4 * (3 - UPF);
      sfor<(I.value < NX ? 4 * UPF + 2 * I.value : I.value + 12), (I.value < NX ? 4 * UPF + 2 * I.value + 2 : I.value + 13)>([&](auto L) LAM {
        if constexpr (L.value < 64) {
          if constexpr ((L.value & 3) == 0) kv_next(L.value >> 2);
          KV[(L.value >> 2) & 3][L.value & 3] = ld128(rWS, vl + (L.value & 3) * 1024, kvc);
        }
      });
      if constexpr (I.value == C3T) cnt_add(3);
      if constexpr (I.value == ID) cnt_add(9);
      if constexpr (I.value == 58) cb1 = cnt_get(1);
      if constexpr (I.value == 60) { if (has_tile && __builtin_amdgcn_readfirstlane(cb1) < 4u) cnt_wait(1, 4); cfence(); }
      if constexpr (I.value >= 62) sfor<0, 4>([&](auto i) LAM { put_part(IC<I.value - 62>{}, i); });
      exps(IC<E3 + 2>{}, IC<16>{});
    };
    Sx[0] = mfma32(KV[0][1], Q[0][1], mfma32(KV[0][0], Q[0][0], Z));
    SBAR();
    sfor<0, CK>(tile);
    vm_on<CK - CP>(fk);
    await(&fk, vfo, sWS, sfo, tag);
    float bound = asf(__builtin_amdgcn_readlane(fk[0], 24)) + asf(__builtin_amdgcn_readlane(fk[0], 25));
    {
      u32 ka_ = fk[0], kb_ = fk[2];
      swap16(ka_, kb_);
      bound *= max8_nonneg(asf(ka_) + asf(kb_));
    }
    PIN(bound);
    sfor<CK, 64>(tile);
    O[3] = smf_hi(KV[3][3], Sx[3], O[3], idx);
    if (__builtin_amdgcn_ballot_w64(!(bound <= 4000.f)) != 0) {
      sfor<0, 4>([&](auto t) LAM { O[t.value] = Z; });
      auto kvo = [&](int kt) LAM -> u32 { return kvb + (kt >> 1) * 32768 + (kt & 1) * 4096; };
      float mx[4] = {-3.0e38f, -3.0e38f, -3.0e38f, -3.0e38f};
      for (int kt = 0; kt < 16; ++kt) {
        const u32x4 k0 = ld128(rWS, vl, kvo(kt)), k1 = ld128(rWS, vl + 1024, kvo(kt));
        sfor<0, 4>([&](auto t) LAM {
          const f32x16 s = mfma32(k1, Q[t.value][1], mfma32(k0, Q[t.value][0], Z));
          sfor<0, 16>([&](auto i) LAM { mx[t.value] = __builtin_fmaxf(mx[t.value], s[i.value]); });
        });
      }
      sfor<0, 4>([&](auto t) LAM {
        mx[t.value] = __builtin_fmaxf(mx[t.value], other_half(mx[t.value]));
        *(LAS float*)(L_VT + ((((w + 1 + t.value) & 3) * 32 + r32) * 4 + w) * 4) = mx[t.value];
      });
      cnt_add(8);
      cnt_wait(8, 4);
      sfor<0, 4>([&](auto t) LAM {
        const f32x4 m4 = lds128f(L_VT + (((w + 1 + t.value) & 3) * 32 + r32) * 16);
        mx[t.value] = __builtin_fmaxf(__builtin_fmaxf(m4[0], m4[1]), __builtin_fmaxf(m4[2], m4[3]));
      });
      for (int kt = 0; kt < 16; ++kt) {
        const u32x4 k0 = ld128(rWS, vl, kvo(kt)), k1 = ld128(rWS, vl + 1024, kvo(kt));
        const u32x4 v0 = ld128(rWS, vl + 2048, kvo(kt)), v1 = ld128(rWS, vl + 3072, kvo(kt));
        sfor<0, 4>([&](auto t) LAM {
          f32x16 s = mfma32(k1, Q[t.value][1], mfma32(k0, Q[t.value][0], Z));
          sfor<0, 16>([&](auto i) LAM { s[i.value] = ex2(s[i.value] - mx[t.value]); });
          O[t.value] = smf_hi(v1, s, smf_lo(v0, s, O[t.value], idx), idx);
        });
      }
      sfor<0, 2>([&](auto t) LAM { sfor<0, 4>([&](auto i) LAM { put_part(t, i); }); });
    }
    STAMP(7);
    sfor<0, 4>([&](auto i) LAM { put_part(IC<2>{}, i); });
    cnt_add(2);
    const u32 red = rgn(w);
    {
      LANES();
      float o[16];
      f32x4 pt[12];
      read_when<12>(pt, 2, 4, 2, 4, [&](int i) LAM -> u32 { return red + i * 1024 + vl; });
      sfor<0, 4>([&](auto i) LAM {
        const f32x4 a0 = pt[i.value], a1 = pt[4 + i.value], a2 = pt[8 + i.value];
        sfor<0, 4>([&](auto j) LAM {
          if constexpr (CPK) o[4 * i.value + j.value] = (a0[j.value] + a1[j.value]) + (a2[j.value] + O[3][4 * i.value + j.value]);
          else o[4 * i.value + j.value] = add1(add1(a0[j.value], a1[j.value]), add1(a2[j.value], O[3][4 * i.value + j.value]));
        });
      });
      const float lo_ = other_half(o[12]);
      const float il = rcp(hf ? o[12] : lo_);
      u32 P[8];
      sfor<0, 8>([&](auto i) LAM { P[i.value] = CPK ? cvtpk(o[2 * i.value] * il, o[2 * i.value + 1] * il) : cvtpk(mul1(o[2 * i.value], il), mul1(o[2 * i.value + 1], il)); });
      const int om = qb * 4 + w;
      st128(rWS, (u32x4){P[0], P[1], P[2], P[3]}, vl, WS_O + (om * 16 + head) * 2048);
      st128(rWS, (u32x4){P[4], P[5], hf ? 0u : P[6], hf ? 0u : P[7]}, vl + 1024, WS_O + (om * 16 + head) * 2048);
      sfor<0, 7>([&](auto e) LAM { dma_i<e.value * 1024, 8 * e.value * 896>(sO, vw0, (56 * w + 112 * (w / 2 + 1)) * 896 + nt * 128, red); });
      vmwait<7>();
      if (lane == 0) put_flag(ws, WS_FO + (om * 16 + head) * FSO, tag);
      STAMP(8);
      if (!has_tile) { vmwait<0>(); STAMP_END(); return; }
    }
    {
    LANES();
    const u32 trow = (4 * hf + q4) * 128, swz = (q4 & 2) << 5;
    const int m = w & 1, kh = w >> 1;
    const rsrc_t rX = __builtin_amdgcn_make_buffer_rsrc((void*)px, 0, S_ * D_ * 2, 0x00020000);
    const u32 xo = ((rt * 64 + m * 32 + r32) * 448 + nt * 64 + kh * 32 + hf * 8) * 2;
    u32x4 xr[2];
    xr[0] = __builtin_amdgcn_raw_buffer_load_b128(rX, xo, 0, 0);
    xr[1] = __builtin_amdgcn_raw_buffer_load_b128(rX, xo + 32, 0, 0);
    const u32 fo = WS_FO + ((2 * rt + m) * 16 + 8 * kh + (lane & 7)) * FSO;
    u64 f0 = 0;
    if constexpr (FS) aflag(f0, ws + fo);
    vm_on1<0>(f0);
    PIN(xr[0]); PIN(xr[1]);
    cnt_add(6);
    u32x4 of[16], wf[16][2];
    u32 wa[2][2], wp[2][2];
    sfor<0, 4>([&](auto i) LAM {
      constexpr int r = i.value >> 1, n = i.value & 1;
      wa[r][n] = (r ? rgn(2 * kh) : kh ? L_QK : 5 * 16384) + trow + ((64 * (n ^ kh) + 32 * blk + 8 * p4) ^ swz);
      wp[r][n] = wa[r][n] - hf * 512;
    });
    auto wo_rd = [&](auto K) LAM {
      constexpr int k = K.value, r = k >> 3, hh = (k >> 1) & 3, s = k & 1;
      constexpr u32 off = (28 * hh + 16 * s) * 128 + (r && hh > 1 ? 12288 - 7168 : 0);
      sfor<0, 2>([&](auto n) LAM { wf[k][n.value] = trd2(wa[r][n.value] + off, (s ? wp[r][n.value] : wa[r][n.value]) + off + 1024); });
    };
    u32 xs[8] = {xr[0][0], xr[0][1], xr[0][2], xr[0][3], xr[1][0], xr[1][1], xr[1][2], xr[1][3]};
    swap32(xs[0], xs[2]); swap32(xs[1], xs[3]); swap32(xs[4], xs[6]); swap32(xs[5], xs[7]);
    sfor<0, 8>([&](auto i) LAM { PIN(xs[i.value]); });
    u32 so_ = opaque(WS_O + ((2 * rt + m) * 16 + 8 * kh) * 2048), vl2 = vl;
    PIN(vl2);
    if constexpr (WPE) {
      cnt_wait(6, 8);
      sfor<0, WPF>(wo_rd);
      sfor<0, 4>([&](auto i) LAM { PIN(wa[i.value >> 1][i.value & 1]); PIN(wp[i.value >> 1][i.value & 1]); });
      sfor<0, 2 * WPF>([&](auto i) LAM { PIN(wf[i.value >> 1][i.value & 1]); });
    }
    if (!FS || __builtin_amdgcn_ballot_w64(f0 != tag) != 0) poll_flags<NAPO>(ws, fo, tag);
    STAMP(9);
    sfor<0, 16>([&](auto k) LAM { of[k.value] = ld128(rWS, vl2 + (k.value & 3) * 1024, so_ + (k.value >> 2) * 4096); });
    SBAR();
    if constexpr (!WPE) { cnt_wait(6, 8); sfor<0, WPF>(wo_rd); }
    SBAR();
    auto xch = [&](int v, int i) LAM -> u32 { return rgn(v) + 8192 + i * 1024; };
    f32x16 acc[2] = {};
    sfor<0, 16>([&](auto k) LAM {
      acc[1] = mfma32(wf[k.value][1], of[k.value], acc[1]);
      SBAR();
      if constexpr (k.value + WPF < 16) wo_rd(IC<k.value + WPF>{});
      if constexpr (k.value >= LAG) acc[0] = mfma32(wf[k.value - LAG][0], of[k.value - LAG], acc[0]);
      SBAR();
    });
    acc[0] = mfma32(wf[16 - LAG][0], of[16 - LAG], acc[0]);
    PIN(acc[0]);
    SBAR();
    sfor<0, 4>([&](auto i) LAM {
      *(LAS f32x4*)(xch(w, i.value) + vl) =
          (f32x4){acc[1][4 * i.value], acc[1][4 * i.value + 1], acc[1][4 * i.value + 2], acc[1][4 * i.value + 3]};
    });
    STAMP(10);
    cnt_add(6 + m);
    sfor<17 - LAG, 16>([&](auto k) LAM { acc[0] = mfma32(wf[k.value][0], of[k.value], acc[0]); });
    PIN(acc[0]);
    f32x4 gt[8];
    read_when<8>(gt, 5, 4, 6 + m, 10 - 8 * m, [&](int i) LAM -> u32 { return (i < 4 ? 16384 + w * 4096 + i * 1024 : xch(w ^ 2, i - 4)) + vl; });
    u32 R[8];
    sfor<0, 4>([&](auto i) LAM {
      const f32x4 a = gt[4 + i.value];
      float y[4];
      sfor<0, 4>([&](auto j) LAM {
        const u32 xd = xs[2 * i.value + (j.value >> 1)];
        const float xv = asf((j.value & 1) ? (xd & 0xffff0000u) : (xd << 16));
        y[j.value] = __builtin_fmaf(acc[0][4 * i.value + j.value] + a[j.value], gt[i.value][j.value], xv);
      });
      R[2 * i.value] = cvtpk(y[0], y[1]);
      R[2 * i.value + 1] = cvtpk(y[2], y[3]);
    });
    swap32(R[0], R[2]); swap32(R[1], R[3]); swap32(R[4], R[6]); swap32(R[5], R[7]);
    const rsrc_t rOut = __builtin_amdgcn_make_buffer_rsrc(pout, 0, S_ * D_ * 2, 0x00020000);
    st128p(rOut, (u32x4){R[0], R[1], R[2], R[3]}, xo);
    st128p(rOut, (u32x4){R[4], R[5], R[6], R[7]}, xo + 32);
    asm volatile(".Lfb_enda:");
    STAMP(11);
    STAMP_END();
    }
  } else {
    const u32 trow = (4 * hf + q4) * 128, swz = (q4 & 2) << 5;
    const int gw = w - 4;
    const u32 lg = gw * 4096;
    if (has_tile) {
      u32x4 ha[4];
      sfor<PI, 4>([&](auto m) LAM { hp[m.value] = geglu(m); PIN(hp[m.value]); });
      STAMP(4);
      cnt_wait(12 + gw, 1);
      sfor<0, 4>([&](auto m) LAM { ha[m.value] = lds128(6 * 16384 + lg + m.value * 1024 + vl); PIN(ha[m.value]); });
      if constexpr (WE == 2) { wload(IC<4>{}); wload(IC<8>{}); }
      const u32 fo = frag_off(), ho = WS_H + ((rb * 8 + gw * 2) * 112 + un * 4) * 1024;
      cnt_wait(3, HCNT);
      if constexpr (WE == 1) { wload(IC<4>{}); wload(IC<8>{}); }
      STAMP(9);
      sfor<0, 8>([&](auto i) LAM {
        constexpr int m = i.value >> 1;
        st128(rWS, (i.value & 1) ? hp[m] : ha[m], fo + (m & 1) * 256 + (i.value & 1) * 2048, ho + (m >> 1) * (112 * 1024));
        if constexpr (i.value < 7) __builtin_amdgcn_s_sleep(HP);
      });
      if constexpr (WE == 3) { wload(IC<4>{}); wload(IC<8>{}); vmwait<8>(); }
      else vmwait<0>();
      if (lane == 0) put_flag(ws, WS_FH + ((rb * 4 + gw) * 32 + un) * FSH, tag);
      STAMP(5);
      if constexpr (WE == 0) { wload(IC<4>{}); wload(IC<8>{}); }
      if constexpr (WD == 0) sfor<5, 8>(wload);
      touch(pwo + ((rq & 3) * 112 + gw * 28 + (lane < 28 ? lane : 0)) * 896 + nt * 128);
      STAMP(8);
    }
    if (bid == 255 && gw == 0) {
      for (u32 i = 0; i < 4; ++i) {
        u32x4 f = {0, 0, 0, (u32)~(tag >> 32)};
        await(&f, lane * 128, sWS, WS_ST + i * 8192, tag);
      }
      if (lane == 0) put_flag(ws, WS_EPOCH, epoch + 1);
    }
    if (!has_tile) { STAMP_END(); return; }
    const u32x4 sH = mk_srd(ws + WS_H, 64 * 112 * 1024);
    u32 vh0 = ((2 * rt + (gw & 1)) * 112 + 28 * gw) * 1024 + vl, vh1 = ((2 * rt + 1 - (gw & 1)) * 112 + 28 * gw) * 1024 + vl, oh = 0;
    PIN(vh0); PIN(vh1);
    u32x4 hfr[DPH][2];
    auto hload = [&](auto K) LAM {
      if constexpr (K.value % 4 == 0) oh = krot(K.value) * 1024;
      aload_sc1<K.value % 4 * 1024>(hfr[K.value % DPH][0], vh0, sH, oh);
      aload_sc1<K.value % 4 * 1024>(hfr[K.value % DPH][1], vh1, sH, oh);
    };
    cnt_wait(3, 4);
    poll_flags<NAPB>(ws, WS_FH + (rt * 32 + 7 * gw + (lane < 7 ? lane : 0)) * FSH, tag);
    STAMP(6);
    sfor<0, DPH>([&](auto K) LAM { hload(K); });
    __builtin_amdgcn_s_setprio(PRB);
    u32 wa[2];
    sfor<0, 2>([&](auto n) LAM { wa[n.value] = lg + trow + ((64 * (n.value ^ (gw >> 1)) + 32 * blk + 8 * p4) ^ swz); });
    f32x16 acc[2][2] = {};
    u32x4 wf[2][2];
    auto ready = [&](auto K, auto N) LAM {
      constexpr int k = K.value;
      constexpr int o = wd_loc(k), d = (o & ~65535) - (k ? (int)(wd_loc(k - 1) & ~65535) : 0);
      vm_on2<N.value>(hfr[k % DPH][0], hfr[k % DPH][1]);
      sfor<0, 2>([&](auto n) LAM {
        if constexpr (d != 0) { wa[n.value] += d; PIN(wa[n.value]); }
        wf[k & 1][n.value] = trd2(wa[n.value] + (o & 65535), wa[n.value] + (o & 65535) + 2048);
      });
    };
    ready(IC<0>{}, IC<2 * (DPH - 1)>{});
    sfor<0, 28>([&](auto K) LAM {
      constexpr int k = K.value;
      if constexpr (k == KD) cnt_wait(9, 4);
      if constexpr (PBT && k == PBT) __builtin_amdgcn_s_setprio(3);
      if constexpr (k + 1 < 28 && k != KD - 1) ready(IC<k + 1>{}, IC<gemm_wait(k + 1)>{});
      sfor<0, 2>([&](auto n) LAM {
        sfor<0, 2>([&](auto m) LAM { acc[m.value][n.value] = mfma32(wf[k & 1][n.value], hfr[k % DPH][m.value], acc[m.value][n.value]); PIN(acc[m.value][n.value]); });
      });
      SBAR();
      if constexpr (k == 7) cnt_add(1);
      if constexpr (k + DPH < 28) hload(IC<k + DPH>{});
      sfor<5, 14>([&](auto B) LAM { if constexpr (wd_at(B.value) == k) wload(B); });
      if constexpr (k == 7) sfor<0, 8>([&](auto e) LAM {
        const u32 p = e.value & 2 && gw == 3 ? 10 : 4 * gw;
        dma_i<(e.value & 3) * 1024 + (e.value < 4 ? 5 * 16384 : L_QK), ((e.value & 3) * 8 + (e.value < 4 ? 0 : 224)) * 896>(sO, vw0, p * (8 * 896) + nt * 128, p * 1024);
      });
      if constexpr (k == KD - 1) { ready(IC<KD>{}, IC<gemm_wait(KD)>{}); cnt_add(6); }
      SBAR();
    });
    STAMP(7);
    sfor<1, 4>([&](auto x) LAM {
      const f32x16& a = acc[x.value & 1][x.value >> 1];
      sfor<0, 4>([&](auto i) LAM {
        *(LAS f32x4*)((5 + x.value) * 16384 + lg + i.value * 1024 + vl) = (f32x4){a[4 * i.value], a[4 * i.value + 1], a[4 * i.value + 2], a[4 * i.value + 3]};
      });
    });
    cnt_add(4);
    f32x4 pt[12];
    read_when<12>(pt, 4, 4, 4, 4, [&](int i) LAM -> u32 { return (6 + i / 4) * 16384 + (gw ^ (1 + i / 4)) * 4096 + (i & 3) * 1024 + vl; });
    sfor<0, 4>([&](auto i) LAM {
      const u32 ra = i.value * 1024 + vl;
      const f32x4 a1 = pt[i.value], a2 = pt[4 + i.value], a3_ = pt[8 + i.value];
      f32x4 gv;
      sfor<0, 4>([&](auto j) LAM {
        if constexpr (BPK) {
          const float f = (acc[0][0][4 * i.value + j.value] + a1[j.value]) + (a2[j.value] + a3_[j.value]);
          gv[j.value] = f * rcp(1.f + ex2(f * -1.4426950408889634f));
        } else {
          const float f = add1(add1(acc[0][0][4 * i.value + j.value], a1[j.value]), add1(a2[j.value], a3_[j.value]));
          gv[j.value] = mul1(f, rcp(add1(1.f, ex2(mul1(f, -1.4426950408889634f)))));
        }
      });
      *(LAS f32x4*)(1 * 16384 + lg + ra) = gv;
    });
    asm volatile(".Lfb_endb:");
    cnt_add(5);
    STAMP(10);
    STAMP_END();
  }
}

extern "C" void kernel_launch(void* const* d_in, const int* in_sizes, int n_in, void* d_out, int out_size, void* d_ws,
                              size_t ws_size, hipStream_t stream) {
  fused_block<<<256, 512, 0, stream>>>(d_in[0], d_in[1], d_in[2], d_in[4], d_in[5], d_in[6], d_out, (char*)d_ws);
}
```

```cpp
#include <hip/hip_runtime.h>
#include <stdint.h>
#pragma clang diagnostic ignored "-Wint-to-pointer-cast"

#define DEV __device__ __forceinline__
#define LAM __attribute__((always_inline))
#define LAS __attribute__((address_space(3)))
#define GAS __attribute__((address_space(1)))

#define STAMPS 0

typedef unsigned u32;
typedef unsigned long long u64;
typedef u32 u32x2 __attribute__((ext_vector_type(2)));
typedef u32 u32x4 __attribute__((ext_vector_type(4)));
typedef short s16x4 __attribute__((ext_vector_type(4)));
typedef float f32x2 __attribute__((ext_vector_type(2)));
typedef float f32x4 __attribute__((ext_vector_type(4)));
typedef float f32x8 __attribute__((ext_vector_type(8)));
typedef float f32x16 __attribute__((ext_vector_type(16)));
typedef __bf16 bf16x8 __attribute__((ext_vector_type(8)));
typedef __bf16 bf16x16 __attribute__((ext_vector_type(16)));

extern "C" __device__ u64 llvm_dispatch_id() __asm("llvm.amdgcn.dispatch.id");

constexpr int S_ = 2048, D_ = 448, F_ = 1792;
#define V3 1
#define V4 1
#define V5 1
#define RWS 0
#define BPK 1
#define FSQ 16
#define FSH 8
#define FSO 8
constexpr u32 WS_EPOCH = 0, WS_ST = 32768, WS_DBG = 65536;
constexpr u32 WS_Q = 1u << 20, WS_KV = 3u << 20, WS_O = 7u << 20, WS_H = 9u << 20;
constexpr u32 WS_CS = 16u << 20, WS_FO = WS_CS + (512u << 10), WS_FH = WS_FO + (256u << 10), WS_FQ = WS_FH + (256u << 10), WS_END = 18u << 20;
constexpr u32 BOX = 512;
constexpr u32 L_QK = 147456, L_CS = 159744, L_CNT = L_CS + 2560, L_DUM = L_CS + 2816, L_RV = L_CS + 3072;
constexpr u32 OOB = 0x7ffffff0;
constexpr u32 L_VT = 5 * 16384 + 14336;
constexpr u32 LDS_BYTES = 163840;

template <int V> struct IC { static constexpr int value = V; };
template <int I, int N, class Fn> DEV void sfor(Fn&& f) {
  if constexpr (I < N) { f(IC<I>{}); sfor<I + 1, N>(f); }
}

#define SBAR() __builtin_amdgcn_sched_barrier(0)
template <class T> DEV void PIN(T& x) { asm volatile("" : "+v"(x)); }
template <class T> DEV void USE(const T& x) { asm volatile("" ::"v"(x)); }
template <class T> DEV void undef(T& x) { asm volatile("" : "=v"(x)); }
template <class T> DEV void nop_on(T& x) { asm volatile("s_nop 7" : "+v"(x)); }
template <class T> DEV void lgkm0_on(T& x) { asm volatile("s_waitcnt lgkmcnt(0)" : "+v"(x)::"memory"); }
template <int T> DEV void colsum(f32x4& c0, f32x4& c1, u32x4 one, const u32x4* w, int who) {
#define CSM(k, c, r) "s_cmp_lg_u32 %8, " #k "\n\ts_cbranch_scc1 .Lcs" #r "_%=\n\tv_mfma_f32_16x16x32_bf16 %" #c ", %2, %" #r ", %" #c "\n.Lcs" #r "_%=:\n\t"
  if constexpr (T == 0)
    asm volatile(CSM(0, 0, 3) CSM(1, 0, 4) CSM(2, 0, 5) CSM(3, 0, 6) CSM(0, 1, 7)
                 : "+v"(c0), "+v"(c1) : "v"(one), "v"(w[1]), "v"(w[2]), "v"(w[3]), "v"(w[4]), "v"(w[0]), "s"(who) : "scc");
  else
    asm volatile(CSM(0, 0, 3) CSM(1, 0, 4) CSM(2, 0, 5) CSM(3, 0, 6)
                 : "+v"(c0), "+v"(c1) : "v"(one), "v"(w[1]), "v"(w[2]), "v"(w[3]), "v"(w[4]), "v"(one), "s"(who) : "scc");
#undef CSM
}
DEV void mfma4_unless(f32x4* c, const u32x4* a, u32x4 b, int skip) {
  asm volatile("s_cmp_lg_u32 %9, 0\n\ts_cbranch_scc1 .Lm4_%=\n\tv_mfma_f32_16x16x32_bf16 %0, %4, %8, %0\n\tv_mfma_f32_16x16x32_bf16 %1, %5, %8, %1\n\t"
               "v_mfma_f32_16x16x32_bf16 %2, %6, %8, %2\n\tv_mfma_f32_16x16x32_bf16 %3, %7, %8, %3\n.Lm4_%=:"
               : "+v"(c[0]), "+v"(c[1]), "+v"(c[2]), "+v"(c[3]) : "v"(a[0]), "v"(a[1]), "v"(a[2]), "v"(a[3]), "v"(b), "s"(skip) : "scc");
}
template <int OFF> DEV void aload(u32x4& d, u32 voff, u32x4 srd, u32 soff) {
  asm volatile("buffer_load_dwordx4 %0, %1, %2, %3 offen offset:%4" : "=v"(d) : "v"(voff), "s"(srd), "s"(soff), "i"(OFF) : "memory");
}
template <int N> DEV void vm_on(u32x4& a) { asm volatile("s_waitcnt vmcnt(%1)" : "+v"(a) : "i"(N) : "memory"); }
DEV void aload_sc1(u32x4& d, u32 voff, u32x4 srd, u32 soff) {
  asm volatile("buffer_load_dwordx4 %0, %1, %2, %3 offen sc1" : "=v"(d) : "v"(voff), "s"(srd), "s"(soff) : "memory");
}
template <int B> DEV void load_if(u32x4* d, u32 voff, u32x4 srd, u32 soff, u32 mask) {
  u32 s2 = soff + 4096;
#define LIF(h, a, b, s, o) "s_bitcmp0_b32 %12, %13+" #h "\n\ts_cbranch_scc1 .Lli" #h "_%=\n\tbuffer_load_dwordx4 %" #a ", %8, %9, " s " offen offset:" #o " sc1\n\t" \
  "buffer_load_dwordx4 %" #b ", %8, %9, " s " offen offset:" #o "+1024 sc1\n.Lli" #h "_%=:\n\t"
  asm volatile(LIF(0, 0, 1, "%10", 0) LIF(1, 2, 3, "%10", 2048) LIF(2, 4, 5, "%11", 0) LIF(3, 6, 7, "%11", 2048)
               : "+v"(d[0]), "+v"(d[1]), "+v"(d[2]), "+v"(d[3]), "+v"(d[4]), "+v"(d[5]), "+v"(d[6]), "+v"(d[7])
               : "v"(voff), "s"(srd), "s"(soff), "s"(s2), "s"(mask), "i"(B) : "memory", "scc");
#undef LIF
}
template <int N> DEV void vm_on2(u32x4& a, u32x4& b) { asm volatile("s_waitcnt vmcnt(%2)" : "+v"(a), "+v"(b) : "i"(N) : "memory"); }

DEV f32x16 mfma32(u32x4 a, u32x4 b, f32x16 c) {
  return __builtin_amdgcn_mfma_f32_32x32x16_bf16(__builtin_bit_cast(bf16x8, a), __builtin_bit_cast(bf16x8, b), c, 0, 0, 0);
}
template <bool FIRST> DEV void mac16(f32x4& c, u32x4 a, u32x4 b) {
  if constexpr (FIRST) asm volatile("v_mfma_f32_16x16x32_bf16 %0, %1, %2, 0" : "=&v"(c) : "v"(a), "v"(b));
  else asm volatile("v_mfma_f32_16x16x32_bf16 %0, %1, %2, %0" : "+v"(c) : "v"(a), "v"(b));
}
DEV f32x16 smf_lo(u32x4 a, f32x16 p, f32x16 c, int idx) {
  f32x8 b = __builtin_shufflevector(p, p, 0, 1, 2, 3, 4, 5, 6, 7);
  return __builtin_amdgcn_smfmac_f32_32x32x32_bf16(__builtin_bit_cast(bf16x8, a), __builtin_bit_cast(bf16x16, b), c, idx, 0, 0);
}
DEV f32x16 smf_hi(u32x4 a, f32x16 p, f32x16 c, int idx) {
  f32x8 b = __builtin_shufflevector(p, p, 8, 9, 10, 11, 12, 13, 14, 15);
  return __builtin_amdgcn_smfmac_f32_32x32x32_bf16(__builtin_bit_cast(bf16x8, a), __builtin_bit_cast(bf16x16, b), c, idx, 0, 0);
}
typedef __bf16 bf16x2 __attribute__((ext_vector_type(2)));
DEV u32 cvtpk(float lo, float hi) {
  f32x2 v = {lo, hi};
  return __builtin_bit_cast(u32, __builtin_convertvector(v, bf16x2));
}
DEV float asf(u32 u) { return __builtin_bit_cast(float, u); }
DEV u32 asu(float f) { return __builtin_bit_cast(u32, f); }
DEV float mul1(float a, float b) { float r; asm("v_mul_f32 %0, %1, %2" : "=v"(r) : "v"(a), "v"(b)); return r; }
DEV float add1(float a, float b) { float r; asm("v_add_f32 %0, %1, %2" : "=v"(r) : "v"(a), "v"(b)); return r; }
DEV float fma1(float a, float b, float c) { float r; asm("v_fma_f32 %0, %1, %2, %3" : "=v"(r) : "v"(a), "v"(b), "v"(c)); return r; }
DEV void scale4(f32x4& a, float s) {
  float x = a[0], y = a[1], z = a[2], w = a[3];
  asm("v_mul_f32 %0, %0, %4\n\tv_mul_f32 %1, %1, %4\n\tv_mul_f32 %2, %2, %4\n\tv_mul_f32 %3, %3, %4" : "+v"(x), "+v"(y), "+v"(z), "+v"(w) : "v"(s));
  a = (f32x4){x, y, z, w};
}
DEV float sumsq4(f32x4 a) {
  float r, t;
  asm("v_mul_f32 %0, %2, %2\n\tv_mul_f32 %1, %3, %3\n\tv_fmac_f32 %0, %4, %4\n\tv_fmac_f32 %1, %5, %5\n\tv_add_f32 %0, %0, %1"
      : "=&v"(r), "=&v"(t) : "v"(a[0]), "v"(a[1]), "v"(a[2]), "v"(a[3]));
  return r;
}
DEV float bfround(float f) { return asf(cvtpk(f, f) << 16); }
DEV float ex2(float x) { return __builtin_amdgcn_exp2f(x); }
DEV float rcp(float x) { return __builtin_amdgcn_rcpf(x); }
DEV float other_half(float v) {
  auto r = __builtin_amdgcn_permlane32_swap(asu(v), asu(v), false, false);
  return asf(r[0] ^ r[1] ^ asu(v));
}
DEV u32x4 mk_srd(const void* p, u32 bytes) {
  u64 a = (u64)p;
  u32x4 r = {(u32)a, (u32)(a >> 32) & 0xffffu, bytes, 0x00020000u};
  return r;
}
template <int LD> DEV void dma_half(u32x4 srd, u32 voff, u32 so, u32 l0) {
#define EXH "0"
  asm volatile("s_add_u32 m0, %0, %1\n\ts_mov_b32 exec_hi, " EXH "\n\tbuffer_load_dwordx4 %2, %3, %4 offen lds\n\ts_mov_b32 exec_hi, -1"
               ::"s"(l0), "i"(LD), "v"(voff), "s"(srd), "s"(so) : "memory", "scc");
}
template <int LD, int OFF> DEV void dma1(u32x4 srd, u32 voff, u32 so, u32 l0) {
  asm volatile("s_add_u32 m0, %0, %1\n\ts_nop 0\n\tbuffer_load_dwordx4 %2, %3, %4 offen offset:%5 lds"
               ::"s"(l0), "i"(LD), "v"(voff), "s"(srd), "s"(so), "i"(OFF) : "memory", "scc");
}
template <int LD, int OFF> DEV void dma2(u32x4 s0, u32 v0, u32x4 s1, u32 v1, u32 so, u32 l0) {
  asm volatile("s_add_u32 m0, %0, %1\n\ts_nop 0\n\tbuffer_load_dwordx4 %2, %3, %6 offen offset:%7 lds\n\tbuffer_load_dwordx4 %4, %5, %6 offen offset:%8 lds"
               ::"s"(l0), "i"(LD), "v"(v0), "s"(s0), "v"(v1), "s"(s1), "s"(so), "i"(OFF), "i"(OFF + 1024) : "memory", "scc");
}
DEV void dma_sc1(u32x4 srd, u32 voff, u32 so, u32 l0) {
  asm volatile("s_mov_b32 m0, %0\n\ts_nop 0\n\tbuffer_load_dwordx4 %1, %2, %3 offen sc1 lds" ::"s"(l0), "v"(voff), "s"(srd), "s"(so) : "memory");
}
DEV u32 opaque(u32 x) { asm volatile("" : "+s"(x)); return x; }
template <int LD, int ADD> DEV void dma_i(u32x4 srd, u32 voff, u32 s0, u32 l0) {
  u32 t;
  asm volatile("s_add_u32 m0, %1, %2\n\ts_add_u32 %0, %3, %4\n\tbuffer_load_dwordx4 %5, %6, %0 offen lds"
               : "=&s"(t) : "s"(l0), "i"(LD), "s"(s0), "i"(ADD), "v"(voff), "s"(srd) : "memory", "scc");
}
DEV int lane_id() {
  u32 z = 0;
  asm volatile("" : "+s"(z));
  return __builtin_amdgcn_mbcnt_hi(~0u, __builtin_amdgcn_mbcnt_lo(~0u, z));
}
#define LANES() const int lane = lane_id(), r32 = lane & 31, hf = lane >> 5, blk = (lane >> 4) & 1, q4 = (lane & 15) >> 2, p4 = lane & 3; \
  const u32 vl = lane * 16; (void)r32; (void)hf; (void)blk; (void)q4; (void)p4; (void)vl
DEV void touch(const void* p) {
  asm volatile("s_mov_b32 m0, %0\n\ts_nop 0\n\tglobal_load_lds_dword %1, off" ::"i"(L_DUM), "v"(p) : "memory");
}
DEV void touch_b(u32x4 srd, u32 voff, u32 so) {
  asm volatile("s_mov_b32 m0, %0\n\ts_nop 0\n\tbuffer_load_dword %1, %2, %3 offen lds" ::"i"(L_DUM), "v"(voff), "s"(srd), "s"(so) : "memory");
}
template <int N> DEV void vmwait() { asm volatile("s_waitcnt vmcnt(%0)" ::"i"(N) : "memory"); }
DEV void cfence() { asm volatile("" ::: "memory"); }
DEV void cnt_add(int i) {
  asm volatile("s_mov_b64 exec, 1\n\tds_inc_u32 %0, %0\n\ts_mov_b64 exec, -1" ::"v"(L_CNT + 4 * i) : "memory");
}
DEV u32 cnt_take(int i) {
  u32 r;
  asm volatile("s_mov_b64 exec, 1\n\tds_inc_rtn_u32 %0, %1, %1\n\ts_mov_b64 exec, -1" : "=v"(r) : "v"(L_CNT + 4 * i) : "memory");
  return r;
}
DEV u32 cnt_get(int i) { return __hip_atomic_load((LAS u32*)(L_CNT + 4 * i), __ATOMIC_RELAXED, __HIP_MEMORY_SCOPE_WORKGROUP); }
DEV void cnt_wait(int i, u32 n) {
  while (__builtin_amdgcn_readfirstlane(cnt_get(i)) < n) __builtin_amdgcn_s_sleep(1);
  cfence();
}
DEV u32x2 trd(u32 a) { return __builtin_bit_cast(u32x2, __builtin_amdgcn_ds_read_tr16_b64_v4i16((LAS s16x4*)a)); }
DEV u32x4 trd2(u32 a0, u32 a1) { u32x2 x = trd(a0), y = trd(a1); u32x4 r = {x[0], x[1], y[0], y[1]}; return r; }
DEV u32x4 lds128(u32 a) { return *(LAS u32x4*)a; }
DEV f32x4 lds128f(u32 a) { return *(LAS f32x4*)a; }
template <int N, class Fn> DEV void read_when(f32x4* d, int i, u32 n, int i2, u32 n2, Fn&& at) {
  for (;;) {
    const u32 c = cnt_get(i), c2 = i2 != i ? cnt_get(i2) : n2;
    cfence();
    sfor<0, N>([&](auto k) LAM { d[k.value] = lds128f(at(k.value)); });
    sfor<0, N>([&](auto k) LAM { PIN(d[k.value]); });
    if (__builtin_amdgcn_readfirstlane(c) >= n && __builtin_amdgcn_readfirstlane(c2) >= n2) break;
    if (RWS) __builtin_amdgcn_s_sleep(RWS);
  }
}
template <int N, int L = 0> DEV void sync_stage() { asm volatile("s_waitcnt vmcnt(%0) lgkmcnt(%1)\n\ts_barrier" ::"i"(N), "i"(L) : "memory"); }
template <int I> DEV void wlane(u32& f, u32 v) { asm volatile("v_writelane_b32 %0, %1, %2" : "+v"(f) : "s"(v), "i"(I)); }
typedef __amdgpu_buffer_rsrc_t rsrc_t;
DEV void st128(rsrc_t r, u32x4 v, u32 voff, u32 soff) {
  __builtin_amdgcn_raw_buffer_store_b128(v, r, voff, __builtin_amdgcn_readfirstlane(soff), 16);
  asm volatile("s_nop 1" ::"v"(v) : "memory");
}
DEV void st128p(rsrc_t r, u32x4 v, u32 voff) {
  __builtin_amdgcn_raw_buffer_store_b128(v, r, voff, 0, 16);
}
DEV void aflag(u64& d, const char* p) { asm volatile("global_load_dwordx2 %0, %1, off sc1" : "=v"(d) : "v"(p) : "memory"); }
template <int N> DEV void vm_on1(u64& a) { asm volatile("s_waitcnt vmcnt(%1)" : "+v"(a) : "i"(N) : "memory"); }
DEV void swap32(u32& a, u32& b) {
  auto r = __builtin_amdgcn_permlane32_swap(a, b, false, false);
  a = r[0]; b = r[1];
}
DEV void swap16(u32& a, u32& b) {
  auto r = __builtin_amdgcn_permlane16_swap(a, b, false, false);
  a = r[0]; b = r[1];
}
DEV u32 from_lane(int l, u32 v) { return __builtin_amdgcn_ds_bpermute(l * 4, v); }
DEV u32x4 ld128(rsrc_t r, u32 voff, u32 soff) { return __builtin_amdgcn_raw_buffer_load_b128(r, voff, __builtin_amdgcn_readfirstlane(soff), 16); }
DEV void put_flag(char* ws, u32 off, u64 tag) { __hip_atomic_store((GAS u64*)(ws + off), tag, __ATOMIC_RELAXED, __HIP_MEMORY_SCOPE_AGENT); }
template <int NAP> DEV void poll_flags(char* ws, u32 off, u64 tag) {
#pragma nounroll
  for (u32 spins = 0; spins < (1u << 20); ++spins) {
    u64 v = __hip_atomic_load((GAS u64*)(ws + off), __ATOMIC_RELAXED, __HIP_MEMORY_SCOPE_AGENT);
    if (__builtin_amdgcn_ballot_w64(v != tag) == 0) break;
    __builtin_amdgcn_s_sleep(NAP);
  }
  __builtin_amdgcn_fence(__ATOMIC_ACQUIRE, "wavefront");
  asm volatile("" ::: "memory");
}
DEV void row_max_to(u32 a, u32 x) {
#define DPPMAX(ctrl) { u32 y = __builtin_amdgcn_update_dpp(0u, x, ctrl, 0xf, 0xf, true); x = x > y ? x : y; }
  DPPMAX(0x111) DPPMAX(0x112) DPPMAX(0x114) DPPMAX(0x118)
  asm volatile("s_mov_b32 exec_lo, 0x80008000\n\ts_mov_b32 exec_hi, 0x80008000\n\tds_max_u32 %0, %1\n\ts_mov_b64 exec, -1" ::"v"(a), "v"(x) : "memory");
}
DEV float max8_nonneg(float v) {
  u32 x = asu(v);
  DPPMAX(0x111) DPPMAX(0x112) DPPMAX(0x114)
#undef DPPMAX
  return asf(__builtin_amdgcn_readlane(x, 7));
}

DEV void widen(float& lo, float& hi, u32 x) {
  asm volatile("v_lshlrev_b32 %0, 16, %2\n\tv_and_b32 %1, 0xffff0000, %2" : "=&v"(lo), "=v"(hi) : "v"(x));
}
DEV float wave_sum(float v) {
#define DPPADD(ctrl) v += asf(__builtin_amdgcn_update_dpp(0u, asu(v), ctrl, 0xf, 0xf, true));
  DPPADD(0x111) DPPADD(0x112) DPPADD(0x114) DPPADD(0x118) DPPADD(0x142) DPPADD(0x143)
  return v;
}
DEV float row_sum(float v) {
  DPPADD(0x128) DPPADD(0x124) DPPADD(0x122) DPPADD(0x121)
#undef DPPADD
  return v;
}
DEV u64 stale(u32x4 f, u64 tag) { return __builtin_amdgcn_ballot_w64(f[1] != (u32)tag) | __builtin_amdgcn_ballot_w64(f[3] != (u32)(tag >> 32)); }
DEV bool fresh(u32x4 f, u64 tag) { return stale(f, tag) == 0; }
template <int N = 1> DEV void await(u32x4* f, u32 voff, u32x4 srd, u32 soff, u64 tag) {
  auto old = [&]() LAM -> bool { u64 o = 0; sfor<0, N>([&](auto i) LAM { o |= stale(f[i.value], tag); }); return o != 0; };
  if (__builtin_expect(old(), 0)) {
    u32 spins = 0;
#pragma nounroll
    do {
      __builtin_amdgcn_s_sleep(1);
      sfor<0, N>([&](auto i) LAM { aload_sc1(f[i.value], voff, srd, soff + i.value * 1024); });
      sfor<0, N>([&](auto i) LAM { vm_on<0>(f[i.value]); });
    } while (old() && ++spins < (1u << 20));
  }
}

#define STAMP(i) do {} while (0)
#define STAMPF(i) do {} while (0)
#define STAMP_END() do {} while (0)

#define STAMPG(i) do {} while (0)
#define ROT1 2
#define PAT 0
#define DPH 10
#define PRIO 1
#define NR 4
#define NR1 1
#define C3E 0
#define PROG 0
#define DW 14
#define PGA 0
#define GA 4
#define PI 1
#define WL 0
#define OWN 0
#define PK0 1
#define CPK 0
#define P9 1
#define C3T -1
#define WOL 14
#define C6E 1
#define WPF 3
#define WPE 1
#define LAG 8
#define FS 0
#define SE 1
#define XP 0
#define B0 1
#define NAPB 4
#define WE 0
#define HP 4
#define PRB 0
#define HCNT 4
#define UPF 2
#define WS 1
#define WT 2
#define NAPA 1
#define NAPO 1
#define G0 1
#define WA 4
#define TJ1 1
#define TW 1
#define TJ2 5
constexpr int TWM = (TW & 1 ? 4 << TJ1 : 0) | (TW & 2 ? 20 << TJ2 : 0);
#define KD 18
#define ID 60

constexpr int piece_at(int T, int j, int i) {
  constexpr int at[2][2][7] = {{{2, 5, 8, 11, 14, 16, 18}, {6, 11, 16, 19, 5, 14, 18}}, {{1, 3, -1, 5, 7, 9, 11}, {3, 7, -1, 11, 2, 6, 10}}};
  const bool all = j == 0 || (T == 0 && j == 7);
  if (T == 0 && j == 8) return -1;
  if (j == 12) return i >= 6 && i < 14 && i % 2 == 0 ? i / 2 - 3 : -1;
  if (j == 11) return -1;
  for (int p = all || (j & 1) ? 0 : 4; p < (!all && (j & 1) ? 4 : 7); ++p) if (at[T][!all][p] == i) return p;
  return -1;
}
constexpr int slots(int T, int j) { return T == 1 || j > 10 ? 16 : j == 10 ? 36 : 20; }
constexpr int slot_tile(int T, int j, int i) { return T == 1 || j > 10 ? i / 4 + 1 : j < 10 || i < 12 ? i / 4 : i < 28 ? 0 : i / 4 - 4; }
constexpr int slot_step(int T, int j, int i) { return T == 1 || j != 10 || i < 12 || i >= 28 ? j : i < 16 ? 11 : i < 20 ? -1 : 7 + i / 4; }
constexpr int slot_read(int T, int j, int i) {
  const int r = i - 2 * (j & 1);
  if (T == 1) return j < 13 && r >= 0 && r < 12 ? 16 * (j + 1) + (r < 2 ? r + 6 : r < 6 ? r : r + 2) : -1;
  if (j < 10) return r >= 0 && r < 14 ? 16 * (j + 1) + r : -1;
  if (j == 10) return i < 8 ? 16 * 11 + i : i < 20 ? 16 * (12 + (i - 8) / 6) + (i - 8) % 6 : i < 26 ? 16 * 11 + i - 12 : -1;
  return j < 13 && i < 8 ? 16 * (j + 1) + i + 6 : -1;
}
constexpr int wd_blk(int b) { return (int)(0x61087610875432ull >> (4 * b)) & 15; }
constexpr u32 wd_loc(int k) { return wd_blk(k / 2) * 16384 + (k & 1) * 1024; }

constexpr int wd_at(int b) { return 2 * b - 9; }
constexpr int gemm_wait(int k) {
  int n = 0, last = 0;
  const int end = k == KD ? KD : k - 1;
  for (int i = -DPH; i < end; ++i) {
    if (i + DPH < 28) { n += 2; if (i + DPH == k) last = n; }
    if (i == 7) n += 8;
    for (int b = 9; b < 14; ++b) if (wd_at(b) == i) { n += 4; if (k / 2 == b) last = n; }
  }
  return n - last < 63 ? n - last : 63;
}
static_assert(KD - DPH > 7);
typedef u32 u32x16 __attribute__((ext_vector_type(16)));
DEV u32x4 mk_srd2(u32 lo, u32 hi, u32 bytes) { return (u32x4){lo, hi & 0xffffu, bytes, 0x00020000u}; }

__global__ __launch_bounds__(512, 2) void fused_block(const void*, const void*, const void*, const void*, const void*, const void*,
                                                           void*, char*) {
  __shared__ __attribute__((aligned(16))) char smem[LDS_BYTES];
  u32x16 ka;
  asm volatile("s_load_dwordx16 %0, %1, 0x0" : "=s"(ka) : "s"(__builtin_amdgcn_kernarg_segment_ptr()));
  SBAR();
  const int tid = __builtin_amdgcn_workitem_id_x();
  const int w = __builtin_amdgcn_readfirstlane(tid >> 6);
  const int bid = __builtin_amdgcn_workgroup_id_x();
  const int xcd = bid & 7, bi = bid >> 3;
  u64 pc0;
  u32 clen;
  asm volatile("s_getpc_b64 %0\n.Lfb_pc0:\n\ts_max_u32 %1, .Lfb_enda-.Lfb_pc0, 0\n\ts_max_u32 %1, .Lfb_endb-.Lfb_pc0, %1" : "=s"(pc0), "=s"(clen)::"scc");
  {
    const u32 o = (bi * 16 + w * 2 + (tid & 1)) * 128;
    touch((const char*)pc0 + (o < clen ? o : clen));
  }
  SBAR();
  const int rb = 4 * (xcd & 1) + (bi & 3), quad = xcd >> 1, cl = bi >> 2, cb = 8 * quad + cl;
  const bool t1 = cl == 7;
  const int mg = w & 3, g = w >> 2, un = 7 * quad + cl;
  const u32 rot = 2 * (bi & 3);
  const int rr = (tid >> 3) & 7, ch = tid & 7, odd = (tid >> 5) & 2;
  u32 vx0 = rr * 896 + ((ch ^ (rr >> 1)) << 4), vx1 = (vx0 ^ 64) + 6144;
  u32 ox = (rb * 8 + w) * (32 * 896), lw = w * 1024, so0 = ox + rot * 128;
  asm volatile("s_waitcnt lgkmcnt(0)" : "+s"(ka), "+v"(vx0), "+v"(vx1), "+s"(so0), "+s"(lw));
  STAMPF(3);
  const u32x4 sX = mk_srd2(ka[0], ka[1], S_ * D_ * 2);
  u32x4 xr;
  const u32 row = rb * 256 + cb * 8 + w;
  const u32 vr = (tid & 63) < 56 ? (tid & 63) * 16 : OOB;
  if constexpr (XP == 0) aload<0>(xr, vr, sX, row * 896);
  dma2<0, 0>(sX, vx0, sX, vx1, so0, 4 * lw);
  if constexpr (XP == 1) aload<0>(xr, vr, sX, row * 896);
  dma2<0, 2048>(sX, vx0, sX, vx1, so0 + 12288, 4 * lw);
  if constexpr (XP == 2) aload<0>(xr, vr, sX, row * 896);
  SBAR();
  const u32 wst = t1 ? 2688 : 3584, cw = ch ^ ((rr & 2) << 1);
  const u32 cv = (tid & 15) ^ ((tid >> 2) & 12) ^ odd;
  const u32 va = t1 ? (rr >> 1) * 2688 + (cv >= 14 ? cv - 2 : cv) * 16 : rr * 3584 + (cw ^ odd) * 16;
  const u32 ow = 8 * w * wst + (t1 ? 1792 + 224 * quad : un * 128);
  const u32 vq = ((tid >> 2) & 7) * 2688 + ((tid & 3) ^ odd) * 16;
  const u32 oq = w * (8 * 2688) + 64 * un;
  const u64 pb_ = (t1 ? ((u64)ka[3] << 32 | ka[2]) + 4 * 2688 : ((u64)ka[9] << 32 | ka[8])) - 1024;
  const u32x4 sA = mk_srd2(t1 ? ka[2] : ka[6], t1 ? ka[3] : ka[7], 1u << 21), sB = mk_srd2((u32)pb_, (u32)(pb_ >> 32), 1u << 21);
  const u32x4 sQ = mk_srd2(ka[2], ka[3], D_ * 3 * D_ * 2);
  u32 kst = rot, so = 0;
  auto piece = [&](auto H_, auto I_) LAM {
    constexpr int H = H_.value, i = I_.value;
    if constexpr (i == 0) {
      if constexpr (H > 0) { kst = opaque(kst) + 1; kst = kst == 7 ? 0 : kst; }
      so = ow + kst * (64 * wst);
    }
    if constexpr (i < 2) dma1<(6 + H % 3) * 16384, i * 1024>(i ? sB : sA, va, so, 2 * lw);
    if constexpr (i == 2) dma_half<L_QK + (H % 3) * 4096>(sQ, vq, oq + opaque(kst) * (64 * 2688), lw >> 1);
    if constexpr (i == 3) so = ox + opaque(kst) * 128;
    if constexpr (i == 5) so = opaque(so) + 12288;
    if constexpr (i >= 3) dma1<(H % 3) * 32768, (i - 3) * 1024>(sX, (i & 1) ? vx0 : vx1, so, 4 * lw);
  };
  sfor<0, 3>([&](auto i) LAM { piece(IC<0>{}, i); });
  if (B0) asm volatile("s_barrier" ::: "memory");
  STAMPF(4);
  char* const ws = (char*)((u64)ka[15] << 32 | ka[14]);
  u64 epoch;
  asm volatile("s_load_dwordx2 %0, %1, 0x0" : "=s"(epoch) : "s"(ws));
  asm volatile("" ::"v"((u32)(uintptr_t)(LAS char*)smem));
  const char* const px = (const char*)((u64)ka[1] << 32 | ka[0]);
  const char* const pwo = (const char*)((u64)ka[5] << 32 | ka[4]);
  char* const pout = (char*)((u64)ka[13] << 32 | ka[12]);
  const u32x4 sD = mk_srd2(ka[10], ka[11], F_ * D_ * 2), sWS = mk_srd2(ka[14], ka[15], WS_END);
  const rsrc_t rWS = __builtin_amdgcn_make_buffer_rsrc(ws, 0, WS_END, 0x00020000);
  if (tid < 32) *(LAS u32*)(L_CNT + tid * 4) = 0;
  const bool has_tile = !t1;
  const int rq = bi / 7, rt = 4 * xcd + rq, nt = bi - 7 * rq;
  const int rotg = (4 * nt + 8 * rq) % 28;
  const int head = 2 * xcd + (bi >> 4), qb = bi & 15;
  const u32 vw0 = rr * 896 + cw * 16, vw1 = vw0 + 6144;
  auto krot = [&](u32 k) LAM -> u32 { k += opaque(rotg); return k >= 28 ? k - 28 : k; };
  const u32 od = 448 * mg * 896 + nt * 128;

  asm volatile("s_waitcnt lgkmcnt(0)" : "+s"(epoch));
  u64 tag = (epoch + 0x9E3779B97F4A7C15ull) * 0xBF58476D1CE4E5B9ull;
  tag = (tag ^ (tag >> 29)) + llvm_dispatch_id() * 0x94D049BB133111EBull;
  tag = (tag ^ (tag >> 31)) * 0xD6E8FEB86659FD93ull & 0xBFFFBFFFBFFFBFFFull;
  asm volatile("" : "+s"(tag));
  float rs[4];
  f32x4 acc[5][4];
  auto geglu = [&](auto M) LAM -> u32x4 {
    constexpr int m = M.value;
    const float r2 = mul1(rs[m], rs[m]), K1 = (-2.f * 0.7978845608028654f * 1.4426950408889634f) * rs[m], K2 = K1 * 0.044715f * r2, IR2 = rcp(r2);
    u32x4 hp;
    f32x2 y[4], z[4];
    auto all = [&](auto f) LAM {
      sfor<0, 4>([&](auto i) LAM {
        const f32x4 t = acc[1 + (i.value & 2)][m], u = acc[2 + (i.value & 2)][m];
        constexpr int e = 2 * (i.value & 1);
        f(y[i.value], z[i.value], (f32x2){t[e], t[e + 1]}, (f32x2){u[e], u[e + 1]});
      });
      SBAR();
    };
    auto mul = [&](f32x2 a, f32x2 b) LAM -> f32x2 { if constexpr (m < PK0) return (f32x2){mul1(a[0], b[0]), mul1(a[1], b[1])}; else return a * b; };
    auto fma = [&](f32x2 a, float b, float c) LAM -> f32x2 {
      if constexpr (m < PK0) return (f32x2){fma1(a[0], b, c), fma1(a[1], b, c)}; else return __builtin_elementwise_fma(a, (f32x2){b, b}, (f32x2){c, c});
    };
    SBAR();
    all([&](f32x2& y, f32x2&, f32x2 t, f32x2) LAM { y = mul(t, t); });
    all([&](f32x2& y, f32x2& z, f32x2 t, f32x2 u) LAM { y = fma(y, K2, K1); z = mul(t, u); });
    all([&](f32x2& y, f32x2&, f32x2 t, f32x2) LAM { y = mul(y, t); });
    all([&](f32x2& y, f32x2&, f32x2, f32x2) LAM { y = (f32x2){ex2(y[0]), ex2(y[1])}; });
    all([&](f32x2& y, f32x2&, f32x2, f32x2) LAM { y = fma(y, IR2, IR2); });
    all([&](f32x2& y, f32x2&, f32x2, f32x2) LAM { y = (f32x2){rcp(y[0]), rcp(y[1])}; });
    all([&](f32x2& y, f32x2& z, f32x2, f32x2) LAM { y = mul(y, z); });
    sfor<0, 4>([&](auto j) LAM { hp[j.value] = cvtpk(y[j.value][0], y[j.value][1]); });
    PIN(hp);
    return hp;
  };
  auto frag_off = [&]() LAM -> u32 { const u32 l = lane_id(); return (l >> 4) * 512 + (l & 15) * 16; };
  u32x4 hp[4];

  auto phase1 = [&](auto T_) LAM {
    constexpr int T = T_.value;
    u32 xa[2][2], fa[4], qa, vl_, vcs, bt, vq1, vq2, vv[4];
    const bool isq = quad < 2;
    const u32 ts = isq ? 2048 : 4096;
    {
      const int lane = lane_id(), g4 = lane >> 4, tok = lane & 15, q4 = tok >> 2, p4 = lane & 3;
      xa[0][0] = (mg * 64 + tok) * 128 + ((g4 ^ (tok >> 1)) << 4);
      xa[0][1] = xa[0][0] ^ 64;
      xa[1][0] = xa[0][0] + 65536;
      xa[1][1] = xa[0][1] + 65536;
      sfor<0, 4>([&](auto t) LAM {
        if constexpr (T == 0) fa[t.value] = 6 * 16384 + g4 * 2048 + q4 * 128 + ((64 * g + 16 * (t.value & 2)) ^ ((q4 & 2) << 5) ^ ((g4 & 1) << 5)) + 8 * p4;
        else fa[t.value] = 6 * 16384 + g4 * 2048 + q4 * 256 + ((128 * g + 32 * t.value + 8 * p4) ^ (q4 * 64) ^ ((g4 & 1) << 5));
      });
      qa = L_QK + g4 * 512 + q4 * 64 + ((32 * g + 8 * p4) ^ ((g4 & 1) << 5));
      PIN(xa[0][0]); PIN(xa[0][1]); PIN(xa[1][0]); PIN(xa[1][1]); PIN(fa[0]); PIN(fa[2]);
      if constexpr (T == 1) { PIN(fa[1]); PIN(fa[3]); }
      vl_ = lane * 16;
      bt = tok * 4;
      vcs = mg * 1024 + g4 * 4096 + tok * 16;
      vcs = lane < (T == 0 && mg == 0 ? 32 : 16) ? vcs : OOB;
      PIN(vl_); PIN(bt); PIN(vcs);
      if constexpr (T == 0) {
        const u32 G = 4 * cl + 2 * g + (lane >> 5), p = (G * 37) >> 8, r = G - 7 * p;
        vq2 = (isq ? vl_ & 0x1f0u : (vl_ & 0x30u) | ((vl_ & 0xc0u) << 1) | ((vl_ & 0x100u) >> 2)) + p * (128 * ts);
        vq1 = vq2 + (r >> 2) * (64 * ts) + (r & 3) * 512;
        vq2 = r == 3 ? vq1 + 64 * ts : OOB;
        PIN(qa); PIN(vq1); PIN(vq2);
      }
      if constexpr (T == 1) sfor<0, 4>([&](auto t) LAM {
        const u32 c = 64 * g + 16 * t.value + tok, hd = (c * 147) >> 12;
        vv[t.value] = hd * (64 * 4096 - 28 * 16) + c * 16 + g4 * 512;
        PIN(vv[t.value]);
        acc[4][t.value] = (f32x4){};
        PIN(acc[4][t.value]);
      });
    }
    u32x4 one = {0x3f803f80u, 0x3f803f80u, 0x3f803f80u, 0x3f803f80u};
    PIN(one);
    u32x4 xf[4][4], wf[4][5], sg, cf[5], nm[4];
    f32x4 c0 = {}, c1 = {};
    PIN(c0); PIN(c1);
    const u32 scs = WS_CS + (cb * 2 + g) * 5120, sst = WS_ST + (rb * 256 + mg * 64) * 16;
    f32x4 rv[4];
    float rl, sc[4];
    u32 nl;

    SBAR();
    sfor<0, 7>([&](auto i) LAM { if constexpr (T == 0 || i.value != 2) piece(IC<1>{}, i); });
    {
      vmwait<14 - T - 2 * XP>();
      float a[8], t = 0.f, q = 0.f;
      sfor<0, 4>([&](auto i) LAM {
        widen(a[2 * i.value], a[2 * i.value + 1], xr[i.value]);
        t += a[2 * i.value] + a[2 * i.value + 1];
      });
      const float nmean = asf(__builtin_amdgcn_readlane(asu(wave_sum(t)), 63)) * (-1.f / 448.f);
      sfor<0, 8>([&](auto i) LAM { const float e = a[i.value] + nmean; q = __builtin_fmaf(e, e, q); });
      const float var = wave_sum(vl_ < 896 ? q : 0.f) * (1.f / 448.f);
      const u32 d = cvtpk(nmean, __builtin_amdgcn_sqrtf(bfround(bfround(var) + 1e-5f)));
      st128(rWS, (u32x4){d, (u32)tag, d, (u32)(tag >> 32)}, vl_ == 1008 ? 0 : OOB, WS_ST + row * 16);
      SBAR();
    }
    sync_stage<8 - T>();
    STAMP(1);
    auto rd = [&](auto J, auto R) LAM {
      constexpr int j = J.value, r = R.value, sl = (j / 2) % 3, s = j & 1;
      if constexpr (r >= 2 && r < 6) xf[j & 3][r - 2] = lds128(xa[sl >> 1][s] + (sl & 1) * 32768 + (r - 2) * 2048);
      else {
        constexpr int t = r < 2 ? 0 : (r - 4) / 2, h = r & 1;
        u32x2 v;
        if constexpr (t == 0) v = trd(qa + sl * 4096 + s * 2048 + h * 256);
        else if constexpr (T == 0) v = trd(fa[(t - 1) & 2] + sl * 16384 + s * 8192 + ((t - 1) & 1) * 1024 + h * 512);
        else v = trd(fa[t - 1] + sl * 16384 + s * 8192 + h * 1024);
        wf[j & 3][t][2 * h] = v[0];
        wf[j & 3][t][2 * h + 1] = v[1];
      }
    };
    sfor<2 * T, 14>([&](auto r) LAM { rd(IC<0>{}, r); });
    SBAR();
    SBAR();
    u32 P[4], nx, sq, last, tsrc, tdst;
    auto qk_put = [&](auto M) LAM {
      constexpr int m = M.value;
      f32x4& a = acc[0][m];
      scale4(a, sc[m]);
      P[2 * (m & 1)] = cvtpk(a[0], a[1]);
      P[2 * (m & 1) + 1] = cvtpk(a[2], a[3]);
      if constexpr (m & 1) {
        swap16(P[0], P[2]);
        swap16(P[1], P[3]);
        st128(rWS, (u32x4){P[0], P[1], vq2 == OOB ? P[2] : 0u, vq2 == OOB ? P[3] : 0u}, vq1, sq + (m >> 1) * ts);
        st128(rWS, (u32x4){P[2], P[3], 0u, 0u}, vq2, sq + (m >> 1) * ts);
      } else { PIN(P[0]); PIN(P[1]); }
    };
    auto norm = [&](auto I) LAM {
      constexpr int i = I.value;
      if constexpr (i < 4) {
        const f32x4 a = acc[0][i];
        const u32 v = asu(sumsq4(a));
        nx = i == 0 || v > nx ? v : nx;
        PIN(nx);
      } else row_max_to(L_CNT + 64 + 16 * g + (vl_ >> 6 & 12), nx);
    };
    auto tell = [&]() LAM {
      lgkm0_on(last);
      last = __builtin_amdgcn_readfirstlane(last);
      if (last == 7) {
        if constexpr (T == 0) {
          const u32 k2 = asu(row_sum(*(LAS float*)tsrc));
          st128(rWS, (u32x4){k2, (u32)tag, k2, (u32)(tag >> 32)}, tdst, WS_FQ + ((quad & 1) * 8 + cl) * (16 * BOX) + (isq ? 2 * rb * BOX + 24 * 16 : rb * 16));
        } else st128(rWS, (u32x4){0u, (u32)tag, 0u, (u32)(tag >> 32)}, vl_ * (BOX / 16), WS_FQ + quad * (64 * BOX) + (8 + rb) * 16);
      }
      STAMP(13);
    };
    auto v_put = [&](auto I) LAM {
      constexpr int t = 1 + I.value / 2, u = I.value & 1;
      u32x4 V;
      sfor<0, 4>([&](auto e) LAM {
        constexpr int m = 2 * u + e.value / 2, c = 2 * (e.value & 1);
        V[e.value] = cvtpk(mul1(acc[t][m][c], rv[m][c]), mul1(acc[t][m][c + 1], rv[m][c + 1]));
      });
      st128(rWS, V, vv[t - 1], WS_KV + (quad * 256 + rb * 8 + mg * 2 + u) * 4096 + 2048);
    };
    sfor<0, 14>([&](auto J) LAM {
      constexpr int j = J.value, G = j / 2;
      sfor<0, slots(T, j)>([&](auto I) LAM {
        constexpr int i = I.value, t = slot_tile(T, j, i), js = slot_step(T, j, i), m = i % 4;
        if constexpr (T == 1 && t == 4) {
          if constexpr (m == 0) mfma4_unless(acc[4], xf[j & 3], wf[j & 3][4], g);
        } else if constexpr (T == 1) mac16<j == 0>(acc[t][m], xf[j & 3][m], wf[j & 3][t]);
        else if constexpr (js < 0) mac16<false>(acc[0][m], cf[0], nm[m]);
        else mac16<j == 0>(acc[t][m], wf[js & 3][t], xf[js & 3][m]);
        SBAR();
        if constexpr ((j & 1) && i == 1 && j < 11 + 2 * T) sync_stage<(T ? (G == 5 ? 1 : 7 - (G & 1)) : G == 1 ? 7 : 8) + (TWM >> j & 1)>();
        if constexpr (T == 0 && j == 9 && i == 2) STAMPG(5);
        if constexpr (T == 0 && j == 10 && i == 8) { STAMPG(6); sync_stage<4, 2>(); __builtin_amdgcn_s_setprio(3); STAMPG(7); }
        if constexpr (T == 0 && j == 10 && i == 28) STAMPG(8);
        if constexpr (T == 0 && j == 11 && i == 0) STAMPG(9);
        if constexpr (T == 0 && j == 13 && i == 0) STAMPG(13);
        if constexpr ((j & 1) && i == 1 && j < 13) STAMPF(5 + G);
        if constexpr (i == 1 && (j == TJ2 || j == TJ2 + 2) && (TW & 2)) touch_b(sD, vl_ < 25 * 16 ? vl_ * 8 : OOB, ((bi * 8 + w) * 2 + (j - TJ2) / 2) * (25 * 128));
        if constexpr (i == 1 && j == TJ1 && (TW & 1)) touch_b(sD, vl_ < 7 * 16 ? vl_ * 8 : OOB, (bid * 8 + w) * (7 * 128));
        constexpr int r = slot_read(T, j, i);
        if constexpr (r >= 0) rd(IC<(r >= 0 ? r / 16 : 0)>{}, IC<(r >= 0 ? r % 16 : 0)>{});
        if constexpr (j < 4 && i == 3) colsum<T>(c0, c1, one, wf[j & 3], (0xf0700e0fu >> (8 * j + rb)) & 1 ? mg : 4);
        if constexpr (j == 3 && i == 8) {
          PIN(c0); PIN(c1);
          const float a0 = c0[0], a1 = c1[0], v = vl_ < 256 ? a0 : a1, hi = bfround(v);
          __builtin_amdgcn_raw_buffer_store_b64((u32x2){cvtpk(hi, v - hi), (u32)(tag >> (32 * (rb & 1)))}, rWS, vcs, scs + (rb >> 1) * 256 + (rb & 1) * 8, 16);
        }
        constexpr int pc = piece_at(T, j, i), H = (j + 5) / 2;
        if constexpr (pc >= 0 && H < 7) piece(IC<H>{}, IC<(pc >= 0 ? pc : 0)>{});
        if constexpr (T == 0 && (j == 9 || j == 12) && pc >= 0) {
          if constexpr (pc == 0) so = od + krot((j & 4) + 2 * g) * 14336;
          if constexpr (pc == 2) so = opaque(so) + 12288;
          dma1<(j == 9 ? 2 : 4) * 16384, pc * 1024>(sD, (pc & 1) ? vw1 : vw0, so, 4 * lw);
        }
        if constexpr (T == 0 && j == 8 && i == 4) {
          const u32 l = bt >> 2, p = vl_ >> 8;
          tsrc = l < 8 && p == (l + cl >= 7) ? L_CNT + 64 + bt : L_CNT + 124;
          PIN(tsrc);
        }
        if constexpr (T == 0 && j == 8 && i == 6) {
          const u32 p = vl_ >> 8, sd = isq ? 16 : 256;
          tdst = bt * (BOX / 4) + (p == 0 ? 0u : p == 1 ? 16 * BOX + sd : cl == 0 ? sd : 16 * BOX);
          tdst = bt < (isq ? 8 : 64) && p < (cl == 0 || cl == 6 ? 3 : 2) ? tdst : OOB;
          PIN(tdst);
        }
        constexpr int jn = 9 + 2 * T;
        if constexpr (j == jn - 2 && i == 0) aload_sc1(sg, vl_, sWS, sst);
        if constexpr (T == 0 && j == 8 && i == 0) aload_sc1(cf[0], vl_, sWS, scs + 4096);
        if constexpr (j == 9 && i == 1) dma_sc1(sWS, vl_, scs + mg * 1024, 7 * 16384 + lw);
        if constexpr (T == 1 && j == 9 && i == 8) st128(rWS, one, (vl_ >> 8) * (64 * 4096) + (vl_ & 0xc0) * 8 + (vl_ & 48) + 448, WS_KV + (quad * 256 + rb * 8 + w) * 4096 + 2048);
        if constexpr (j == 11 && i == 8 - 8 * T) { vcs = 7 * 16384 + g * 4096 + vl_; PIN(vcs); }
        if constexpr (T == 0 && j == 11 && i >= 9 && i < 13) cf[i - 8] = lds128(vcs + (i - 9) * 1024);
        if constexpr (T == 1 && j == 11 && i == 1) sfor<1, 5>([&](auto u) LAM { cf[u.value] = lds128(vcs + (u.value - 1) * 1024); });
        if constexpr (j == jn - 1 && i >= 10 && i < 14) { nm[i - 10] = (u32x4){}; PIN(nm[i - 10]); }
        if constexpr (j == jn && i == 3) {
          if constexpr (T == 1 || P9) __builtin_amdgcn_s_setprio(3);
          PIN(sg);
          await(&sg, vl_, sWS, sst, tag);
        }
        if constexpr (T == 0 && j == 10 && i == 10) { PIN(cf[0]); await(cf, vl_, sWS, scs + 4096, tag); }
        if constexpr (T == 0 ? j == 12 && i == 14 : j == 11 && i == 6) {
          sfor<1, 5>([&](auto u) LAM { PIN(cf[u.value]); });
          await<4>(cf + 1, vl_, sWS, scs, tag);
        }
        if constexpr (j == jn && i == 8) {
          rl = rcp(asf(sg[0] & 0xffff0000u));
          nl = __builtin_amdgcn_perm(sg[0], sg[0], 0x01000100u);
          if constexpr (T == 1) *(LAS float*)(L_RV + mg * 256 + (vl_ >> 2)) = rl;
        }
        if constexpr (j == jn && i >= 12 && i < 16) {
          nm[i - 12][0] = __builtin_amdgcn_ds_bpermute(bt + 64 * (i - 12), nl);
          PIN(bt);
          nm[i - 12][2] = __builtin_amdgcn_ds_bpermute(bt + 64 * (i - 12), nl);
        }
        if constexpr (T == 0 && j == 10 && i < 4) PIN(nm[i]);
        if constexpr (T == 0 && j == 9 && i >= 16) rs[i - 16] = asf(__builtin_amdgcn_ds_bpermute(bt + 64 * (i - 16), asu(rl)));
        if constexpr (T == 0 && j == 10 && i == 24) sq = opaque((isq ? WS_Q : WS_KV) + ((quad & 1) * 512 + rb * 8 + mg * 2) * ts);
        if constexpr (T == 0 && j == 10 && i == 26) sfor<0, 4>([&](auto e) LAM {
          sc[e.value] = rs[e.value] * (isq ? 0.18898223650461362f * 1.4426950408889634f : 1.f);
          PIN(sc[e.value]);
        });
        if constexpr (T == 0 && j == 10 && i >= 29 && i < 33) qk_put(IC<(i >= 29 && i < 33 ? i - 29 : 0)>{});
        if constexpr (T == 0 && j == 10 && i == 33) { __builtin_amdgcn_s_setprio(2); STAMPF(11); }
        if constexpr (T == 0 && j == 11 && i >= 2 && i < 7) norm(IC<(i >= 2 && i < 7 ? i - 2 : 0)>{});
        if constexpr (j == 13 - T) {
          constexpr int tc = (i + 8) % 16 / 4 + 1;
          SBAR();
          if constexpr (T == 1) mac16<false>(acc[tc][m], nm[m], cf[tc]);
          else mac16<false>(acc[tc][m], cf[tc], nm[m]);
          SBAR();
          if constexpr (T == 1 && i == 10) { vcs = L_RV + mg * 256 + ((vl_ >> 4) & 48); PIN(vcs); }
          if constexpr (T == 1 && i >= 10 && i < 14) rv[i - 10] = lds128f(vcs + 64 * (i - 10));
        }
        if constexpr (T == 0 && j > 10 && 16 * (j - 11) + i == DW) { STAMPG(10); vmwait<(DW < 22 ? 0 : DW < 29 ? (DW - 20) / 2 : 4)>(); STAMPG(11); last = cnt_take(0); }
        if constexpr (T == 0 && j > 10 && 16 * (j - 11) + i == DW + 2) { tell(); __builtin_amdgcn_s_setprio(1); }
        if constexpr (T == 0 && j == 13 && i == 0) cnt_add(24);
        if constexpr (T == 1 && j == 13 && i >= 5 && (i & 1)) v_put(IC<(i >= 5 ? (i - 5) / 2 : 0)>{});
        SBAR();
      });
    });
    asm volatile("s_nop 7");
    SBAR();
    STAMP(2);
    if constexpr (T == 1) {
      if (g == 0) { v_put(IC<6>{}); v_put(IC<7>{}); }
      vmwait<0>();
      last = cnt_take(0);
      tell();
    }
    STAMP(3);
    __builtin_amdgcn_s_setprio(0);
    if (PGA && g == 0) __builtin_amdgcn_s_setprio(PGA);
    SBAR();
    if constexpr (T == 0) sfor<0, PI>([&](auto m) LAM { hp[m.value] = geglu(m); PIN(hp[m.value]); });
  };
  if (!t1) phase1(IC<0>{}); else phase1(IC<1>{});
  LANES();
  auto wload = [&](auto B) LAM {
    const u32 so = od + krot(2 * B.value) * 14336;
    dma2<wd_loc(2 * B.value), 0>(sD, vw0, sD, vw1, so, mg * 4096);
    dma2<wd_loc(2 * B.value), 2048>(sD, vw0, sD, vw1, so + 12288, mg * 4096);
  };
  const u32x4 sO = mk_srd(pwo, D_ * D_ * 2);
  if (w < 4) {
    if (PRIO) __builtin_amdgcn_s_setprio(2);
    u32x4 fk;
    const u32 vfo = (lane < 26 ? lane : 0) * 16, sfo = WS_FQ + (head * 16 + qb) * BOX;
    u32 seen = 0;
    auto inbox = [&](u32 blocks) LAM {
      u32 need = blocks << (qb >> 1);
      need = ((need | need >> 8) & 255) * 0x10101 + 0x3000000;
#pragma nounroll
      for (u32 spins = 0; (seen & need) != need && spins < (1u << 20); ++spins) {
        if (spins) __builtin_amdgcn_s_sleep(NAPA);
        aload_sc1(fk, vfo, sWS, sfo);
        vm_on<0>(fk);
        seen = ~(u32)stale(fk, tag);
      }
    };
    auto hand = [&](auto M0) LAM {
      sfor<M0.value, 4>([&](auto m) LAM { hp[m.value] = geglu(m); PIN(hp[m.value]); });
      cnt_wait(24, 8);
      sfor<0, 4>([&](auto m) LAM { *(LAS u32x4*)(6 * 16384 + w * 4096 + m.value * 1024 + vl) = hp[m.value]; });
      cnt_add(12 + w);
    };
    if (has_tile) {
      aload_sc1(fk, vfo, sWS, sfo);
      if constexpr (GA == 4) hand(IC<PI>{});
      else sfor<PI, GA>([&](auto m) LAM { hp[m.value] = geglu(m); PIN(hp[m.value]); });
      STAMP(3);
      vm_on<0>(fk);
      seen = ~(u32)stale(fk, tag);
    }
    STAMP(4);
    inbox((1u << NR1) - 1);
    STAMP(5);
    const u32 kvb = WS_KV + (head * 64 + 2 * w) * 4096;
    u32 kvc = 0;
    auto kv_next = [&](int kt) LAM {
      if (kt & 1) kvc = opaque(kvc) ^ 4096;
      else kvc = kvb + (qb & 1) * 4096 + ((opaque(qb >> 1) + (kt >> 1)) & 7) * 32768;
    };
    u32x4 Q[4][2], KV[4][4];
    auto q_load = [&](auto t) LAM {
      const u32 so = WS_Q + (head * 64 + qb * 4 + ((w + 1 + t.value) & 3)) * 2048;
      Q[t.value][0] = ld128(rWS, vl, so);
      Q[t.value][1] = ld128(rWS, vl + 1024, so);
    };
    kv_next(0);
    KV[0][0] = ld128(rWS, vl, kvc);
    KV[0][1] = ld128(rWS, vl + 1024, kvc);
    q_load(IC<0>{});
    q_load(IC<1>{});
    KV[0][2] = ld128(rWS, vl + 2048, kvc);
    q_load(IC<2>{});
    KV[0][3] = ld128(rWS, vl + 3072, kvc);
    q_load(IC<3>{});
    SBAR();
    sfor<1, UPF>([&](auto b) LAM {
      kv_next(b.value);
      sfor<0, 4>([&](auto i) LAM { KV[b.value][i.value] = ld128(rWS, vl + i.value * 1024, kvc); });
    });
    SBAR();
    if (C3E) cnt_add(3);
    if constexpr (GA < 4) { if (has_tile) hand(IC<GA>{}); }
    inbox((1u << NR) - 1);
    if (!C3E && C3T < 0) cnt_add(3);
    STAMP(6);
    const int idx = 0xDDDD;
    f32x16 O[4] = {};
    const f32x16 Z = {};
    auto rgn = [&](int t) LAM -> u32 { return 2 * 16384u + t * 12288u; };
    auto put_part = [&](auto t, auto i) LAM {
      const f32x16& o = O[t.value];
      *(LAS f32x4*)(rgn((w + 1 + t.value) & 3) + t.value * 4096 + i.value * 1024 + vl) =
          (f32x4){o[4 * i.value], o[4 * i.value + 1], o[4 * i.value + 2], o[4 * i.value + 3]};
    };
    f32x16 Sx[4];
    u32 cb1 = 0;
    constexpr int CK = 8 * NR - 12, CP = CK - (NR > 3 ? 10 : 8);
    auto tile = [&](auto I) LAM {
      constexpr int kt = I.value >> 2, b = kt & 3, t = I.value & 3, tn = (t + 1) & 3, tp = (t + 3) & 3;
      constexpr int bn = t == 3 ? (b + 1) & 3 : b, bp = t == 0 ? (b + 3) & 3 : b;
      constexpr int E1 = PAT ? 3 : 4, E2 = 2 * E1, E3 = 3 * E1;
      auto exps = [&](auto lo, auto hi) LAM {
        SBAR();
        sfor<lo.value, hi.value>([&](auto e) LAM { Sx[t][e.value] = ex2(Sx[t][e.value]); });
        if constexpr (hi.value == E3) USE(Sx[t]); else PIN(Sx[t]);
        SBAR();
      };
      if constexpr (I.value < 63) Sx[tn] = mfma32(KV[bn][0], Q[tn][0], Z);
      exps(IC<0>{}, IC<E1>{});
      if constexpr (I.value < 63) Sx[tn] = mfma32(KV[bn][1], Q[tn][1], Sx[tn]);
      if constexpr (I.value < 63) PIN(Sx[tn]);
      exps(IC<E1>{}, IC<E2>{});
      if constexpr (I.value > 0) O[tp] = smf_hi(KV[bp][3], Sx[tp], O[tp], idx);
      PIN(O[tp]);
      exps(IC<E2>{}, IC<E3>{});
      O[t] = smf_lo(KV[b][2], Sx[t], O[t], idx);
      PIN(O[t]);
      exps(IC<E3>{}, IC<E3 + 2>{});
      if constexpr (I.value == CP) aload_sc1(fk, vfo, sWS, sfo);
      constexpr int NX = 4 * (3 - UPF);
      sfor<(I.value < NX ? 4 * UPF + 2 * I.value : I.value + 12), (I.value < NX ? 4 * UPF + 2 * I.value + 2 : I.value + 13)>([&](auto L) LAM {
        if constexpr (L.value < 64) {
          if constexpr ((L.value & 3) == 0) kv_next(L.value >> 2);
          KV[(L.value >> 2) & 3][L.value & 3] = ld128(rWS, vl + (L.value & 3) * 1024, kvc);
        }
      });
      if constexpr (I.value == C3T) cnt_add(3);
      if constexpr (I.value == ID) cnt_add(9);
      if constexpr (I.value == 58) cb1 = cnt_get(1);
      if constexpr (I.value == 60) { if (has_tile && __builtin_amdgcn_readfirstlane(cb1) < 4u) cnt_wait(1, 4); cfence(); }
      if constexpr (I.value >= 62) sfor<0, 4>([&](auto i) LAM { put_part(IC<I.value - 62>{}, i); });
      exps(IC<E3 + 2>{}, IC<16>{});
    };
    Sx[0] = mfma32(KV[0][1], Q[0][1], mfma32(KV[0][0], Q[0][0], Z));
    SBAR();
    sfor<0, CK>(tile);
    vm_on<CK - CP>(fk);
    await(&fk, vfo, sWS, sfo, tag);
    float bound = asf(__builtin_amdgcn_readlane(fk[0], 24)) + asf(__builtin_amdgcn_readlane(fk[0], 25));
    {
      u32 ka_ = fk[0], kb_ = fk[2];
      swap16(ka_, kb_);
      bound *= max8_nonneg(asf(ka_) + asf(kb_));
    }
    PIN(bound);
    sfor<CK, 64>(tile);
    O[3] = smf_hi(KV[3][3], Sx[3], O[3], idx);
    if (__builtin_amdgcn_ballot_w64(!(bound <= 4000.f)) != 0) {
      sfor<0, 4>([&](auto t) LAM { O[t.value] = Z; });
      auto kvo = [&](int kt) LAM -> u32 { return kvb + (kt >> 1) * 32768 + (kt & 1) * 4096; };
      float mx[4] = {-3.0e38f, -3.0e38f, -3.0e38f, -3.0e38f};
      for (int kt = 0; kt < 16; ++kt) {
        const u32x4 k0 = ld128(rWS, vl, kvo(kt)), k1 = ld128(rWS, vl + 1024, kvo(kt));
        sfor<0, 4>([&](auto t) LAM {
          const f32x16 s = mfma32(k1, Q[t.value][1], mfma32(k0, Q[t.value][0], Z));
          sfor<0, 16>([&](auto i) LAM { mx[t.value] = __builtin_fmaxf(mx[t.value], s[i.value]); });
        });
      }
      sfor<0, 4>([&](auto t) LAM {
        mx[t.value] = __builtin_fmaxf(mx[t.value], other_half(mx[t.value]));
        *(LAS float*)(L_VT + ((((w + 1 + t.value) & 3) * 32 + r32) * 4 + w) * 4) = mx[t.value];
      });
      cnt_add(8);
      cnt_wait(8, 4);
      sfor<0, 4>([&](auto t) LAM {
        const f32x4 m4 = lds128f(L_VT + (((w + 1 + t.value) & 3) * 32 + r32) * 16);
        mx[t.value] = __builtin_fmaxf(__builtin_fmaxf(m4[0], m4[1]), __builtin_fmaxf(m4[2], m4[3]));
      });
      for (int kt = 0; kt < 16; ++kt) {
        const u32x4 k0 = ld128(rWS, vl, kvo(kt)), k1 = ld128(rWS, vl + 1024, kvo(kt));
        const u32x4 v0 = ld128(rWS, vl + 2048, kvo(kt)), v1 = ld128(rWS, vl + 3072, kvo(kt));
        sfor<0, 4>([&](auto t) LAM {
          f32x16 s = mfma32(k1, Q[t.value][1], mfma32(k0, Q[t.value][0], Z));
          sfor<0, 16>([&](auto i) LAM { s[i.value] = ex2(s[i.value] - mx[t.value]); });
          O[t.value] = smf_hi(v1, s, smf_lo(v0, s, O[t.value], idx), idx);
        });
      }
      sfor<0, 2>([&](auto t) LAM { sfor<0, 4>([&](auto i) LAM { put_part(t, i); }); });
    }
    STAMP(7);
    sfor<0, 4>([&](auto i) LAM { put_part(IC<2>{}, i); });
    cnt_add(2);
    const u32 red = rgn(w);
    {
      LANES();
      float o[16];
      f32x4 pt[12];
      read_when<12>(pt, 2, 4, 2, 4, [&](int i) LAM -> u32 { return red + i * 1024 + vl; });
      sfor<0, 4>([&](auto i) LAM {
        const f32x4 a0 = pt[i.value], a1 = pt[4 + i.value], a2 = pt[8 + i.value];
        sfor<0, 4>([&](auto j) LAM {
          if constexpr (CPK) o[4 * i.value + j.value] = (a0[j.value] + a1[j.value]) + (a2[j.value] + O[3][4 * i.value + j.value]);
          else o[4 * i.value + j.value] = add1(add1(a0[j.value], a1[j.value]), add1(a2[j.value], O[3][4 * i.value + j.value]));
        });
      });
      const float lo_ = other_half(o[12]);
      const float il = rcp(hf ? o[12] : lo_);
      u32 P[8];
      sfor<0, 8>([&](auto i) LAM { P[i.value] = CPK ? cvtpk(o[2 * i.value] * il, o[2 * i.value + 1] * il) : cvtpk(mul1(o[2 * i.value], il), mul1(o[2 * i.value + 1], il)); });
      const int om = qb * 4 + w;
      st128(rWS, (u32x4){P[0], P[1], P[2], P[3]}, vl, WS_O + (om * 16 + head) * 2048);
      st128(rWS, (u32x4){P[4], P[5], hf ? 0u : P[6], hf ? 0u : P[7]}, vl + 1024, WS_O + (om * 16 + head) * 2048);
      sfor<0, 7>([&](auto e) LAM { dma_i<e.value * 1024, 8 * e.value * 896>(sO, vw0, (56 * w + 112 * (w / 2 + 1)) * 896 + nt * 128, red); });
      vmwait<7>();
      if (lane == 0) put_flag(ws, WS_FO + (om * 16 + head) * FSO, tag);
      STAMP(8);
      if (!has_tile) { vmwait<0>(); STAMP_END(); return; }
    }
    {
    LANES();
    const u32 trow = (4 * hf + q4) * 128, swz = (q4 & 2) << 5;
    const int m = w & 1, kh = w >> 1;
    const rsrc_t rX = __builtin_amdgcn_make_buffer_rsrc((void*)px, 0, S_ * D_ * 2, 0x00020000);
    const u32 xo = ((rt * 64 + m * 32 + r32) * 448 + nt * 64 + kh * 32 + hf * 8) * 2;
    u32x4 xr[2];
    xr[0] = __builtin_amdgcn_raw_buffer_load_b128(rX, xo, 0, 0);
    xr[1] = __builtin_amdgcn_raw_buffer_load_b128(rX, xo + 32, 0, 0);
    const u32 fo = WS_FO + ((2 * rt + m) * 16 + 8 * kh + (lane & 7)) * FSO;
    u64 f0 = 0;
    if constexpr (FS) aflag(f0, ws + fo);
    vm_on1<0>(f0);
    PIN(xr[0]); PIN(xr[1]);
    cnt_add(6);
    u32x4 of[16], wf[16][2];
    u32 wa[2][2], wp[2][2];
    sfor<0, 4>([&](auto i) LAM {
      constexpr int r = i.value >> 1, n = i.value & 1;
      wa[r][n] = (r ? rgn(2 * kh) : kh ? L_QK : 5 * 16384) + trow + ((64 * (n ^ kh) + 32 * blk + 8 * p4) ^ swz);
      wp[r][n] = wa[r][n] - hf * 512;
    });
    auto wo_rd = [&](auto K) LAM {
      constexpr int k = K.value, r = k >> 3, hh = (k >> 1) & 3, s = k & 1;
      constexpr u32 off = (28 * hh + 16 * s) * 128 + (r && hh > 1 ? 12288 - 7168 : 0);
      sfor<0, 2>([&](auto n) LAM { wf[k][n.value] = trd2(wa[r][n.value] + off, (s ? wp[r][n.value] : wa[r][n.value]) + off + 1024); });
    };
    u32 xs[8] = {xr[0][0], xr[0][1], xr[0][2], xr[0][3], xr[1][0], xr[1][1], xr[1][2], xr[1][3]};
    swap32(xs[0], xs[2]); swap32(xs[1], xs[3]); swap32(xs[4], xs[6]); swap32(xs[5], xs[7]);
    sfor<0, 8>([&](auto i) LAM { PIN(xs[i.value]); });
    u32 so_ = opaque(WS_O + ((2 * rt + m) * 16 + 8 * kh) * 2048), vl2 = vl;
    PIN(vl2);
    if constexpr (WPE) {
      cnt_wait(6, 8);
      sfor<0, WPF>(wo_rd);
      sfor<0, 4>([&](auto i) LAM { PIN(wa[i.value >> 1][i.value & 1]); PIN(wp[i.value >> 1][i.value & 1]); });
      sfor<0, 2 * WPF>([&](auto i) LAM { PIN(wf[i.value >> 1][i.value & 1]); });
    }
    if (!FS || __builtin_amdgcn_ballot_w64(f0 != tag) != 0) poll_flags<NAPO>(ws, fo, tag);
    STAMP(9);
    sfor<0, 16>([&](auto k) LAM { of[k.value] = ld128(rWS, vl2 + (k.value & 3) * 1024, so_ + (k.value >> 2) * 4096); });
    SBAR();
    if constexpr (!WPE) { cnt_wait(6, 8); sfor<0, WPF>(wo_rd); }
    SBAR();
    auto xch = [&](int v, int i) LAM -> u32 { return rgn(v) + 8192 + i * 1024; };
    f32x16 acc[2] = {};
    sfor<0, 16>([&](auto k) LAM {
      acc[1] = mfma32(wf[k.value][1], of[k.value], acc[1]);
      SBAR();
      if constexpr (k.value + WPF < 16) wo_rd(IC<k.value + WPF>{});
      if constexpr (k.value >= LAG) acc[0] = mfma32(wf[k.value - LAG][0], of[k.value - LAG], acc[0]);
      SBAR();
    });
    acc[0] = mfma32(wf[16 - LAG][0], of[16 - LAG], acc[0]);
    PIN(acc[0]);
    SBAR();
    sfor<0, 4>([&](auto i) LAM {
      *(LAS f32x4*)(xch(w, i.value) + vl) =
          (f32x4){acc[1][4 * i.value], acc[1][4 * i.value + 1], acc[1][4 * i.value + 2], acc[1][4 * i.value + 3]};
    });
    STAMP(10);
    cnt_add(6 + m);
    sfor<17 - LAG, 16>([&](auto k) LAM { acc[0] = mfma32(wf[k.value][0], of[k.value], acc[0]); });
    PIN(acc[0]);
    f32x4 gt[8];
    read_when<8>(gt, 5, 4, 6 + m, 10 - 8 * m, [&](int i) LAM -> u32 { return (i < 4 ? 16384 + w * 4096 + i * 1024 : xch(w ^ 2, i - 4)) + vl; });
    u32 R[8];
    sfor<0, 4>([&](auto i) LAM {
      const f32x4 a = gt[4 + i.value];
      float y[4];
      sfor<0, 4>([&](auto j) LAM {
        const u32 xd = xs[2 * i.value + (j.value >> 1)];
        const float xv = asf((j.value & 1) ? (xd & 0xffff0000u) : (xd << 16));
        y[j.value] = __builtin_fmaf(acc[0][4 * i.value + j.value] + a[j.value], gt[i.value][j.value], xv);
      });
      R[2 * i.value] = cvtpk(y[0], y[1]);
      R[2 * i.value + 1] = cvtpk(y[2], y[3]);
    });
    swap32(R[0], R[2]); swap32(R[1], R[3]); swap32(R[4], R[6]); swap32(R[5], R[7]);
    const rsrc_t rOut = __builtin_amdgcn_make_buffer_rsrc(pout, 0, S_ * D_ * 2, 0x00020000);
    st128p(rOut, (u32x4){R[0], R[1], R[2], R[3]}, xo);
    st128p(rOut, (u32x4){R[4], R[5], R[6], R[7]}, xo + 32);
    asm volatile(".Lfb_enda:");
    STAMP(11);
    STAMP_END();
    }
  } else {
    const u32 trow = (4 * hf + q4) * 128, swz = (q4 & 2) << 5;
    const int gw = w - 4;
    const u32 lg = gw * 4096;
    if (has_tile) {
      u32x4 ha[4];
      sfor<PI, 4>([&](auto m) LAM { hp[m.value] = geglu(m); PIN(hp[m.value]); });
      STAMP(4);
      cnt_wait(12 + gw, 1);
      sfor<0, 4>([&](auto m) LAM { ha[m.value] = lds128(6 * 16384 + lg + m.value * 1024 + vl); PIN(ha[m.value]); });
      if constexpr (WE == 2) { wload(IC<4>{}); wload(IC<8>{}); }
      const u32 fo = frag_off(), ho = WS_H + ((rb * 8 + gw * 2) * 112 + un * 4) * 1024;
      cnt_wait(3, HCNT);
      if constexpr (WE == 1) { wload(IC<4>{}); wload(IC<8>{}); }
      STAMP(9);
      sfor<0, 8>([&](auto i) LAM {
        constexpr int m = i.value >> 1;
        st128(rWS, (i.value & 1) ? hp[m] : ha[m], fo + (m & 1) * 256 + (i.value & 1) * 2048, ho + (m >> 1) * (112 * 1024));
        if constexpr (i.value < 7) __builtin_amdgcn_s_sleep(HP);
      });
      if constexpr (WE == 3) { wload(IC<4>{}); wload(IC<8>{}); vmwait<8>(); }
      else vmwait<0>();
      if (lane == 0) put_flag(ws, WS_FH + ((rb * 4 + gw) * 32 + un) * FSH, tag);
      STAMP(5);
      if constexpr (WE == 0) { wload(IC<4>{}); wload(IC<8>{}); }
      if constexpr (WL == 0) sfor<5, 8>(wload);
      touch(pwo + ((rq & 3) * 112 + gw * 28 + (lane < 28 ? lane : 0)) * 896 + nt * 128);
      STAMP(8);
    }
    if (bid == 255 && gw == 0) {
      for (u32 i = 0; i < 4; ++i) {
        u32x4 f = {0, 0, 0, (u32)~(tag >> 32)};
        await(&f, lane * 128, sWS, WS_ST + i * 8192, tag);
      }
      if (lane == 0) put_flag(ws, WS_EPOCH, epoch + 1);
    }
    if (!has_tile) { STAMP_END(); return; }
    const u32x4 sH = mk_srd(ws + WS_H, 64 * 112 * 1024);
    const u32 oh0 = ((2 * rt + (gw & 1)) * 112 + 28 * gw) * 1024, oh1 = ((2 * rt + 1 - (gw & 1)) * 112 + 28 * gw) * 1024;
    u32x4 hfr[DPH][2];
    auto hload = [&](auto K) LAM {
      aload_sc1(hfr[K.value % DPH][0], vl, sH, oh0 + krot(K.value) * 1024);
      aload_sc1(hfr[K.value % DPH][1], vl, sH, oh1 + krot(K.value) * 1024);
    };
    cnt_wait(3, 4);
    poll_flags<NAPB>(ws, WS_FH + (rt * 32 + 7 * gw + (lane < 7 ? lane : 0)) * FSH, tag);
    STAMP(6);
    sfor<0, DPH>([&](auto K) LAM { hload(K); });
    __builtin_amdgcn_s_setprio(PRB);
    u32 wa[2];
    sfor<0, 2>([&](auto n) LAM { wa[n.value] = lg + trow + ((64 * (n.value ^ (gw >> 1)) + 32 * blk + 8 * p4) ^ swz); });
    f32x16 acc[2][2] = {};
    u32x4 wf[2][2];
    auto ready = [&](auto K, auto N) LAM {
      constexpr int k = K.value;
      constexpr int o = wd_loc(k), d = (o & ~65535) - (k ? (int)(wd_loc(k - 1) & ~65535) : 0);
      vm_on2<N.value>(hfr[k % DPH][0], hfr[k % DPH][1]);
      sfor<0, 2>([&](auto n) LAM {
        if constexpr (d != 0) { wa[n.value] += d; PIN(wa[n.value]); }
        wf[k & 1][n.value] = trd2(wa[n.value] + (o & 65535), wa[n.value] + (o & 65535) + 2048);
      });
    };
    ready(IC<0>{}, IC<2 * (DPH - 1)>{});
    sfor<0, 28>([&](auto K) LAM {
      constexpr int k = K.value;
      if constexpr (k == KD) cnt_wait(9, 4);
      if constexpr (k + 1 < 28 && k != KD - 1) ready(IC<k + 1>{}, IC<gemm_wait(k + 1)>{});
      sfor<0, 2>([&](auto n) LAM {
        sfor<0, 2>([&](auto m) LAM { acc[m.value][n.value] = mfma32(wf[k & 1][n.value], hfr[k % DPH][m.value], acc[m.value][n.value]); PIN(acc[m.value][n.value]); });
      });
      SBAR();
      if constexpr (k == 7) cnt_add(1);
      if constexpr (k + DPH < 28) hload(IC<k + DPH>{});
      sfor<9, 14>([&](auto B) LAM { if constexpr (wd_at(B.value) == k) wload(B); });
      if constexpr (k == 7) sfor<0, 8>([&](auto e) LAM {
        const u32 p = e.value & 2 && gw == 3 ? 10 : 4 * gw;
        dma_i<(e.value & 3) * 1024 + (e.value < 4 ? 5 * 16384 : L_QK), ((e.value & 3) * 8 + (e.value < 4 ? 0 : 224)) * 896>(sO, vw0, p * (8 * 896) + nt * 128, p * 1024);
      });
      if constexpr (k == KD - 1) { ready(IC<KD>{}, IC<gemm_wait(KD)>{}); cnt_add(6); }
      SBAR();
    });
    STAMP(7);
    sfor<1, 4>([&](auto x) LAM {
      const f32x16& a = acc[x.value & 1][x.value >> 1];
      sfor<0, 4>([&](auto i) LAM {
        *(LAS f32x4*)((5 + x.value) * 16384 + lg + i.value * 1024 + vl) = (f32x4){a[4 * i.value], a[4 * i.value + 1], a[4 * i.value + 2], a[4 * i.value + 3]};
      });
    });
    cnt_add(4);
    f32x4 pt[12];
    read_when<12>(pt, 4, 4, 4, 4, [&](int i) LAM -> u32 { return (6 + i / 4) * 16384 + (gw ^ (1 + i / 4)) * 4096 + (i & 3) * 1024 + vl; });
    sfor<0, 4>([&](auto i) LAM {
      const u32 ra = i.value * 1024 + vl;
      const f32x4 a1 = pt[i.value], a2 = pt[4 + i.value], a3_ = pt[8 + i.value];
      f32x4 gv;
      sfor<0, 4>([&](auto j) LAM {
        if constexpr (BPK) {
          const float f = (acc[0][0][4 * i.value + j.value] + a1[j.value]) + (a2[j.value] + a3_[j.value]);
          gv[j.value] = f * rcp(1.f + ex2(f * -1.4426950408889634f));
        } else {
          const float f = add1(add1(acc[0][0][4 * i.value + j.value], a1[j.value]), add1(a2[j.value], a3_[j.value]));
          gv[j.value] = mul1(f, rcp(add1(1.f, ex2(mul1(f, -1.4426950408889634f)))));
        }
      });
      *(LAS f32x4*)(1 * 16384 + lg + ra) = gv;
    });
    asm volatile(".Lfb_endb:");
    cnt_add(5);
    STAMP(10);
    STAMP_END();
  }
}

extern "C" void kernel_launch(void* const* d_in, const int* in_sizes, int n_in, void* d_out, int out_size, void* d_ws,
                              size_t ws_size, hipStream_t stream) {
  fused_block<<<256, 512, 0, stream>>>(d_in[0], d_in[1], d_in[2], d_in[4], d_in[5], d_in[6], d_out, (char*)d_ws);
}
```
